```python
import torch

def _softmax(x, axis=-1):
    return torch.softmax(x, dim=axis)

def _gelu(x):
    return torch.nn.functional.gelu(x, approximate='tanh')

def _silu(x):
    return torch.nn.functional.silu(x)

def _sigmoid(x):
    return torch.sigmoid(x)

def _layernorm(x, axis=-1, eps=1e-05):
    m = x.mean(dim=axis, keepdim=True)
    v = x.var(dim=axis, keepdim=True, unbiased=False)
    return (x - m) / torch.sqrt(v + eps)

def _scatter_add(tgt, idx, src, axis=0):
    return tgt.clone().index_add_(axis, idx, src)

def _segment_sum(src, seg, n):
    out = torch.zeros((n,) + src.shape[1:], dtype=src.dtype)
    return out.index_add_(0, seg, src)

def _pad(x, pw):
    flat = tuple((v for pair in reversed(pw) for v in pair))
    return torch.nn.functional.pad(x, flat)
kernel_name = 'recurrent_scan__memory_aug_recurrent_mem_tr__dense_activation_silu__prompt'

def setup_inputs():

    _w = lambda a: (lambda t: t.to(torch.bfloat16) if t.is_floating_point() else t)(torch.as_tensor(a))

    def _A_setup():
        B, S, Dm = (1, 2048, 576)
        H, Dh = (32, 128)
        M, seg = (8, 32)
        seg = min(seg, S)
        S = S // seg * seg
        return {'x': _w((lambda *s: torch.randn(*s, dtype=torch.bfloat16) * 0.05)(B, S, Dm)), 'mem0': _w((lambda *s: torch.randn(*s, dtype=torch.bfloat16) * 0.8)(B, M, Dm)), 'Wq': _w((lambda *s: torch.randn(*s, dtype=torch.bfloat16) * 1.6)(Dm, H, Dh) * Dm ** (-0.5)), 'Wk': _w((lambda *s: torch.randn(*s, dtype=torch.bfloat16) * 1.6)(Dm, H, Dh) * Dm ** (-0.5)), 'Wv': _w((lambda *s: torch.randn(*s, dtype=torch.bfloat16) * 0.8)(Dm, H, Dh) * Dm ** (-0.5)), 'Wo': _w((lambda *s: torch.randn(*s, dtype=torch.bfloat16) * 0.8)(H, Dh, Dm) * (H * Dh) ** (-0.5)), 'seg_len': seg}
    _A_in = _A_setup()

    def _B_setup():
        B, S, D, F = (1, 2048, 576, 4608)
        return {'x': _w((lambda *s: torch.randn(*s, dtype=torch.bfloat16) * 0.05)(B, S, D)), 'w1': _w((lambda *s: torch.randn(*s, dtype=torch.bfloat16) * 0.8)(D, F) * D ** (-0.5)), 'w2': _w((lambda *s: torch.randn(*s, dtype=torch.bfloat16) * 0.2)(F, D) * F ** (-0.5))}
    _B_in = _B_setup()
    return {'A_x': _A_in['x'], 'A_mem0': _A_in['mem0'], 'A_Wq': _A_in['Wq'], 'A_Wk': _A_in['Wk'], 'A_Wv': _A_in['Wv'], 'A_Wo': _A_in['Wo'], 'A_seg_len': _A_in['seg_len'], 'B_x': _B_in['x'], 'B_w1': _B_in['w1'], 'B_w2': _B_in['w2']}

def reference(A_x, A_mem0, A_Wq, A_Wk, A_Wv, A_Wo, A_seg_len, B_x, B_w1, B_w2):

    def _A_ref(x, mem0, Wq, Wk, Wv, Wo, seg_len):
        B, S, Dm = x.shape
        H, Dh = (Wq.shape[1], Wq.shape[2])
        M = mem0.shape[1]
        scale = Dh ** (-0.5)
        n_seg = S // seg_len
        L = M + seg_len
        idx = torch.arange(L)
        mask = (idx[:, None] < M) | (idx[:, None] >= idx[None, :])
        mem = mem0
        outs = []
        for s in range(n_seg):
            xs = x[:, s * seg_len:(s + 1) * seg_len, :].clone()
            h = torch.concatenate([mem, xs], axis=1)
            q = torch.einsum('btd,dhe->bhte', h, Wq)
            k = torch.einsum('btd,dhe->bhte', h, Wk)
            v = torch.einsum('btd,dhe->bhte', h, Wv)
            scores = torch.einsum('bhqe,bhke->bhqk', q, k) * scale
            scores = torch.where(mask[None, None], scores, -1000000000.0)
            p = _softmax(scores, axis=-1)
            o = torch.einsum('bhqk,bhke->bhqe', p, v)
            o = torch.einsum('bhte,hed->btd', o, Wo)
            mem = o[:, :M, :].clone()
            outs.append(o[:, M:, :])
        return torch.concatenate(outs, axis=1)

    def _B_ref(x, w1, w2):
        h = torch.einsum('bsd,df->bsf', x, w1)
        h = _silu(h)
        out = torch.einsum('bsf,fd->bsd', h, w2)
        return out
    x = A_x
    xn = _layernorm(x, axis=-1)
    s = _A_ref(xn, A_mem0, A_Wq, A_Wk, A_Wv, A_Wo, A_seg_len)
    g = _silu(_B_ref(xn, B_w1, B_w2))
    return x + s * g
```

```cpp
#include <hip/hip_runtime.h>
#include <hip/hip_bf16.h>
#include <stdint.h>

typedef __attribute__((ext_vector_type(8))) __bf16 bf16x8;
typedef __attribute__((ext_vector_type(4))) float f32x4;
typedef unsigned short u16;

static constexpr int S = 2048, DM = 576, H = 32, DH = 128, M = 8, SEG = 32, NSEG = 64, FF = 4608, HD = 4096;

__device__ __forceinline__ float bf2f(u16 v) { return __uint_as_float(((unsigned)v) << 16); }
__device__ __forceinline__ u16 f2bf(float f) {
    unsigned u = __float_as_uint(f);
    if ((u & 0x7fffffffu) > 0x7f800000u) return (u16)((u >> 16) | 0x40);
    u += 0x7fffu + ((u >> 16) & 1);
    return (u16)(u >> 16);
}
__device__ __forceinline__ float rbf(float f) { return bf2f(f2bf(f)); }

__global__ void k_ln(const u16* __restrict__ x, u16* __restrict__ xn) {
    int row = blockIdx.x * 4 + (threadIdx.x >> 6), lane = threadIdx.x & 63;
    float v[9];
    float s = 0;
#pragma unroll
    for (int i = 0; i < 9; i++) { v[i] = bf2f(x[row * DM + i * 64 + lane]); s += v[i]; }
    for (int o = 32; o; o >>= 1) s += __shfl_xor(s, o);
    float m = s * (1.0f / DM);
    float q = 0;
#pragma unroll
    for (int i = 0; i < 9; i++) { float d = v[i] - m; q += d * d; }
    for (int o = 32; o; o >>= 1) q += __shfl_xor(q, o);
    float var = rbf(q * (1.0f / DM));
    float mb = rbf(m);
    float sd = rbf(sqrtf(rbf(var + 1e-5f)));
#pragma unroll
    for (int i = 0; i < 9; i++) xn[row * DM + i * 64 + lane] = f2bf(rbf(v[i] - mb) / sd);
}

template <int EPI>
__global__ __launch_bounds__(256) void k_gemm(const u16* __restrict__ A, int lda, const u16* __restrict__ B, int ldb,
                                              u16* __restrict__ C, int ldc, int K, const u16* __restrict__ X,
                                              const u16* __restrict__ G) {
    __shared__ u16 As[64][40];
    __shared__ u16 Bs[64][40];
    int tid = threadIdx.x, lane = tid & 63, w = tid >> 6;
    int m0 = blockIdx.y * 64, n0 = blockIdx.x * 64;
    f32x4 acc[2][2];
#pragma unroll
    for (int i = 0; i < 2; i++)
#pragma unroll
        for (int j = 0; j < 2; j++) acc[i][j] = (f32x4){0, 0, 0, 0};
    int wm = (w >> 1) * 32, wn = (w & 1) * 32;
    for (int k0 = 0; k0 < K; k0 += 32) {
        {
            int r = tid >> 2, c = (tid & 3) * 8;
            uint4 a = *(const uint4*)(A + (size_t)(m0 + r) * lda + k0 + c);
            *(uint4*)&As[r][c] = a;
            int kr = tid >> 3, nc = (tid & 7) * 8;
            uint4 b = *(const uint4*)(B + (size_t)(k0 + kr) * ldb + n0 + nc);
            const u16* bp = (const u16*)&b;
#pragma unroll
            for (int j = 0; j < 8; j++) Bs[nc + j][kr] = bp[j];
        }
        __syncthreads();
        bf16x8 af[2], bfr[2];
#pragma unroll
        for (int i = 0; i < 2; i++) af[i] = *(const bf16x8*)&As[wm + i * 16 + (lane & 15)][(lane >> 4) * 8];
#pragma unroll
        for (int j = 0; j < 2; j++) bfr[j] = *(const bf16x8*)&Bs[wn + j * 16 + (lane & 15)][(lane >> 4) * 8];
#pragma unroll
        for (int i = 0; i < 2; i++)
#pragma unroll
            for (int j = 0; j < 2; j++) acc[i][j] = __builtin_amdgcn_mfma_f32_16x16x32_bf16(af[i], bfr[j], acc[i][j], 0, 0, 0);
        __syncthreads();
    }
#pragma unroll
    for (int i = 0; i < 2; i++)
#pragma unroll
        for (int j = 0; j < 2; j++)
#pragma unroll
            for (int r = 0; r < 4; r++) {
                int row = m0 + wm + i * 16 + (lane >> 4) * 4 + r, col = n0 + wn + j * 16 + (lane & 15);
                float v = rbf(acc[i][j][r]);
                if (EPI == 1 || EPI == 2) v = v / (1.0f + __expf(-v));
                if (EPI == 3) v = bf2f(X[(size_t)row * DM + col]) + rbf(v * bf2f(G[(size_t)row * DM + col]));
                C[(size_t)row * ldc + col] = f2bf(v);
            }
}

__global__ __launch_bounds__(384) void k_head(const u16* __restrict__ mem, const u16* __restrict__ Wq, const u16* __restrict__ Wk,
                                              const u16* __restrict__ Wv, const u16* __restrict__ Wo, const u16* __restrict__ Kx,
                                              const u16* __restrict__ Vx, u16* __restrict__ Km, u16* __restrict__ Vm,
                                              float* __restrict__ part, int s) {
    __shared__ float ms[M][DM];
    __shared__ float qkv[3][M][DH];
    __shared__ float p[M][40];
    __shared__ float o[M][DH];
    int h = blockIdx.x, tid = threadIdx.x;
    for (int i = tid; i < M * DM; i += 384) ms[i / DM][i % DM] = bf2f(mem[i]);
    __syncthreads();
    {
        int which = tid >> 7, e = tid & 127;
        const u16* W = which == 0 ? Wq : which == 1 ? Wk : Wv;
        float a[M];
#pragma unroll
        for (int i = 0; i < M; i++) a[i] = 0;
        for (int d = 0; d < DM; d++) {
            float wv = bf2f(W[(size_t)d * HD + h * DH + e]);
#pragma unroll
            for (int i = 0; i < M; i++) a[i] += ms[i][d] * wv;
        }
#pragma unroll
        for (int i = 0; i < M; i++) {
            float r = rbf(a[i]);
            qkv[which][i][e] = r;
            if (which == 1) Km[((size_t)s * M + i) * HD + h * DH + e] = f2bf(r);
            if (which == 2) Vm[((size_t)s * M + i) * HD + h * DH + e] = f2bf(r);
        }
    }
    __syncthreads();
    if (s == NSEG - 1) return;
    if (tid < M * 40) {
        int i = tid / 40, j = tid % 40;
        float a = 0;
        for (int e = 0; e < DH; e++) {
            float kv = j < M ? qkv[1][j][e] : bf2f(Kx[(size_t)(s * SEG + j - M) * HD + h * DH + e]);
            a += qkv[0][i][e] * kv;
        }
        p[i][j] = rbf(rbf(a) * 0.08838834764831845f);
    }
    __syncthreads();
    if (tid < M) {
        float mx = -1e30f;
        for (int j = 0; j < 40; j++) mx = fmaxf(mx, p[tid][j]);
        float sum = 0;
        for (int j = 0; j < 40; j++) { float e = __expf(p[tid][j] - mx); p[tid][j] = e; sum += e; }
        for (int j = 0; j < 40; j++) p[tid][j] = rbf(p[tid][j] / sum);
    }
    __syncthreads();
    for (int idx = tid; idx < M * DH; idx += 384) {
        int i = idx >> 7, e = idx & 127;
        float a = 0;
        for (int j = 0; j < 40; j++) {
            float vv = j < M ? qkv[2][j][e] : bf2f(Vx[(size_t)(s * SEG + j - M) * HD + h * DH + e]);
            a += p[i][j] * vv;
        }
        o[i][e] = rbf(a);
    }
    __syncthreads();
    for (int d = tid; d < DM; d += 384) {
        float a[M];
#pragma unroll
        for (int i = 0; i < M; i++) a[i] = 0;
        for (int e = 0; e < DH; e++) {
            float wv = bf2f(Wo[((size_t)h * DH + e) * DM + d]);
#pragma unroll
            for (int i = 0; i < M; i++) a[i] += o[i][e] * wv;
        }
#pragma unroll
        for (int i = 0; i < M; i++) part[((size_t)h * M + i) * DM + d] = a[i];
    }
}

__global__ void k_red(const float* __restrict__ part, u16* __restrict__ mem) {
    int i = blockIdx.x * 256 + threadIdx.x;
    if (i >= M * DM) return;
    float a = 0;
    for (int h = 0; h < H; h++) a += part[(size_t)h * M * DM + i];
    mem[i] = f2bf(a);
}

__global__ __launch_bounds__(256) void k_attn(const u16* __restrict__ Q, const u16* __restrict__ Kx, const u16* __restrict__ Vx,
                                              const u16* __restrict__ Km, const u16* __restrict__ Vm, u16* __restrict__ O) {
    __shared__ float q[SEG][DH + 1], k[40][DH + 1], v[40][DH + 1], p[SEG][41];
    int s = blockIdx.x, h = blockIdx.y, tid = threadIdx.x;
    for (int i = tid; i < SEG * DH; i += 256) {
        int r = i >> 7, e = i & 127;
        size_t off = (size_t)(s * SEG + r) * HD + h * DH + e;
        q[r][e] = bf2f(Q[off]); k[M + r][e] = bf2f(Kx[off]); v[M + r][e] = bf2f(Vx[off]);
    }
    for (int i = tid; i < M * DH; i += 256) {
        int r = i >> 7, e = i & 127;
        size_t off = ((size_t)s * M + r) * HD + h * DH + e;
        k[r][e] = bf2f(Km[off]); v[r][e] = bf2f(Vm[off]);
    }
    __syncthreads();
    for (int idx = tid; idx < SEG * 40; idx += 256) {
        int i = idx / 40, j = idx % 40;
        float a = 0;
        for (int e = 0; e < DH; e++) a += q[i][e] * k[j][e];
        a = rbf(rbf(a) * 0.08838834764831845f);
        p[i][j] = (j <= i + M) ? a : -1e9f;
    }
    __syncthreads();
    if (tid < SEG) {
        float mx = -1e30f;
        for (int j = 0; j < 40; j++) mx = fmaxf(mx, p[tid][j]);
        float sum = 0;
        for (int j = 0; j < 40; j++) { float e = __expf(p[tid][j] - mx); p[tid][j] = e; sum += e; }
        for (int j = 0; j < 40; j++) p[tid][j] = rbf(p[tid][j] / sum);
    }
    __syncthreads();
    for (int idx = tid; idx < SEG * DH; idx += 256) {
        int i = idx >> 7, e = idx & 127;
        float a = 0;
        for (int j = 0; j < 40; j++) a += p[i][j] * v[j][e];
        O[(size_t)(s * SEG + i) * HD + h * DH + e] = f2bf(a);
    }
}

extern "C" void kernel_launch(void* const* d_in, const int* in_sizes, int n_in, void* d_out, int out_size, void* d_ws,
                              size_t ws_size, hipStream_t stream) {
    const u16* x = (const u16*)d_in[0];
    const u16* mem0 = (const u16*)d_in[1];
    const u16* Wq = (const u16*)d_in[2];
    const u16* Wk = (const u16*)d_in[3];
    const u16* Wv = (const u16*)d_in[4];
    const u16* Wo = (const u16*)d_in[5];
    const u16* w1 = (const u16*)d_in[8];
    const u16* w2 = (const u16*)d_in[9];
    u16* out = (u16*)d_out;
    char* ws = (char*)d_ws;
    size_t off = 0;
    auto alloc = [&](size_t b) { char* p = ws + off; off += (b + 255) & ~(size_t)255; return p; };
    u16* xn = (u16*)alloc((size_t)S * DM * 2);
    u16* Q = (u16*)alloc((size_t)S * HD * 2);
    u16* Kx = (u16*)alloc((size_t)S * HD * 2);
    u16* Vx = (u16*)alloc((size_t)S * HD * 2);
    u16* Hh = (u16*)alloc((size_t)S * FF * 2);
    u16* G = (u16*)alloc((size_t)S * DM * 2);
    u16* O = (u16*)alloc((size_t)S * HD * 2);
    u16* Km = (u16*)alloc((size_t)NSEG * M * HD * 2);
    u16* Vm = (u16*)alloc((size_t)NSEG * M * HD * 2);
    float* part = (float*)alloc((size_t)H * M * DM * 4);
    u16* mem = (u16*)alloc((size_t)NSEG * M * DM * 2);
    k_ln<<<S / 4, 256, 0, stream>>>(x, xn);
    k_gemm<0><<<dim3(HD / 64, S / 64), 256, 0, stream>>>(xn, DM, Wq, HD, Q, HD, DM, nullptr, nullptr);
    k_gemm<0><<<dim3(HD / 64, S / 64), 256, 0, stream>>>(xn, DM, Wk, HD, Kx, HD, DM, nullptr, nullptr);
    k_gemm<0><<<dim3(HD / 64, S / 64), 256, 0, stream>>>(xn, DM, Wv, HD, Vx, HD, DM, nullptr, nullptr);
    k_gemm<1><<<dim3(FF / 64, S / 64), 256, 0, stream>>>(xn, DM, w1, FF, Hh, FF, DM, nullptr, nullptr);
    k_gemm<2><<<dim3(DM / 64, S / 64), 256, 0, stream>>>(Hh, FF, w2, DM, G, DM, FF, nullptr, nullptr);
    for (int s = 0; s < NSEG; s++) {
        const u16* mi = s == 0 ? mem0 : mem + (size_t)s * M * DM;
        k_head<<<H, 384, 0, stream>>>(mi, Wq, Wk, Wv, Wo, Kx, Vx, Km, Vm, part, s);
        if (s < NSEG - 1) k_red<<<(M * DM + 255) / 256, 256, 0, stream>>>(part, mem + (size_t)(s + 1) * M * DM);
    }
    k_attn<<<dim3(NSEG, H), 256, 0, stream>>>(Q, Kx, Vx, Km, Vm, O);
    k_gemm<3><<<dim3(DM / 64, S / 64), 256, 0, stream>>>(O, HD, Wo, DM, out, DM, HD, x, G);
}
```

```cpp
#include <hip/hip_runtime.h>
#include <stdint.h>
#define WBUD 2000000
#define HEAD_ASM \
"s_setprio 3\n" \
"s_mov_b32 s94, 0x55555555\n" \
"s_mov_b32 s95, 0x55555555\n" \
"s_mov_b32 s88, 0x3f803f80\n" \
"s_mov_b32 s89, 0x3f80\n" \
"s_mov_b32 s72, -1\n" \
"s_mov_b32 s73, 1000000\n" \
"s_mov_b32 s41, 0\n" \
"s_mov_b32 s43, 0\n" \
"s_lshl_b32 s31, s49, 10\n" \
"s_mul_i32 s49, s49, 16\n" \
"s_sub_u32 s49, 100, s49\n" \
"s_mov_b32 s74, 0x3e0293ee\n" \
"s_mov_b32 s75, 0x3e0293ee\n" \
"s_mov_b32 s93, 2000000\n" \
"global_load_dword v56, v66, s[56:57] sc1\n" \
"s_getpc_b64 s[90:91]\n" \
".Lh_pc%=:\n" \
"s_add_u32 s90, s90, .Lh_load%=-.Lh_pc%=\n" \
"s_addc_u32 s91, s91, 0\n" \
"s_add_u32 m0, s68, 0\n" \
"s_nop 0\n" \
"global_load_lds_dwordx4 v40, s[58:59] offset:0\n" \
"global_load_lds_dwordx4 v41, s[58:59] offset:1024\n" \
"global_load_lds_dwordx4 v42, s[58:59] offset:2048\n" \
"global_load_lds_dwordx4 v43, s[58:59] offset:3072\n" \
"s_add_u32 s58, s58, 524288\n" \
"s_addc_u32 s59, s59, 0\n" \
"s_add_u32 m0, s68, 4096\n" \
"s_nop 0\n" \
"global_load_lds_dwordx4 v40, s[58:59] offset:0\n" \
"global_load_lds_dwordx4 v41, s[58:59] offset:1024\n" \
"global_load_lds_dwordx4 v42, s[58:59] offset:2048\n" \
"global_load_lds_dwordx4 v43, s[58:59] offset:3072\n" \
"s_add_u32 s58, s58, 524288\n" \
"s_addc_u32 s59, s59, 0\n" \
"s_add_u32 m0, s68, 8192\n" \
"s_nop 0\n" \
"global_load_lds_dwordx4 v40, s[58:59] offset:0\n" \
"global_load_lds_dwordx4 v41, s[58:59] offset:1024\n" \
"global_load_lds_dwordx4 v42, s[58:59] offset:2048\n" \
"global_load_lds_dwordx4 v43, s[58:59] offset:3072\n" \
"s_add_u32 s58, s58, 524288\n" \
"s_addc_u32 s59, s59, 0\n" \
"s_add_u32 m0, s68, 12288\n" \
"s_nop 0\n" \
"global_load_lds_dwordx4 v40, s[58:59] offset:0\n" \
"global_load_lds_dwordx4 v41, s[58:59] offset:1024\n" \
"global_load_lds_dwordx4 v42, s[58:59] offset:2048\n" \
"global_load_lds_dwordx4 v43, s[58:59] offset:3072\n" \
"s_add_u32 s58, s58, 524288\n" \
"s_addc_u32 s59, s59, 0\n" \
"s_add_u32 m0, s68, 16384\n" \
"s_nop 0\n" \
"global_load_lds_dwordx4 v40, s[58:59] offset:0\n" \
"global_load_lds_dwordx4 v41, s[58:59] offset:1024\n" \
"global_load_lds_dwordx4 v42, s[58:59] offset:2048\n" \
"global_load_lds_dwordx4 v43, s[58:59] offset:3072\n" \
"s_add_u32 s58, s58, 524288\n" \
"s_addc_u32 s59, s59, 0\n" \
"s_add_u32 m0, s68, 20480\n" \
"s_nop 0\n" \
"global_load_lds_dwordx4 v40, s[58:59] offset:0\n" \
"global_load_lds_dwordx4 v41, s[58:59] offset:1024\n" \
"global_load_lds_dwordx4 v42, s[58:59] offset:2048\n" \
"global_load_lds_dwordx4 v43, s[58:59] offset:3072\n" \
"s_add_u32 s58, s58, 524288\n" \
"s_addc_u32 s59, s59, 0\n" \
"s_add_u32 m0, s68, 24576\n" \
"s_nop 0\n" \
"global_load_lds_dwordx4 v40, s[58:59] offset:0\n" \
"global_load_lds_dwordx4 v41, s[58:59] offset:1024\n" \
"global_load_lds_dwordx4 v42, s[58:59] offset:2048\n" \
"global_load_lds_dwordx4 v43, s[58:59] offset:3072\n" \
"s_add_u32 s58, s58, 524288\n" \
"s_addc_u32 s59, s59, 0\n" \
"s_add_u32 m0, s68, 28672\n" \
"s_nop 0\n" \
"global_load_lds_dwordx4 v40, s[58:59] offset:0\n" \
"global_load_lds_dwordx4 v41, s[58:59] offset:1024\n" \
"global_load_lds_dwordx4 v42, s[58:59] offset:2048\n" \
"global_load_lds_dwordx4 v43, s[58:59] offset:3072\n" \
"s_add_u32 s58, s58, 524288\n" \
"s_addc_u32 s59, s59, 0\n" \
"s_add_u32 m0, s68, 32768\n" \
"s_nop 0\n" \
"global_load_lds_dwordx4 v40, s[58:59] offset:0\n" \
"global_load_lds_dwordx4 v41, s[58:59] offset:1024\n" \
"global_load_lds_dwordx4 v42, s[58:59] offset:2048\n" \
"global_load_lds_dwordx4 v43, s[58:59] offset:3072\n" \
"s_add_u32 s58, s58, 524288\n" \
"s_addc_u32 s59, s59, 0\n" \
"s_waitcnt vmcnt(32)\n" \
"s_barrier\n" \
"ds_read_b64_tr_b16 a[0:1], v48 offset:0\n" \
"ds_read_b64_tr_b16 a[2:3], v48 offset:512\n" \
"ds_read_b64_tr_b16 a[4:5], v49 offset:0\n" \
"ds_read_b64_tr_b16 a[6:7], v49 offset:512\n" \
"ds_read_b64_tr_b16 a[72:73], v50 offset:0\n" \
"ds_read_b64_tr_b16 a[74:75], v50 offset:512\n" \
"ds_read_b64_tr_b16 a[76:77], v51 offset:0\n" \
"ds_read_b64_tr_b16 a[78:79], v51 offset:512\n" \
"s_waitcnt lgkmcnt(0)\n" \
"s_waitcnt vmcnt(28)\n" \
"s_barrier\n" \
"s_add_u32 m0, s68, 0\n" \
"s_nop 0\n" \
"global_load_lds_dwordx4 v40, s[60:61] offset:0\n" \
"global_load_lds_dwordx4 v41, s[60:61] offset:1024\n" \
"global_load_lds_dwordx4 v42, s[60:61] offset:2048\n" \
"global_load_lds_dwordx4 v43, s[60:61] offset:3072\n" \
"s_add_u32 s60, s60, 524288\n" \
"s_addc_u32 s61, s61, 0\n" \
"ds_read_b64_tr_b16 a[8:9], v48 offset:4096\n" \
"ds_read_b64_tr_b16 a[10:11], v48 offset:4608\n" \
"ds_read_b64_tr_b16 a[12:13], v49 offset:4096\n" \
"ds_read_b64_tr_b16 a[14:15], v49 offset:4608\n" \
"ds_read_b64_tr_b16 a[80:81], v50 offset:4096\n" \
"ds_read_b64_tr_b16 a[82:83], v50 offset:4608\n" \
"ds_read_b64_tr_b16 a[84:85], v51 offset:4096\n" \
"ds_read_b64_tr_b16 a[86:87], v51 offset:4608\n" \
"s_waitcnt lgkmcnt(0)\n" \
"s_waitcnt vmcnt(28)\n" \
"s_barrier\n" \
"s_add_u32 m0, s68, 4096\n" \
"s_nop 0\n" \
"global_load_lds_dwordx4 v40, s[60:61] offset:0\n" \
"global_load_lds_dwordx4 v41, s[60:61] offset:1024\n" \
"global_load_lds_dwordx4 v42, s[60:61] offset:2048\n" \
"global_load_lds_dwordx4 v43, s[60:61] offset:3072\n" \
"s_add_u32 s60, s60, 524288\n" \
"s_addc_u32 s61, s61, 0\n" \
"ds_read_b64_tr_b16 a[16:17], v48 offset:8192\n" \
"ds_read_b64_tr_b16 a[18:19], v48 offset:8704\n" \
"ds_read_b64_tr_b16 a[20:21], v49 offset:8192\n" \
"ds_read_b64_tr_b16 a[22:23], v49 offset:8704\n" \
"ds_read_b64_tr_b16 a[88:89], v50 offset:8192\n" \
"ds_read_b64_tr_b16 a[90:91], v50 offset:8704\n" \
"ds_read_b64_tr_b16 a[92:93], v51 offset:8192\n" \
"ds_read_b64_tr_b16 a[94:95], v51 offset:8704\n" \
"s_waitcnt lgkmcnt(0)\n" \
"s_waitcnt vmcnt(28)\n" \
"s_barrier\n" \
"s_add_u32 m0, s68, 8192\n" \
"s_nop 0\n" \
"global_load_lds_dwordx4 v40, s[60:61] offset:0\n" \
"global_load_lds_dwordx4 v41, s[60:61] offset:1024\n" \
"global_load_lds_dwordx4 v42, s[60:61] offset:2048\n" \
"global_load_lds_dwordx4 v43, s[60:61] offset:3072\n" \
"s_add_u32 s60, s60, 524288\n" \
"s_addc_u32 s61, s61, 0\n" \
"ds_read_b64_tr_b16 a[24:25], v48 offset:12288\n" \
"ds_read_b64_tr_b16 a[26:27], v48 offset:12800\n" \
"ds_read_b64_tr_b16 a[28:29], v49 offset:12288\n" \
"ds_read_b64_tr_b16 a[30:31], v49 offset:12800\n" \
"ds_read_b64_tr_b16 a[96:97], v50 offset:12288\n" \
"ds_read_b64_tr_b16 a[98:99], v50 offset:12800\n" \
"ds_read_b64_tr_b16 a[100:101], v51 offset:12288\n" \
"ds_read_b64_tr_b16 a[102:103], v51 offset:12800\n" \
"s_waitcnt lgkmcnt(0)\n" \
"s_waitcnt vmcnt(28)\n" \
"s_barrier\n" \
"s_add_u32 m0, s68, 12288\n" \
"s_nop 0\n" \
"global_load_lds_dwordx4 v40, s[60:61] offset:0\n" \
"global_load_lds_dwordx4 v41, s[60:61] offset:1024\n" \
"global_load_lds_dwordx4 v42, s[60:61] offset:2048\n" \
"global_load_lds_dwordx4 v43, s[60:61] offset:3072\n" \
"s_add_u32 s60, s60, 524288\n" \
"s_addc_u32 s61, s61, 0\n" \
"ds_read_b64_tr_b16 a[32:33], v48 offset:16384\n" \
"ds_read_b64_tr_b16 a[34:35], v48 offset:16896\n" \
"ds_read_b64_tr_b16 a[36:37], v49 offset:16384\n" \
"ds_read_b64_tr_b16 a[38:39], v49 offset:16896\n" \
"ds_read_b64_tr_b16 a[104:105], v50 offset:16384\n" \
"ds_read_b64_tr_b16 a[106:107], v50 offset:16896\n" \
"ds_read_b64_tr_b16 a[108:109], v51 offset:16384\n" \
"ds_read_b64_tr_b16 a[110:111], v51 offset:16896\n" \
"s_waitcnt lgkmcnt(0)\n" \
"s_waitcnt vmcnt(28)\n" \
"s_barrier\n" \
"s_add_u32 m0, s68, 16384\n" \
"s_nop 0\n" \
"global_load_lds_dwordx4 v40, s[60:61] offset:0\n" \
"global_load_lds_dwordx4 v41, s[60:61] offset:1024\n" \
"global_load_lds_dwordx4 v42, s[60:61] offset:2048\n" \
"global_load_lds_dwordx4 v43, s[60:61] offset:3072\n" \
"s_add_u32 s60, s60, 524288\n" \
"s_addc_u32 s61, s61, 0\n" \
"ds_read_b64_tr_b16 a[40:41], v48 offset:20480\n" \
"ds_read_b64_tr_b16 a[42:43], v48 offset:20992\n" \
"ds_read_b64_tr_b16 a[44:45], v49 offset:20480\n" \
"ds_read_b64_tr_b16 a[46:47], v49 offset:20992\n" \
"ds_read_b64_tr_b16 a[112:113], v50 offset:20480\n" \
"ds_read_b64_tr_b16 a[114:115], v50 offset:20992\n" \
"ds_read_b64_tr_b16 a[116:117], v51 offset:20480\n" \
"ds_read_b64_tr_b16 a[118:119], v51 offset:20992\n" \
"s_waitcnt lgkmcnt(0)\n" \
"s_waitcnt vmcnt(28)\n" \
"s_barrier\n" \
"s_add_u32 m0, s68, 20480\n" \
"s_nop 0\n" \
"global_load_lds_dwordx4 v40, s[60:61] offset:0\n" \
"global_load_lds_dwordx4 v41, s[60:61] offset:1024\n" \
"global_load_lds_dwordx4 v42, s[60:61] offset:2048\n" \
"global_load_lds_dwordx4 v43, s[60:61] offset:3072\n" \
"s_add_u32 s60, s60, 524288\n" \
"s_addc_u32 s61, s61, 0\n" \
"ds_read_b64_tr_b16 a[48:49], v48 offset:24576\n" \
"ds_read_b64_tr_b16 a[50:51], v48 offset:25088\n" \
"ds_read_b64_tr_b16 a[52:53], v49 offset:24576\n" \
"ds_read_b64_tr_b16 a[54:55], v49 offset:25088\n" \
"ds_read_b64_tr_b16 a[120:121], v50 offset:24576\n" \
"ds_read_b64_tr_b16 a[122:123], v50 offset:25088\n" \
"ds_read_b64_tr_b16 a[124:125], v51 offset:24576\n" \
"ds_read_b64_tr_b16 a[126:127], v51 offset:25088\n" \
"s_waitcnt lgkmcnt(0)\n" \
"s_waitcnt vmcnt(28)\n" \
"s_barrier\n" \
"s_add_u32 m0, s68, 24576\n" \
"s_nop 0\n" \
"global_load_lds_dwordx4 v40, s[60:61] offset:0\n" \
"global_load_lds_dwordx4 v41, s[60:61] offset:1024\n" \
"global_load_lds_dwordx4 v42, s[60:61] offset:2048\n" \
"global_load_lds_dwordx4 v43, s[60:61] offset:3072\n" \
"s_add_u32 s60, s60, 524288\n" \
"s_addc_u32 s61, s61, 0\n" \
"ds_read_b64_tr_b16 a[56:57], v48 offset:28672\n" \
"ds_read_b64_tr_b16 a[58:59], v48 offset:29184\n" \
"ds_read_b64_tr_b16 a[60:61], v49 offset:28672\n" \
"ds_read_b64_tr_b16 a[62:63], v49 offset:29184\n" \
"ds_read_b64_tr_b16 a[128:129], v50 offset:28672\n" \
"ds_read_b64_tr_b16 a[130:131], v50 offset:29184\n" \
"ds_read_b64_tr_b16 a[132:133], v51 offset:28672\n" \
"ds_read_b64_tr_b16 a[134:135], v51 offset:29184\n" \
"s_waitcnt lgkmcnt(0)\n" \
"s_waitcnt vmcnt(28)\n" \
"s_barrier\n" \
"s_add_u32 m0, s68, 28672\n" \
"s_nop 0\n" \
"global_load_lds_dwordx4 v40, s[60:61] offset:0\n" \
"global_load_lds_dwordx4 v41, s[60:61] offset:1024\n" \
"global_load_lds_dwordx4 v42, s[60:61] offset:2048\n" \
"global_load_lds_dwordx4 v43, s[60:61] offset:3072\n" \
"s_add_u32 s60, s60, 524288\n" \
"s_addc_u32 s61, s61, 0\n" \
"ds_read_b64_tr_b16 a[64:65], v48 offset:32768\n" \
"ds_read_b64_tr_b16 a[66:67], v48 offset:33280\n" \
"ds_read_b64_tr_b16 a[68:69], v49 offset:32768\n" \
"ds_read_b64_tr_b16 a[70:71], v49 offset:33280\n" \
"ds_read_b64_tr_b16 a[136:137], v50 offset:32768\n" \
"ds_read_b64_tr_b16 a[138:139], v50 offset:33280\n" \
"ds_read_b64_tr_b16 a[140:141], v51 offset:32768\n" \
"ds_read_b64_tr_b16 a[142:143], v51 offset:33280\n" \
"s_waitcnt lgkmcnt(0)\n" \
"s_waitcnt vmcnt(28)\n" \
"s_barrier\n" \
"s_add_u32 m0, s68, 32768\n" \
"s_nop 0\n" \
"global_load_lds_dwordx4 v40, s[60:61] offset:0\n" \
"global_load_lds_dwordx4 v41, s[60:61] offset:1024\n" \
"global_load_lds_dwordx4 v42, s[60:61] offset:2048\n" \
"global_load_lds_dwordx4 v43, s[60:61] offset:3072\n" \
"s_add_u32 s60, s60, 524288\n" \
"s_addc_u32 s61, s61, 0\n" \
"ds_read_b64_tr_b16 a[144:145], v48 offset:0\n" \
"ds_read_b64_tr_b16 a[146:147], v48 offset:512\n" \
"ds_read_b64_tr_b16 a[148:149], v49 offset:0\n" \
"ds_read_b64_tr_b16 a[150:151], v49 offset:512\n" \
"ds_read_b64_tr_b16 a[216:217], v50 offset:0\n" \
"ds_read_b64_tr_b16 a[218:219], v50 offset:512\n" \
"ds_read_b64_tr_b16 a[220:221], v51 offset:0\n" \
"ds_read_b64_tr_b16 a[222:223], v51 offset:512\n" \
"s_waitcnt lgkmcnt(0)\n" \
"s_waitcnt vmcnt(28)\n" \
"s_barrier\n" \
"s_add_u32 m0, s68, 0\n" \
"s_nop 0\n" \
"global_load_lds_dwordx4 v40, s[62:63] offset:0\n" \
"global_load_lds_dwordx4 v41, s[62:63] offset:1024\n" \
"global_load_lds_dwordx4 v42, s[62:63] offset:2048\n" \
"global_load_lds_dwordx4 v43, s[62:63] offset:3072\n" \
"s_add_u32 s62, s62, 524288\n" \
"s_addc_u32 s63, s63, 0\n" \
"ds_read_b64_tr_b16 a[152:153], v48 offset:4096\n" \
"ds_read_b64_tr_b16 a[154:155], v48 offset:4608\n" \
"ds_read_b64_tr_b16 a[156:157], v49 offset:4096\n" \
"ds_read_b64_tr_b16 a[158:159], v49 offset:4608\n" \
"ds_read_b64_tr_b16 a[224:225], v50 offset:4096\n" \
"ds_read_b64_tr_b16 a[226:227], v50 offset:4608\n" \
"ds_read_b64_tr_b16 a[228:229], v51 offset:4096\n" \
"ds_read_b64_tr_b16 a[230:231], v51 offset:4608\n" \
"s_waitcnt lgkmcnt(0)\n" \
"s_waitcnt vmcnt(28)\n" \
"s_barrier\n" \
"s_add_u32 m0, s68, 4096\n" \
"s_nop 0\n" \
"global_load_lds_dwordx4 v40, s[62:63] offset:0\n" \
"global_load_lds_dwordx4 v41, s[62:63] offset:1024\n" \
"global_load_lds_dwordx4 v42, s[62:63] offset:2048\n" \
"global_load_lds_dwordx4 v43, s[62:63] offset:3072\n" \
"s_add_u32 s62, s62, 524288\n" \
"s_addc_u32 s63, s63, 0\n" \
"ds_read_b64_tr_b16 a[160:161], v48 offset:8192\n" \
"ds_read_b64_tr_b16 a[162:163], v48 offset:8704\n" \
"ds_read_b64_tr_b16 a[164:165], v49 offset:8192\n" \
"ds_read_b64_tr_b16 a[166:167], v49 offset:8704\n" \
"ds_read_b64_tr_b16 a[232:233], v50 offset:8192\n" \
"ds_read_b64_tr_b16 a[234:235], v50 offset:8704\n" \
"ds_read_b64_tr_b16 a[236:237], v51 offset:8192\n" \
"ds_read_b64_tr_b16 a[238:239], v51 offset:8704\n" \
"s_waitcnt lgkmcnt(0)\n" \
"s_waitcnt vmcnt(28)\n" \
"s_barrier\n" \
"s_add_u32 m0, s68, 8192\n" \
"s_nop 0\n" \
"global_load_lds_dwordx4 v40, s[62:63] offset:0\n" \
"global_load_lds_dwordx4 v41, s[62:63] offset:1024\n" \
"global_load_lds_dwordx4 v42, s[62:63] offset:2048\n" \
"global_load_lds_dwordx4 v43, s[62:63] offset:3072\n" \
"s_add_u32 s62, s62, 524288\n" \
"s_addc_u32 s63, s63, 0\n" \
"ds_read_b64_tr_b16 a[168:169], v48 offset:12288\n" \
"ds_read_b64_tr_b16 a[170:171], v48 offset:12800\n" \
"ds_read_b64_tr_b16 a[172:173], v49 offset:12288\n" \
"ds_read_b64_tr_b16 a[174:175], v49 offset:12800\n" \
"ds_read_b64_tr_b16 a[240:241], v50 offset:12288\n" \
"ds_read_b64_tr_b16 a[242:243], v50 offset:12800\n" \
"ds_read_b64_tr_b16 a[244:245], v51 offset:12288\n" \
"ds_read_b64_tr_b16 a[246:247], v51 offset:12800\n" \
"s_waitcnt lgkmcnt(0)\n" \
"s_waitcnt vmcnt(28)\n" \
"s_barrier\n" \
"s_add_u32 m0, s68, 12288\n" \
"s_nop 0\n" \
"global_load_lds_dwordx4 v40, s[62:63] offset:0\n" \
"global_load_lds_dwordx4 v41, s[62:63] offset:1024\n" \
"global_load_lds_dwordx4 v42, s[62:63] offset:2048\n" \
"global_load_lds_dwordx4 v43, s[62:63] offset:3072\n" \
"s_add_u32 s62, s62, 524288\n" \
"s_addc_u32 s63, s63, 0\n" \
"ds_read_b64_tr_b16 a[176:177], v48 offset:16384\n" \
"ds_read_b64_tr_b16 a[178:179], v48 offset:16896\n" \
"ds_read_b64_tr_b16 a[180:181], v49 offset:16384\n" \
"ds_read_b64_tr_b16 a[182:183], v49 offset:16896\n" \
"ds_read_b64_tr_b16 a[248:249], v50 offset:16384\n" \
"ds_read_b64_tr_b16 a[250:251], v50 offset:16896\n" \
"ds_read_b64_tr_b16 a[252:253], v51 offset:16384\n" \
"ds_read_b64_tr_b16 a[254:255], v51 offset:16896\n" \
"s_waitcnt lgkmcnt(0)\n" \
"s_waitcnt vmcnt(28)\n" \
"s_barrier\n" \
"s_add_u32 m0, s68, 16384\n" \
"s_nop 0\n" \
"global_load_lds_dwordx4 v40, s[62:63] offset:0\n" \
"global_load_lds_dwordx4 v41, s[62:63] offset:1024\n" \
"global_load_lds_dwordx4 v42, s[62:63] offset:2048\n" \
"global_load_lds_dwordx4 v43, s[62:63] offset:3072\n" \
"s_add_u32 s62, s62, 524288\n" \
"s_addc_u32 s63, s63, 0\n" \
"ds_read_b64_tr_b16 a[184:185], v48 offset:20480\n" \
"ds_read_b64_tr_b16 a[186:187], v48 offset:20992\n" \
"ds_read_b64_tr_b16 a[188:189], v49 offset:20480\n" \
"ds_read_b64_tr_b16 a[190:191], v49 offset:20992\n" \
"ds_read_b64_tr_b16 v[80:81], v50 offset:20480\n" \
"ds_read_b64_tr_b16 v[82:83], v50 offset:20992\n" \
"ds_read_b64_tr_b16 v[84:85], v51 offset:20480\n" \
"ds_read_b64_tr_b16 v[86:87], v51 offset:20992\n" \
"s_waitcnt lgkmcnt(0)\n" \
"s_waitcnt vmcnt(28)\n" \
"s_barrier\n" \
"s_add_u32 m0, s68, 20480\n" \
"s_nop 0\n" \
"global_load_lds_dwordx4 v40, s[62:63] offset:0\n" \
"global_load_lds_dwordx4 v41, s[62:63] offset:1024\n" \
"global_load_lds_dwordx4 v42, s[62:63] offset:2048\n" \
"global_load_lds_dwordx4 v43, s[62:63] offset:3072\n" \
"s_add_u32 s62, s62, 524288\n" \
"s_addc_u32 s63, s63, 0\n" \
"ds_read_b64_tr_b16 a[192:193], v48 offset:24576\n" \
"ds_read_b64_tr_b16 a[194:195], v48 offset:25088\n" \
"ds_read_b64_tr_b16 a[196:197], v49 offset:24576\n" \
"ds_read_b64_tr_b16 a[198:199], v49 offset:25088\n" \
"ds_read_b64_tr_b16 v[88:89], v50 offset:24576\n" \
"ds_read_b64_tr_b16 v[90:91], v50 offset:25088\n" \
"ds_read_b64_tr_b16 v[92:93], v51 offset:24576\n" \
"ds_read_b64_tr_b16 v[94:95], v51 offset:25088\n" \
"s_waitcnt lgkmcnt(0)\n" \
"s_waitcnt vmcnt(28)\n" \
"s_barrier\n" \
"s_add_u32 m0, s68, 24576\n" \
"s_nop 0\n" \
"global_load_lds_dwordx4 v40, s[62:63] offset:0\n" \
"global_load_lds_dwordx4 v41, s[62:63] offset:1024\n" \
"global_load_lds_dwordx4 v42, s[62:63] offset:2048\n" \
"global_load_lds_dwordx4 v43, s[62:63] offset:3072\n" \
"s_add_u32 s62, s62, 524288\n" \
"s_addc_u32 s63, s63, 0\n" \
"ds_read_b64_tr_b16 a[200:201], v48 offset:28672\n" \
"ds_read_b64_tr_b16 a[202:203], v48 offset:29184\n" \
"ds_read_b64_tr_b16 a[204:205], v49 offset:28672\n" \
"ds_read_b64_tr_b16 a[206:207], v49 offset:29184\n" \
"ds_read_b64_tr_b16 v[96:97], v50 offset:28672\n" \
"ds_read_b64_tr_b16 v[98:99], v50 offset:29184\n" \
"ds_read_b64_tr_b16 v[100:101], v51 offset:28672\n" \
"ds_read_b64_tr_b16 v[102:103], v51 offset:29184\n" \
"s_waitcnt lgkmcnt(0)\n" \
"s_waitcnt vmcnt(28)\n" \
"s_barrier\n" \
"s_add_u32 m0, s68, 28672\n" \
"s_nop 0\n" \
"global_load_lds_dwordx4 v40, s[62:63] offset:0\n" \
"global_load_lds_dwordx4 v41, s[62:63] offset:1024\n" \
"global_load_lds_dwordx4 v42, s[62:63] offset:2048\n" \
"global_load_lds_dwordx4 v43, s[62:63] offset:3072\n" \
"s_add_u32 s62, s62, 524288\n" \
"s_addc_u32 s63, s63, 0\n" \
"ds_read_b64_tr_b16 a[208:209], v48 offset:32768\n" \
"ds_read_b64_tr_b16 a[210:211], v48 offset:33280\n" \
"ds_read_b64_tr_b16 a[212:213], v49 offset:32768\n" \
"ds_read_b64_tr_b16 a[214:215], v49 offset:33280\n" \
"ds_read_b64_tr_b16 v[104:105], v50 offset:32768\n" \
"ds_read_b64_tr_b16 v[106:107], v50 offset:33280\n" \
"ds_read_b64_tr_b16 v[108:109], v51 offset:32768\n" \
"ds_read_b64_tr_b16 v[110:111], v51 offset:33280\n" \
"s_waitcnt lgkmcnt(0)\n" \
"s_waitcnt vmcnt(28)\n" \
"s_barrier\n" \
"s_add_u32 m0, s68, 32768\n" \
"s_nop 0\n" \
"global_load_lds_dwordx4 v40, s[62:63] offset:0\n" \
"global_load_lds_dwordx4 v41, s[62:63] offset:1024\n" \
"global_load_lds_dwordx4 v42, s[62:63] offset:2048\n" \
"global_load_lds_dwordx4 v43, s[62:63] offset:3072\n" \
"s_add_u32 s62, s62, 524288\n" \
"s_addc_u32 s63, s63, 0\n" \
"ds_read_b64_tr_b16 v[112:113], v48 offset:0\n" \
"ds_read_b64_tr_b16 v[114:115], v48 offset:512\n" \
"ds_read_b64_tr_b16 v[116:117], v49 offset:0\n" \
"ds_read_b64_tr_b16 v[118:119], v49 offset:512\n" \
"ds_read_b64_tr_b16 v[184:185], v50 offset:0\n" \
"ds_read_b64_tr_b16 v[186:187], v50 offset:512\n" \
"ds_read_b64_tr_b16 v[188:189], v51 offset:0\n" \
"ds_read_b64_tr_b16 v[190:191], v51 offset:512\n" \
"s_waitcnt lgkmcnt(0)\n" \
"s_waitcnt vmcnt(28)\n" \
"s_barrier\n" \
"s_add_u32 m0, s68, 0\n" \
"s_nop 0\n" \
"global_load_lds_dwordx4 v44, s[64:65] offset:0\n" \
"global_load_lds_dwordx4 v45, s[64:65] offset:1024\n" \
"global_load_lds_dwordx4 v46, s[64:65] offset:2048\n" \
"global_load_lds_dwordx4 v47, s[64:65] offset:3072\n" \
"s_add_u32 s64, s64, 128\n" \
"s_addc_u32 s65, s65, 0\n" \
"ds_read_b64_tr_b16 v[120:121], v48 offset:4096\n" \
"ds_read_b64_tr_b16 v[122:123], v48 offset:4608\n" \
"ds_read_b64_tr_b16 v[124:125], v49 offset:4096\n" \
"ds_read_b64_tr_b16 v[126:127], v49 offset:4608\n" \
"ds_read_b64_tr_b16 v[192:193], v50 offset:4096\n" \
"ds_read_b64_tr_b16 v[194:195], v50 offset:4608\n" \
"ds_read_b64_tr_b16 v[196:197], v51 offset:4096\n" \
"ds_read_b64_tr_b16 v[198:199], v51 offset:4608\n" \
"s_waitcnt lgkmcnt(0)\n" \
"s_waitcnt vmcnt(28)\n" \
"s_barrier\n" \
"s_add_u32 m0, s68, 4096\n" \
"s_nop 0\n" \
"global_load_lds_dwordx4 v44, s[64:65] offset:0\n" \
"global_load_lds_dwordx4 v45, s[64:65] offset:1024\n" \
"global_load_lds_dwordx4 v46, s[64:65] offset:2048\n" \
"global_load_lds_dwordx4 v47, s[64:65] offset:3072\n" \
"s_add_u32 s64, s64, 128\n" \
"s_addc_u32 s65, s65, 0\n" \
"ds_read_b64_tr_b16 v[128:129], v48 offset:8192\n" \
"ds_read_b64_tr_b16 v[130:131], v48 offset:8704\n" \
"ds_read_b64_tr_b16 v[132:133], v49 offset:8192\n" \
"ds_read_b64_tr_b16 v[134:135], v49 offset:8704\n" \
"ds_read_b64_tr_b16 v[200:201], v50 offset:8192\n" \
"ds_read_b64_tr_b16 v[202:203], v50 offset:8704\n" \
"ds_read_b64_tr_b16 v[204:205], v51 offset:8192\n" \
"ds_read_b64_tr_b16 v[206:207], v51 offset:8704\n" \
"s_waitcnt lgkmcnt(0)\n" \
"s_waitcnt vmcnt(28)\n" \
"s_barrier\n" \
"s_add_u32 m0, s68, 8192\n" \
"s_nop 0\n" \
"global_load_lds_dwordx4 v44, s[64:65] offset:0\n" \
"global_load_lds_dwordx4 v45, s[64:65] offset:1024\n" \
"global_load_lds_dwordx4 v46, s[64:65] offset:2048\n" \
"global_load_lds_dwordx4 v47, s[64:65] offset:3072\n" \
"s_add_u32 s64, s64, 128\n" \
"s_addc_u32 s65, s65, 0\n" \
"ds_read_b64_tr_b16 v[136:137], v48 offset:12288\n" \
"ds_read_b64_tr_b16 v[138:139], v48 offset:12800\n" \
"ds_read_b64_tr_b16 v[140:141], v49 offset:12288\n" \
"ds_read_b64_tr_b16 v[142:143], v49 offset:12800\n" \
"ds_read_b64_tr_b16 v[208:209], v50 offset:12288\n" \
"ds_read_b64_tr_b16 v[210:211], v50 offset:12800\n" \
"ds_read_b64_tr_b16 v[212:213], v51 offset:12288\n" \
"ds_read_b64_tr_b16 v[214:215], v51 offset:12800\n" \
"s_waitcnt lgkmcnt(0)\n" \
"s_waitcnt vmcnt(28)\n" \
"s_barrier\n" \
"s_add_u32 m0, s68, 12288\n" \
"s_nop 0\n" \
"global_load_lds_dwordx4 v44, s[64:65] offset:0\n" \
"global_load_lds_dwordx4 v45, s[64:65] offset:1024\n" \
"global_load_lds_dwordx4 v46, s[64:65] offset:2048\n" \
"global_load_lds_dwordx4 v47, s[64:65] offset:3072\n" \
"s_add_u32 s64, s64, 128\n" \
"s_addc_u32 s65, s65, 0\n" \
"ds_read_b64_tr_b16 v[144:145], v48 offset:16384\n" \
"ds_read_b64_tr_b16 v[146:147], v48 offset:16896\n" \
"ds_read_b64_tr_b16 v[148:149], v49 offset:16384\n" \
"ds_read_b64_tr_b16 v[150:151], v49 offset:16896\n" \
"ds_read_b64_tr_b16 v[216:217], v50 offset:16384\n" \
"ds_read_b64_tr_b16 v[218:219], v50 offset:16896\n" \
"ds_read_b64_tr_b16 v[220:221], v51 offset:16384\n" \
"ds_read_b64_tr_b16 v[222:223], v51 offset:16896\n" \
"s_waitcnt lgkmcnt(0)\n" \
"s_waitcnt vmcnt(28)\n" \
"s_barrier\n" \
"s_add_u32 m0, s68, 16384\n" \
"s_nop 0\n" \
"global_load_lds_dwordx4 v44, s[64:65] offset:0\n" \
"global_load_lds_dwordx4 v45, s[64:65] offset:1024\n" \
"global_load_lds_dwordx4 v46, s[64:65] offset:2048\n" \
"global_load_lds_dwordx4 v47, s[64:65] offset:3072\n" \
"s_add_u32 s64, s64, 128\n" \
"s_addc_u32 s65, s65, 0\n" \
"ds_read_b64_tr_b16 v[152:153], v48 offset:20480\n" \
"ds_read_b64_tr_b16 v[154:155], v48 offset:20992\n" \
"ds_read_b64_tr_b16 v[156:157], v49 offset:20480\n" \
"ds_read_b64_tr_b16 v[158:159], v49 offset:20992\n" \
"ds_read_b64_tr_b16 v[224:225], v50 offset:20480\n" \
"ds_read_b64_tr_b16 v[226:227], v50 offset:20992\n" \
"ds_read_b64_tr_b16 v[228:229], v51 offset:20480\n" \
"ds_read_b64_tr_b16 v[230:231], v51 offset:20992\n" \
"s_waitcnt lgkmcnt(0)\n" \
"s_waitcnt vmcnt(28)\n" \
"s_barrier\n" \
"s_add_u32 m0, s68, 20480\n" \
"s_nop 0\n" \
"global_load_lds_dwordx4 v44, s[64:65] offset:0\n" \
"global_load_lds_dwordx4 v45, s[64:65] offset:1024\n" \
"global_load_lds_dwordx4 v46, s[64:65] offset:2048\n" \
"global_load_lds_dwordx4 v47, s[64:65] offset:3072\n" \
"s_add_u32 s64, s64, 128\n" \
"s_addc_u32 s65, s65, 0\n" \
"ds_read_b64_tr_b16 v[160:161], v48 offset:24576\n" \
"ds_read_b64_tr_b16 v[162:163], v48 offset:25088\n" \
"ds_read_b64_tr_b16 v[164:165], v49 offset:24576\n" \
"ds_read_b64_tr_b16 v[166:167], v49 offset:25088\n" \
"ds_read_b64_tr_b16 v[232:233], v50 offset:24576\n" \
"ds_read_b64_tr_b16 v[234:235], v50 offset:25088\n" \
"ds_read_b64_tr_b16 v[236:237], v51 offset:24576\n" \
"ds_read_b64_tr_b16 v[238:239], v51 offset:25088\n" \
"s_waitcnt lgkmcnt(0)\n" \
"s_waitcnt vmcnt(28)\n" \
"s_barrier\n" \
"s_add_u32 m0, s68, 24576\n" \
"s_nop 0\n" \
"global_load_lds_dwordx4 v44, s[64:65] offset:0\n" \
"global_load_lds_dwordx4 v45, s[64:65] offset:1024\n" \
"global_load_lds_dwordx4 v46, s[64:65] offset:2048\n" \
"global_load_lds_dwordx4 v47, s[64:65] offset:3072\n" \
"s_add_u32 s64, s64, 128\n" \
"s_addc_u32 s65, s65, 0\n" \
"ds_read_b64_tr_b16 v[168:169], v48 offset:28672\n" \
"ds_read_b64_tr_b16 v[170:171], v48 offset:29184\n" \
"ds_read_b64_tr_b16 v[172:173], v49 offset:28672\n" \
"ds_read_b64_tr_b16 v[174:175], v49 offset:29184\n" \
"ds_read_b64_tr_b16 v[240:241], v50 offset:28672\n" \
"ds_read_b64_tr_b16 v[242:243], v50 offset:29184\n" \
"ds_read_b64_tr_b16 v[244:245], v51 offset:28672\n" \
"ds_read_b64_tr_b16 v[246:247], v51 offset:29184\n" \
"s_waitcnt lgkmcnt(0)\n" \
"s_waitcnt vmcnt(28)\n" \
"s_barrier\n" \
"global_load_dword v57, v66, s[56:57] sc1\n" \
"s_add_u32 m0, s68, 28672\n" \
"s_nop 0\n" \
"global_load_lds_dwordx4 v44, s[64:65] offset:0\n" \
"global_load_lds_dwordx4 v45, s[64:65] offset:1024\n" \
"global_load_lds_dwordx4 v46, s[64:65] offset:2048\n" \
"global_load_lds_dwordx4 v47, s[64:65] offset:3072\n" \
"s_add_u32 s64, s64, 128\n" \
"s_addc_u32 s65, s65, 0\n" \
"ds_read_b64_tr_b16 v[176:177], v48 offset:32768\n" \
"ds_read_b64_tr_b16 v[178:179], v48 offset:33280\n" \
"ds_read_b64_tr_b16 v[180:181], v49 offset:32768\n" \
"ds_read_b64_tr_b16 v[182:183], v49 offset:33280\n" \
"ds_read_b64_tr_b16 v[248:249], v50 offset:32768\n" \
"ds_read_b64_tr_b16 v[250:251], v50 offset:33280\n" \
"ds_read_b64_tr_b16 v[252:253], v51 offset:32768\n" \
"ds_read_b64_tr_b16 v[254:255], v51 offset:33280\n" \
"s_waitcnt lgkmcnt(0)\n" \
"s_waitcnt vmcnt(29)\n" \
"s_barrier\n" \
"s_add_u32 m0, s68, 32768\n" \
"s_nop 0\n" \
"global_load_lds_dwordx4 v44, s[64:65] offset:0\n" \
"global_load_lds_dwordx4 v45, s[64:65] offset:1024\n" \
"global_load_lds_dwordx4 v46, s[64:65] offset:2048\n" \
"global_load_lds_dwordx4 v47, s[64:65] offset:3072\n" \
"s_add_u32 s64, s64, 128\n" \
"s_addc_u32 s65, s65, 0\n" \
"ds_read_b64_tr_b16 v[0:1], v52 offset:0\n" \
"ds_read_b64_tr_b16 v[2:3], v52 offset:256\n" \
"ds_read_b64_tr_b16 v[4:5], v53 offset:0\n" \
"ds_read_b64_tr_b16 v[6:7], v53 offset:256\n" \
"ds_read_b64_tr_b16 v[8:9], v54 offset:0\n" \
"ds_read_b64_tr_b16 v[10:11], v54 offset:256\n" \
"ds_read_b64_tr_b16 v[12:13], v55 offset:0\n" \
"ds_read_b64_tr_b16 v[14:15], v55 offset:256\n" \
"s_waitcnt lgkmcnt(0)\n" \
"s_waitcnt vmcnt(29)\n" \
"s_barrier\n" \
"ds_write_b128 v69, v[0:3] offset:0\n" \
"ds_write_b128 v69, v[4:7] offset:1024\n" \
"ds_write_b128 v69, v[8:11] offset:2048\n" \
"ds_write_b128 v69, v[12:15] offset:3072\n" \
"ds_read_b64_tr_b16 v[16:17], v52 offset:4096\n" \
"ds_read_b64_tr_b16 v[18:19], v52 offset:4352\n" \
"ds_read_b64_tr_b16 v[20:21], v53 offset:4096\n" \
"ds_read_b64_tr_b16 v[22:23], v53 offset:4352\n" \
"ds_read_b64_tr_b16 v[24:25], v54 offset:4096\n" \
"ds_read_b64_tr_b16 v[26:27], v54 offset:4352\n" \
"ds_read_b64_tr_b16 v[28:29], v55 offset:4096\n" \
"ds_read_b64_tr_b16 v[30:31], v55 offset:4352\n" \
"s_waitcnt lgkmcnt(0)\n" \
"s_waitcnt vmcnt(25)\n" \
"s_barrier\n" \
"ds_write_b128 v69, v[16:19] offset:4096\n" \
"ds_write_b128 v69, v[20:23] offset:5120\n" \
"ds_write_b128 v69, v[24:27] offset:6144\n" \
"ds_write_b128 v69, v[28:31] offset:7168\n" \
"ds_read_b64_tr_b16 v[0:1], v52 offset:8192\n" \
"ds_read_b64_tr_b16 v[2:3], v52 offset:8448\n" \
"ds_read_b64_tr_b16 v[4:5], v53 offset:8192\n" \
"ds_read_b64_tr_b16 v[6:7], v53 offset:8448\n" \
"ds_read_b64_tr_b16 v[8:9], v54 offset:8192\n" \
"ds_read_b64_tr_b16 v[10:11], v54 offset:8448\n" \
"ds_read_b64_tr_b16 v[12:13], v55 offset:8192\n" \
"ds_read_b64_tr_b16 v[14:15], v55 offset:8448\n" \
"s_waitcnt lgkmcnt(0)\n" \
"s_waitcnt vmcnt(21)\n" \
"s_barrier\n" \
"ds_write_b128 v69, v[0:3] offset:8192\n" \
"ds_write_b128 v69, v[4:7] offset:9216\n" \
"ds_write_b128 v69, v[8:11] offset:10240\n" \
"ds_write_b128 v69, v[12:15] offset:11264\n" \
"ds_read_b64_tr_b16 v[16:17], v52 offset:12288\n" \
"ds_read_b64_tr_b16 v[18:19], v52 offset:12544\n" \
"ds_read_b64_tr_b16 v[20:21], v53 offset:12288\n" \
"ds_read_b64_tr_b16 v[22:23], v53 offset:12544\n" \
"ds_read_b64_tr_b16 v[24:25], v54 offset:12288\n" \
"ds_read_b64_tr_b16 v[26:27], v54 offset:12544\n" \
"ds_read_b64_tr_b16 v[28:29], v55 offset:12288\n" \
"ds_read_b64_tr_b16 v[30:31], v55 offset:12544\n" \
"s_waitcnt lgkmcnt(0)\n" \
"s_waitcnt vmcnt(17)\n" \
"s_barrier\n" \
"ds_write_b128 v69, v[16:19] offset:12288\n" \
"ds_write_b128 v69, v[20:23] offset:13312\n" \
"ds_write_b128 v69, v[24:27] offset:14336\n" \
"ds_write_b128 v69, v[28:31] offset:15360\n" \
"ds_read_b64_tr_b16 v[0:1], v52 offset:16384\n" \
"ds_read_b64_tr_b16 v[2:3], v52 offset:16640\n" \
"ds_read_b64_tr_b16 v[4:5], v53 offset:16384\n" \
"ds_read_b64_tr_b16 v[6:7], v53 offset:16640\n" \
"ds_read_b64_tr_b16 v[8:9], v54 offset:16384\n" \
"ds_read_b64_tr_b16 v[10:11], v54 offset:16640\n" \
"ds_read_b64_tr_b16 v[12:13], v55 offset:16384\n" \
"ds_read_b64_tr_b16 v[14:15], v55 offset:16640\n" \
"s_waitcnt lgkmcnt(0)\n" \
"s_waitcnt vmcnt(13)\n" \
"s_barrier\n" \
"ds_write_b128 v69, v[0:3] offset:16384\n" \
"ds_write_b128 v69, v[4:7] offset:17408\n" \
"ds_write_b128 v69, v[8:11] offset:18432\n" \
"ds_write_b128 v69, v[12:15] offset:19456\n" \
"ds_read_b64_tr_b16 v[16:17], v52 offset:20480\n" \
"ds_read_b64_tr_b16 v[18:19], v52 offset:20736\n" \
"ds_read_b64_tr_b16 v[20:21], v53 offset:20480\n" \
"ds_read_b64_tr_b16 v[22:23], v53 offset:20736\n" \
"ds_read_b64_tr_b16 v[24:25], v54 offset:20480\n" \
"ds_read_b64_tr_b16 v[26:27], v54 offset:20736\n" \
"ds_read_b64_tr_b16 v[28:29], v55 offset:20480\n" \
"ds_read_b64_tr_b16 v[30:31], v55 offset:20736\n" \
"s_waitcnt lgkmcnt(0)\n" \
"s_waitcnt vmcnt(9)\n" \
"s_barrier\n" \
"ds_write_b128 v69, v[16:19] offset:20480\n" \
"ds_write_b128 v69, v[20:23] offset:21504\n" \
"ds_write_b128 v69, v[24:27] offset:22528\n" \
"ds_write_b128 v69, v[28:31] offset:23552\n" \
"ds_read_b64_tr_b16 v[0:1], v52 offset:24576\n" \
"ds_read_b64_tr_b16 v[2:3], v52 offset:24832\n" \
"ds_read_b64_tr_b16 v[4:5], v53 offset:24576\n" \
"ds_read_b64_tr_b16 v[6:7], v53 offset:24832\n" \
"ds_read_b64_tr_b16 v[8:9], v54 offset:24576\n" \
"ds_read_b64_tr_b16 v[10:11], v54 offset:24832\n" \
"ds_read_b64_tr_b16 v[12:13], v55 offset:24576\n" \
"ds_read_b64_tr_b16 v[14:15], v55 offset:24832\n" \
"s_waitcnt lgkmcnt(0)\n" \
"s_waitcnt vmcnt(4)\n" \
"s_barrier\n" \
".Lh_hbc%=:\n" \
"v_cmp_eq_u32 vcc, v56, v57\n" \
"s_nop 4\n" \
"s_cbranch_vccnz .Lh_hbs%=\n" \
"global_load_dword v57, v67, s[56:57] sc1\n" \
"ds_write_b128 v69, v[0:3] offset:24576\n" \
"ds_write_b128 v69, v[4:7] offset:25600\n" \
"ds_write_b128 v69, v[8:11] offset:26624\n" \
"ds_write_b128 v69, v[12:15] offset:27648\n" \
"ds_read_b64_tr_b16 v[16:17], v52 offset:28672\n" \
"ds_read_b64_tr_b16 v[18:19], v52 offset:28928\n" \
"ds_read_b64_tr_b16 v[20:21], v53 offset:28672\n" \
"ds_read_b64_tr_b16 v[22:23], v53 offset:28928\n" \
"ds_read_b64_tr_b16 v[24:25], v54 offset:28672\n" \
"ds_read_b64_tr_b16 v[26:27], v54 offset:28928\n" \
"ds_read_b64_tr_b16 v[28:29], v55 offset:28672\n" \
"ds_read_b64_tr_b16 v[30:31], v55 offset:28928\n" \
"s_waitcnt lgkmcnt(0)\n" \
"s_waitcnt vmcnt(1)\n" \
"s_barrier\n" \
"ds_write_b128 v69, v[16:19] offset:28672\n" \
"ds_write_b128 v69, v[20:23] offset:29696\n" \
"ds_write_b128 v69, v[24:27] offset:30720\n" \
"ds_write_b128 v69, v[28:31] offset:31744\n" \
"ds_read_b64_tr_b16 v[0:1], v52 offset:32768\n" \
"ds_read_b64_tr_b16 v[2:3], v52 offset:33024\n" \
"ds_read_b64_tr_b16 v[4:5], v53 offset:32768\n" \
"ds_read_b64_tr_b16 v[6:7], v53 offset:33024\n" \
"ds_read_b64_tr_b16 v[8:9], v54 offset:32768\n" \
"ds_read_b64_tr_b16 v[10:11], v54 offset:33024\n" \
"ds_read_b64_tr_b16 v[12:13], v55 offset:32768\n" \
"ds_read_b64_tr_b16 v[14:15], v55 offset:33024\n" \
"s_waitcnt lgkmcnt(0)\n" \
"s_barrier\n" \
"ds_write_b128 v69, v[0:3] offset:32768\n" \
"ds_write_b128 v69, v[4:7] offset:33792\n" \
"ds_write_b128 v69, v[8:11] offset:34816\n" \
"ds_write_b128 v69, v[12:15] offset:35840\n" \
"s_add_u32 m0, s68, 34816\n" \
"v_readfirstlane_b32 s64, v64\n" \
"v_readfirstlane_b32 s65, v65\n" \
"s_mov_b32 s92, 1\n" \
"s_branch .Lh_kv1%=\n" \
".Lh_hbs%=:\n" \
"s_sub_u32 s93, s93, 1\n" \
"s_cbranch_scc1 .Lh_end%=\n" \
"global_load_dword v57, v66, s[56:57] sc1\n" \
"s_waitcnt vmcnt(0)\n" \
"s_branch .Lh_hbc%=\n" \
".Lh_kv0%=:\n" \
"s_sub_u32 s93, s93, 1\n" \
"s_cbranch_scc1 .Lh_end%=\n" \
"global_load_dword v57, v67, s[56:57] sc1\n" \
".Lh_kv1%=:\n" \
"s_waitcnt vmcnt(0)\n" \
"v_cmp_eq_u32 vcc, 0, v57\n" \
"s_nop 4\n" \
"s_cbranch_vccnz .Lh_kv0%=\n" \
"v_min_u32_dpp v57, v57, v57 row_shr:1 row_mask:0xf bank_mask:0xf\n" \
"s_nop 1\n" \
"v_min_u32_dpp v57, v57, v57 row_shr:2 row_mask:0xf bank_mask:0xf\n" \
"s_nop 1\n" \
"v_min_u32_dpp v57, v57, v57 row_shr:4 row_mask:0xf bank_mask:0xf\n" \
"s_nop 1\n" \
"v_min_u32_dpp v57, v57, v57 row_shr:8 row_mask:0xf bank_mask:0xf\n" \
"s_nop 0\n" \
"v_readlane_b32 s84, v57, 15\n" \
"v_readlane_b32 s85, v57, 31\n" \
"s_min_u32 s84, s84, s85\n" \
"s_and_b32 s84, s84, 1\n" \
"s_lshl_b32 s84, s84, 13\n" \
"s_add_u32 s40, s40, s84\n" \
"s_add_u32 s42, s42, s84\n" \
"s_add_u32 s46, s46, s84\n" \
"s_lshl1_add_u32 s48, s84, s48\n" \
"s_add_u32 s52, s52, s84\n" \
"s_sub_u32 s54, s54, s84\n" \
"s_mov_b32 s76, s40\n" \
"s_mov_b32 s77, s47\n" \
"s_mov_b32 s78, s42\n" \
"s_mov_b32 s79, s47\n" \
"s_xor_b32 s44, s44, s84\n" \
"s_xor_b32 s50, s50, s84\n" \
"global_load_dwordx4 v[60:63], v68, s[44:45] sc1\n" \
"global_load_dwordx4 v[64:67], v68, s[44:45] offset:1024 sc1\n" \
"global_load_lds_dwordx4 v68, s[44:45] offset:2048 sc1\n" \
"global_load_lds_dwordx4 v68, s[44:45] offset:3072 sc1\n" \
"s_add_u32 s60, s42, 69632\n" \
"s_add_u32 s60, s60, s31\n" \
"s_mov_b32 s61, s47\n" \
"s_mov_b32 s62, 0x33333333\n" \
"s_mov_b32 s63, 0x33333333\n" \
"s_memtime s[4:5]\n" \
"s_memrealtime s[6:7]\n" \
"s_mov_b32 s58, 1533542\n" \
"s_waitcnt lgkmcnt(0)\n" \
"s_mov_b32 s67, s4\n" \
"s_lshl_b32 s66, s6, 16\n" \
"s_sub_u32 s83, s66, 0x2000000\n" \
"s_waitcnt lgkmcnt(0)\n" \
"s_mov_b32 s10, 0\n" \
"s_branch .Lh_load%=\n" \
"s_nop 0\n" \
"s_nop 0\n" \
"s_nop 0\n" \
"s_nop 0\n" \
"s_nop 0\n" \
"s_nop 0\n" \
"s_nop 0\n" \
"s_nop 0\n" \
"s_nop 0\n" \
"s_nop 0\n" \
"s_nop 0\n" \
"s_nop 0\n" \
"s_nop 0\n" \
"s_nop 0\n" \
"s_nop 0\n" \
"s_nop 0\n" \
"s_nop 0\n" \
"s_nop 0\n" \
"s_nop 0\n" \
"s_nop 0\n" \
"s_nop 0\n" \
"s_nop 0\n" \
"s_nop 0\n" \
"s_nop 0\n" \
"s_nop 0\n" \
"s_nop 0\n" \
"s_nop 0\n" \
"s_nop 0\n" \
"s_nop 0\n" \
"s_nop 0\n" \
"s_nop 0\n" \
"s_nop 0\n" \
"s_nop 0\n" \
"s_nop 0\n" \
"s_nop 0\n" \
"s_nop 0\n" \
"s_nop 0\n" \
"s_nop 0\n" \
"s_nop 0\n" \
"s_nop 0\n" \
"s_nop 0\n" \
"s_nop 0\n" \
"s_nop 0\n" \
"s_nop 0\n" \
"s_nop 0\n" \
"s_nop 0\n" \
"s_nop 0\n" \
"s_nop 0\n" \
"s_nop 0\n" \
"s_nop 0\n" \
"s_nop 0\n" \
"s_nop 0\n" \
"s_nop 0\n" \
"s_nop 0\n" \
"s_nop 0\n" \
"s_nop 0\n" \
"s_nop 0\n" \
"s_nop 0\n" \
"s_nop 0\n" \
"s_nop 0\n" \
"s_nop 0\n" \
"s_nop 0\n" \
"s_nop 0\n" \
"s_nop 0\n" \
".Lh_load%=:\n" \
"global_load_dwordx4 v[0:3], v68, s[76:77] offset:-4096 sc1\n" \
"v_mov_b64 v[36:37], 0\n" \
"s_add_u32 s72, s72, 1\n" \
"s_add_u32 s92, s92, 1\n" \
"s_mul_hi_u32 s7, s58, 7844000\n" \
"v_mov_b64 v[38:39], 0\n" \
"s_sub_u32 s59, 5609, s7\n" \
"s_andn2_b32 s59, s59, 15\n" \
"s_sub_u32 s7, s67, 1314\n" \
"v_mov_b64 v[40:41], 0\n" \
"s_mul_i32 s7, s7, s59\n" \
"s_and_b32 s5, s72, 15\n" \
"s_add_u32 s6, s66, s5\n" \
"v_mov_b64 v[42:43], 0\n" \
"s_sub_u32 s68, s6, s7\n" \
"s_add_u32 s8, s83, s64\n" \
"global_load_dwordx4 v[4:7], v68, s[76:77] offset:-3072 sc1\n" \
"v_mov_b64 v[44:45], 0\n" \
"s_add_u32 s9, s8, s65\n" \
"s_sub_u32 s68, s68, s8\n" \
"s_add_u32 s50, s50, 16384\n" \
"v_mov_b64 v[46:47], 0\n" \
"s_addc_u32 s51, s51, 0\n" \
"s_sub_u32 s46, s48, s46\n" \
"s_xor_b32 s60, s60, 0x10000\n" \
"v_mov_b64 v[48:49], 0\n" \
"s_mov_b64 s[12:13], s[44:45]\n" \
"s_add_u32 s44, s44, 32768\n" \
"s_addc_u32 s45, s45, 0\n" \
"v_mov_b64 v[50:51], 0\n" \
"s_cmp_ge_u32 s10, s92\n" \
"s_cmov_b64 s[12:13], s[44:45]\n" \
"global_load_dwordx4 v[8:11], v68, s[76:77] offset:-2048 sc1\n" \
"v_mov_b64 v[52:53], 0\n" \
"s_cselect_b32 s7, 0, 131072\n" \
"s_add_u32 s8, s8, s7\n" \
"s_add_u32 s9, s9, s7\n" \
"v_mov_b64 v[54:55], 0\n" \
"s_nop 2\n" \
"v_mov_b64 v[56:57], 0\n" \
"s_nop 2\n" \
"v_mov_b64 v[58:59], 0\n" \
"s_nop 1\n" \
"global_load_dwordx4 v[12:15], v68, s[76:77] offset:-1024 sc1\n" \
"v_and_b32 v79, 0xe0, v71\n" \
"s_nop 2\n" \
"v_lshlrev_b32 v79, 9, v79\n" \
"s_nop 2\n" \
"v_writelane_b32 v79, s54, 63\n" \
"s_nop 2\n" \
"v_mov_b32 v77, v77\n" \
"s_nop 1\n" \
"global_load_dwordx4 v[16:19], v68, s[76:77] offset:0 sc1\n" \
"v_mov_b32 v77, v77\n" \
"s_nop 2\n" \
"v_mov_b32 v77, v77\n" \
"s_nop 2\n" \
"v_mov_b32 v77, v77\n" \
"s_nop 2\n" \
"v_mov_b32 v77, v77\n" \
"s_nop 1\n" \
"global_load_dwordx4 v[20:23], v68, s[76:77] offset:1024 sc1\n" \
"v_mov_b32 v77, v77\n" \
"s_nop 2\n" \
"v_mov_b32 v77, v77\n" \
"s_nop 2\n" \
"v_mov_b32 v77, v77\n" \
"s_nop 2\n" \
"v_mov_b32 v77, v77\n" \
"s_nop 1\n" \
"global_load_dwordx4 v[24:27], v68, s[76:77] offset:2048 sc1\n" \
"v_mov_b32 v77, v77\n" \
"s_nop 2\n" \
".Lh_qk%=:\n" \
"s_waitcnt vmcnt(6)\n" \
"v_smfmac_f32_16x16x64_bf16 v[36:39], v[0:3], a[0:7], v70\n" \
"s_nop 0\n" \
"global_load_dwordx4 v[28:31], v68, s[76:77] offset:3072 sc1\n" \
"v_cmp_eq_u32 vcc, -1, v0\n" \
"v_smfmac_f32_16x16x64_bf16 v[40:43], v[0:3], a[144:151], v70\n" \
"v_smfmac_f32_16x16x64_bf16 v[44:47], v[0:3], a[72:79], v70\n" \
"s_cbranch_vccnz .Lh_rt0%=\n" \
".Lh_ct0%=:\n" \
"v_smfmac_f32_16x16x64_bf16 v[48:51], v[0:3], a[216:223], v70\n" \
"s_waitcnt vmcnt(6)\n" \
"v_smfmac_f32_16x16x64_bf16 v[36:39], v[4:7], a[8:15], v70\n" \
"s_nop 0\n" \
"global_load_dwordx4 v[32:35], v68, s[78:79] sc1\n" \
"v_cmp_eq_u32 vcc, -1, v4\n" \
"v_smfmac_f32_16x16x64_bf16 v[40:43], v[4:7], a[152:159], v70\n" \
"v_smfmac_f32_16x16x64_bf16 v[44:47], v[4:7], a[80:87], v70\n" \
"s_cbranch_vccnz .Lh_rt1%=\n" \
".Lh_ct1%=:\n" \
"v_smfmac_f32_16x16x64_bf16 v[48:51], v[4:7], a[224:231], v70\n" \
"s_waitcnt vmcnt(6)\n" \
"v_smfmac_f32_16x16x64_bf16 v[36:39], v[8:11], a[16:23], v70\n" \
"v_cmp_eq_u32 vcc, -1, v8\n" \
"v_smfmac_f32_16x16x64_bf16 v[40:43], v[8:11], a[160:167], v70\n" \
"v_smfmac_f32_16x16x64_bf16 v[44:47], v[8:11], a[88:95], v70\n" \
"s_cbranch_vccnz .Lh_rt2%=\n" \
".Lh_ct2%=:\n" \
"v_smfmac_f32_16x16x64_bf16 v[48:51], v[8:11], a[232:239], v70\n" \
"s_waitcnt vmcnt(5)\n" \
"v_smfmac_f32_16x16x64_bf16 v[36:39], v[12:15], a[24:31], v70\n" \
"s_memtime s[80:81]\n" \
"v_cmp_eq_u32 vcc, -1, v12\n" \
"v_smfmac_f32_16x16x64_bf16 v[40:43], v[12:15], a[168:175], v70\n" \
"v_smfmac_f32_16x16x64_bf16 v[44:47], v[12:15], a[96:103], v70\n" \
"s_cbranch_vccnz .Lh_rt3%=\n" \
".Lh_ct3%=:\n" \
"v_smfmac_f32_16x16x64_bf16 v[48:51], v[12:15], a[240:247], v70\n" \
"s_waitcnt lgkmcnt(0)\n" \
"s_waitcnt vmcnt(4)\n" \
"v_smfmac_f32_16x16x64_bf16 v[36:39], v[16:19], a[32:39], v70\n" \
"s_mul_i32 s82, s80, s59\n" \
"s_add_u32 s82, s82, s68\n" \
"v_cmp_eq_u32 vcc, -1, v16\n" \
"v_smfmac_f32_16x16x64_bf16 v[40:43], v[16:19], a[176:183], v70\n" \
"v_smfmac_f32_16x16x64_bf16 v[44:47], v[16:19], a[104:111], v70\n" \
"s_cbranch_vccnz .Lh_rt4%=\n" \
".Lh_ct4%=:\n" \
"v_smfmac_f32_16x16x64_bf16 v[48:51], v[16:19], a[248:255], v70\n" \
"s_cmp_gt_i32 s82, 0\n" \
"s_cselect_b32 s81, s9, s8\n" \
"s_waitcnt vmcnt(3)\n" \
"v_smfmac_f32_16x16x64_bf16 v[36:39], v[20:23], a[40:47], v70\n" \
"s_add_u32 s82, s82, s81\n" \
"s_pack_lh_b32_b16 s81, s92, s82\n" \
"v_cmp_eq_u32 vcc, -1, v20\n" \
"v_smfmac_f32_16x16x64_bf16 v[40:43], v[20:23], a[184:191], v70\n" \
"v_mov_b32 v78, s82\n" \
"v_smfmac_f32_16x16x64_bf16 v[44:47], v[20:23], a[112:119], v70\n" \
"s_cbranch_vccnz .Lh_rt5%=\n" \
".Lh_ct5%=:\n" \
"v_smfmac_f32_16x16x64_bf16 v[48:51], v[20:23], v[80:87], v70\n" \
"v_writelane_b32 v78, s81, 63\n" \
"s_waitcnt vmcnt(2)\n" \
"v_smfmac_f32_16x16x64_bf16 v[36:39], v[24:27], a[48:55], v70\n" \
"s_nop 0\n" \
"global_store_dword v79, v78, s[52:53] sc1\n" \
"v_cmp_eq_u32 vcc, -1, v24\n" \
"v_smfmac_f32_16x16x64_bf16 v[40:43], v[24:27], a[192:199], v70\n" \
"v_smfmac_f32_16x16x64_bf16 v[44:47], v[24:27], a[120:127], v70\n" \
"s_cbranch_vccnz .Lh_rt6%=\n" \
".Lh_ct6%=:\n" \
"v_smfmac_f32_16x16x64_bf16 v[48:51], v[24:27], v[88:95], v70\n" \
"s_waitcnt vmcnt(2)\n" \
"v_smfmac_f32_16x16x64_bf16 v[36:39], v[28:31], a[56:63], v70\n" \
"v_cmp_eq_u32 vcc, -1, v28\n" \
"v_smfmac_f32_16x16x64_bf16 v[40:43], v[28:31], a[200:207], v70\n" \
"v_smfmac_f32_16x16x64_bf16 v[44:47], v[28:31], a[128:135], v70\n" \
"s_cbranch_vccnz .Lh_rt7%=\n" \
".Lh_ct7%=:\n" \
"v_smfmac_f32_16x16x64_bf16 v[48:51], v[28:31], v[96:103], v70\n" \
"s_waitcnt vmcnt(1)\n" \
"v_smfmac_f32_16x16x64_bf16 v[36:39], v[32:35], a[64:71], v70\n" \
"v_smfmac_f32_16x16x64_bf16 v[40:43], v[32:35], a[208:215], v70\n" \
"v_smfmac_f32_16x16x64_bf16 v[44:47], v[32:35], a[136:143], v70\n" \
"s_mov_b64 vcc, s[94:95]\n" \
"v_add_f32 v36, v36, v38\n" \
"v_smfmac_f32_16x16x64_bf16 v[48:51], v[32:35], v[104:111], v70\n" \
"v_add_f32 v37, v37, v39\n" \
"v_add_f32 v40, v40, v42\n" \
"v_add_f32 v41, v41, v43\n" \
"v_cvt_pk_bf16_f32 v36, v36, v37\n" \
"v_cvt_pk_bf16_f32 v37, v40, v41\n" \
"v_pk_add_f32 v[44:45], v[44:45], v[46:47]\n" \
"v_pk_add_f32 v[48:49], v[48:49], v[50:51]\n" \
"v_mfma_f32_16x16x16_bf16 v[40:43], v[36:37], v[72:73], 0\n" \
"v_cvt_pk_bf16_f32 v44, v44, v45\n" \
"v_cvt_pk_bf16_f32 v45, v48, v49\n" \
"v_cmp_u_f32 s[86:87], v48, v49\n" \
"v_lshrrev_b32 v79, 16, v70\n" \
"v_mfma_f32_16x16x16_bf16 v[48:51], v[44:45], v[72:73], 0\n" \
"s_nop 0\n" \
"v_mov_b32 v78, -1\n" \
"s_cmp_lg_u64 s[86:87], 0\n" \
"v_smfmac_f32_16x16x64_bf16 v[52:55], v[0:3], v[112:119], v70\n" \
"s_cbranch_scc1 .Lh_slow%=\n" \
".Lh_resume%=:\n" \
"v_cvt_pk_bf16_f32 v36, v40, v41\n" \
"v_cvt_pk_bf16_f32 v37, v42, v43\n" \
"v_cvt_pk_bf16_f32 v38, v48, v49\n" \
"v_cvt_pk_bf16_f32 v39, v50, v51\n" \
"s_add_u32 s84, s72, 1\n" \
"s_cmp_eq_u32 s72, 63\n" \
"v_mfma_f32_16x16x32_bf16 v[48:51], v[36:39], v[36:39], 0\n" \
"s_cbranch_scc1 .Lh_last%=\n" \
"v_mfma_f32_16x16x32_bf16 v[40:43], v[60:63], v[36:39], 0\n" \
"s_and_b32 s84, s84, 3\n" \
"s_lshl_b32 s85, s84, 10\n" \
"s_add_u32 s78, s42, s85\n" \
"v_mfma_f32_16x16x32_bf16 v[44:47], v[64:67], v[36:39], 0\n" \
"s_lshl4_add_u32 s76, s85, s40\n" \
"v_cndmask_b32_dpp v48, v51, v49, vcc quad_perm:[0,0,2,2] row_mask:0xf bank_mask:0xf\n" \
"v_smfmac_f32_16x16x64_bf16 v[56:59], v[0:3], v[184:191], v70\n" \
"s_nop 0\n" \
"ds_write_b32 v77, v48\n" \
"v_mov_b64 v[0:1], 0\n" \
"v_mov_b32 v77, v77\n" \
"v_cndmask_b32_dpp v40, v44, v40, vcc quad_perm:[0,0,2,2] row_mask:0xf bank_mask:0xf\n" \
"v_cndmask_b32_dpp v41, v45, v41, vcc quad_perm:[0,0,2,2] row_mask:0xf bank_mask:0xf\n" \
"v_cndmask_b32_dpp v42, v46, v42, vcc quad_perm:[0,0,2,2] row_mask:0xf bank_mask:0xf\n" \
"v_cndmask_b32_dpp v43, v47, v43, vcc quad_perm:[0,0,2,2] row_mask:0xf bank_mask:0xf\n" \
"v_smfmac_f32_16x16x64_bf16 v[52:55], v[4:7], v[120:127], v70\n" \
"s_nop 0\n" \
"ds_write_b128 v76, v[40:43]\n" \
"v_smfmac_f32_16x16x64_bf16 v[56:59], v[4:7], v[192:199], v70\n" \
"s_nop 0\n" \
"ds_write_b32 v74, v78\n" \
"v_smfmac_f32_16x16x64_bf16 v[52:55], v[8:11], v[128:135], v70\n" \
"s_nop 0\n" \
"ds_write_b32 v74, v78 offset:128\n" \
"v_smfmac_f32_16x16x64_bf16 v[56:59], v[8:11], v[200:207], v70\n" \
"s_nop 0\n" \
"global_store_dwordx4 v68, v[36:39], s[50:51] sc1\n" \
"v_smfmac_f32_16x16x64_bf16 v[52:55], v[12:15], v[136:143], v70\n" \
"s_waitcnt lgkmcnt(0)\n" \
"s_barrier\n" \
"ds_read_b128 v[48:51], v68 offset:4096\n" \
"v_smfmac_f32_16x16x64_bf16 v[56:59], v[12:15], v[208:215], v70\n" \
"s_nop 0\n" \
"ds_read_b128 v[40:43], v68\n" \
"ds_read_b128 v[44:47], v68 offset:1024\n" \
"v_smfmac_f32_16x16x64_bf16 v[52:55], v[16:19], v[144:151], v70\n" \
"s_nop 0\n" \
"ds_read_b128 v[60:63], v68 offset:2048\n" \
"ds_read_b128 v[64:67], v68 offset:3072\n" \
"v_smfmac_f32_16x16x64_bf16 v[56:59], v[16:19], v[216:223], v70\n" \
"s_nop 0\n" \
"ds_read_b64 v[8:9], v69 offset:36864\n" \
"v_mov_b64 v[2:3], 0\n" \
"v_smfmac_f32_16x16x64_bf16 v[52:55], v[20:23], v[152:159], v70\n" \
"s_nop 0\n" \
"ds_read_b64 v[12:13], v69 offset:36872\n" \
"v_mov_b64 v[4:5], 0\n" \
"v_smfmac_f32_16x16x64_bf16 v[56:59], v[20:23], v[224:231], v70\n" \
"s_nop 0\n" \
"ds_read_b64 v[16:17], v69 offset:37888\n" \
"v_mov_b64 v[6:7], 0\n" \
"v_smfmac_f32_16x16x64_bf16 v[52:55], v[24:27], v[160:167], v70\n" \
"s_nop 0\n" \
"ds_read_b64 v[20:21], v69 offset:37896\n" \
"v_smfmac_f32_16x16x64_bf16 v[56:59], v[24:27], v[232:239], v70\n" \
"s_waitcnt lgkmcnt(8)\n" \
"v_add_f32 v48, v48, v50\n" \
"v_smfmac_f32_16x16x64_bf16 v[52:55], v[28:31], v[168:175], v70\n" \
"s_waitcnt lgkmcnt(4)\n" \
"v_add_f32 v49, v49, v51\n" \
"v_add_f32 v40, v40, v44\n" \
"v_add_f32 v48, v48, v49\n" \
"v_add_f32 v41, v41, v45\n" \
"v_pk_add_f32 v[42:43], v[42:43], v[46:47]\n" \
"v_pk_add_f32 v[40:41], v[40:41], v[60:61]\n" \
"v_pk_add_f32 v[42:43], v[42:43], v[62:63]\n" \
"v_pk_add_f32 v[40:41], v[40:41], v[64:65]\n" \
"v_pk_add_f32 v[42:43], v[42:43], v[66:67]\n" \
"v_max3_f32 v78, v40, v41, v48\n" \
"v_max3_f32 v78, v78, v42, v43\n" \
"v_smfmac_f32_16x16x64_bf16 v[56:59], v[28:31], v[240:247], v70\n" \
"s_nop 0\n" \
"ds_read_b128 v[24:27], v69 offset:0\n" \
"global_load_dwordx4 v[60:63], v68, s[12:13] sc1\n" \
"v_max_f32_dpp v50, v78, v78 quad_perm:[2,3,0,1] row_mask:0xf bank_mask:0xf\n" \
"v_max_f32_dpp v78, v78, v78 quad_perm:[2,3,0,1] row_mask:0xf bank_mask:0xf\n" \
"v_smfmac_f32_16x16x64_bf16 v[52:55], v[32:35], v[176:183], v70\n" \
"s_nop 0\n" \
"ds_read_b128 v[28:31], v69 offset:1024\n" \
"global_load_dwordx4 v[64:67], v68, s[12:13] offset:1024 sc1\n" \
"v_permlane16_swap_b32 v78, v50\n" \
"v_max_f32 v51, v78, v50\n" \
"v_max_f32 v78, v78, v50\n" \
"v_smfmac_f32_16x16x64_bf16 v[56:59], v[32:35], v[248:255], v70\n" \
"s_waitcnt lgkmcnt(2)\n" \
"ds_read_b128 v[36:39], v69 offset:3072\n" \
"global_load_lds_dwordx4 v68, s[12:13] offset:2048 sc1\n" \
"v_permlane32_swap_b32 v78, v51\n" \
"v_max_f32 v78, v78, v51\n" \
"v_mul_f32 v78, s75, v78\n" \
"v_fma_f32 v48, v48, s75, -v78\n" \
"v_pk_fma_f32 v[40:41], v[40:41], s[74:75], v[78:79] op_sel_hi:[1,1,0] neg_lo:[0,0,1] neg_hi:[0,0,1]\n" \
"v_pk_fma_f32 v[42:43], v[42:43], s[74:75], v[78:79] op_sel_hi:[1,1,0] neg_lo:[0,0,1] neg_hi:[0,0,1]\n" \
"v_exp_f32 v48, v48\n" \
"v_exp_f32 v40, v40\n" \
"v_exp_f32 v41, v41\n" \
"v_exp_f32 v42, v42\n" \
"v_exp_f32 v43, v43\n" \
"v_cvt_pk_bf16_f32 v49, v48, v48\n" \
"v_cvt_pk_bf16_f32 v44, v40, v41\n" \
"v_cvt_pk_bf16_f32 v10, v52, v54\n" \
"v_cvt_pk_bf16_f32 v14, v53, v55\n" \
"v_cndmask_b32_e64 v45, 0, v49, s[62:63]\n" \
"v_cndmask_b32_e64 v47, v49, 0, s[62:63]\n" \
"v_cvt_pk_bf16_f32 v46, v42, v43\n" \
"v_cvt_pk_bf16_f32 v18, v56, v58\n" \
"v_cvt_pk_bf16_f32 v22, v57, v59\n" \
"v_smfmac_f32_16x16x64_bf16 v[0:3], v[44:47], v[8:15], v79\n" \
"s_nop 0\n" \
"ds_read_b128 v[32:35], v69 offset:2048\n" \
"v_mov_b64 v[40:41], s[88:89]\n" \
"v_smfmac_f32_16x16x64_bf16 v[4:7], v[44:47], v[16:23], v79\n" \
"s_nop 0\n" \
"ds_read_b128 v[8:11], v69 offset:4096\n" \
"ds_read_b128 v[12:15], v69 offset:5120\n" \
"global_load_lds_dwordx4 v68, s[12:13] offset:3072 sc1\n" \
"v_add_f32 v0, v0, v2\n" \
"v_add_f32 v1, v1, v3\n" \
"v_mov_b64 v[42:43], s[88:89]\n" \
"v_add_f32 v52, v52, v54\n" \
"v_pk_add_f32 v[4:5], v[4:5], v[6:7]\n" \
"v_cvt_pk_bf16_f32 v78, v0, v4\n" \
"v_cvt_pk_bf16_f32 v79, v1, v5\n" \
"v_mfma_f32_16x16x32_bf16 v[48:51], v[44:47], v[40:43], 0\n" \
"s_nop 0\n" \
"ds_write2_b32 v74, v78, v79 offset1:32\n" \
"v_mfma_f32_16x16x32_bf16 v[0:3], v[44:47], v[40:43], 0\n" \
"s_barrier\n" \
"ds_read_b128 v[16:19], v75\n" \
"ds_read_b128 v[20:23], v75 offset:1024\n" \
"v_add_f32 v53, v53, v55\n" \
"ds_read_b128 v[40:43], v69 offset:6144\n" \
"ds_read_b128 v[44:47], v69 offset:7168\n" \
"v_mov_b32 v55, 0\n" \
"v_pk_add_f32 v[48:49], v[48:49], v[50:51]\n" \
"v_mov_b64 v[4:5], 0\n" \
"v_mov_b64 v[6:7], 0\n" \
"v_mov_b64 v[0:1], 0\n" \
"v_mov_b64 v[2:3], 0\n" \
"v_rcp_f32 v48, v48\n" \
"v_rcp_f32 v49, v49\n" \
".Lh_wo%=:\n" \
"s_waitcnt lgkmcnt(3)\n" \
"v_smfmac_f32_16x16x64_bf16 v[0:3], v[16:19], v[24:31], v70\n" \
"s_waitcnt lgkmcnt(2)\n" \
"global_load_dword v54, v55, s[56:57] sc1\n" \
"v_smfmac_f32_16x16x64_bf16 v[0:3], v[20:23], v[32:39], v70\n" \
"s_nop 0\n" \
"ds_read_b128 v[24:27], v69 offset:8192\n" \
"ds_read_b128 v[28:31], v69 offset:9216\n" \
"ds_read_b128 v[32:35], v69 offset:10240\n" \
"ds_read_b128 v[36:39], v69 offset:11264\n" \
"v_min_u32 v50, 124, v71\n" \
"v_lshl_or_b32 v50, v50, 3, v50\n" \
"v_and_b32 v50, 0x38c, v50\n" \
"v_pk_add_f32 v[0:1], v[0:1], v[2:3]\n" \
"v_pk_mul_f32 v[0:1], v[0:1], v[48:49]\n" \
"v_cvt_pk_bf16_f32 v78, v0, v1\n" \
"v_smfmac_f32_16x16x64_bf16 v[4:7], v[16:19], v[8:15], v70\n" \
"s_waitcnt lgkmcnt(4)\n" \
"global_load_dword v51, v50, s[60:61] sc1\n" \
"global_store_dword v71, v78, s[46:47] offset:0 sc1\n" \
"v_smfmac_f32_16x16x64_bf16 v[4:7], v[20:23], v[40:47], v70\n" \
"s_nop 0\n" \
"ds_read_b128 v[8:11], v69 offset:12288\n" \
"ds_read_b128 v[12:15], v69 offset:13312\n" \
"ds_read_b128 v[40:43], v69 offset:14336\n" \
"ds_read_b128 v[44:47], v69 offset:15360\n" \
"v_mov_b64 v[0:1], 0\n" \
"v_mov_b64 v[2:3], 0\n" \
"s_waitcnt lgkmcnt(6)\n" \
"v_pk_add_f32 v[4:5], v[4:5], v[6:7]\n" \
"v_pk_mul_f32 v[4:5], v[4:5], v[48:49]\n" \
"v_cvt_pk_bf16_f32 v79, v4, v5\n" \
"v_smfmac_f32_16x16x64_bf16 v[0:3], v[16:19], v[24:31], v70\n" \
"s_waitcnt lgkmcnt(4)\n" \
"global_store_dword v71, v79, s[46:47] offset:256 sc1\n" \
"v_smfmac_f32_16x16x64_bf16 v[0:3], v[20:23], v[32:39], v70\n" \
"s_nop 0\n" \
"ds_read_b128 v[24:27], v69 offset:16384\n" \
"ds_read_b128 v[28:31], v69 offset:17408\n" \
"ds_read_b128 v[32:35], v69 offset:18432\n" \
"ds_read_b128 v[36:39], v69 offset:19456\n" \
"v_mov_b64 v[4:5], 0\n" \
"v_mov_b64 v[6:7], 0\n" \
"s_waitcnt lgkmcnt(6)\n" \
"v_pk_add_f32 v[0:1], v[0:1], v[2:3]\n" \
"v_pk_mul_f32 v[0:1], v[0:1], v[48:49]\n" \
"v_cvt_pk_bf16_f32 v78, v0, v1\n" \
"v_smfmac_f32_16x16x64_bf16 v[4:7], v[16:19], v[8:15], v70\n" \
"s_waitcnt lgkmcnt(4)\n" \
"global_store_dword v71, v78, s[46:47] offset:512 sc1\n" \
"v_smfmac_f32_16x16x64_bf16 v[4:7], v[20:23], v[40:47], v70\n" \
"s_nop 0\n" \
"ds_read_b128 v[8:11], v69 offset:20480\n" \
"ds_read_b128 v[12:15], v69 offset:21504\n" \
"ds_read_b128 v[40:43], v69 offset:22528\n" \
"ds_read_b128 v[44:47], v69 offset:23552\n" \
"v_mov_b64 v[0:1], 0\n" \
"v_mov_b64 v[2:3], 0\n" \
"s_waitcnt lgkmcnt(6)\n" \
"v_pk_add_f32 v[4:5], v[4:5], v[6:7]\n" \
"v_pk_mul_f32 v[4:5], v[4:5], v[48:49]\n" \
"v_cvt_pk_bf16_f32 v79, v4, v5\n" \
"v_smfmac_f32_16x16x64_bf16 v[0:3], v[16:19], v[24:31], v70\n" \
"s_waitcnt lgkmcnt(4)\n" \
"global_store_dword v71, v79, s[46:47] offset:768 sc1\n" \
"v_smfmac_f32_16x16x64_bf16 v[0:3], v[20:23], v[32:39], v70\n" \
"s_nop 0\n" \
"ds_read_b128 v[24:27], v69 offset:24576\n" \
"ds_read_b128 v[28:31], v69 offset:25600\n" \
"ds_read_b128 v[32:35], v69 offset:26624\n" \
"ds_read_b128 v[36:39], v69 offset:27648\n" \
"v_mov_b64 v[4:5], 0\n" \
"v_mov_b64 v[6:7], 0\n" \
"s_waitcnt lgkmcnt(6)\n" \
"v_pk_add_f32 v[0:1], v[0:1], v[2:3]\n" \
"v_pk_mul_f32 v[0:1], v[0:1], v[48:49]\n" \
"v_cvt_pk_bf16_f32 v78, v0, v1\n" \
"v_smfmac_f32_16x16x64_bf16 v[4:7], v[16:19], v[8:15], v70\n" \
"s_waitcnt lgkmcnt(4)\n" \
"global_store_dword v71, v78, s[46:47] offset:1024 sc1\n" \
"v_smfmac_f32_16x16x64_bf16 v[4:7], v[20:23], v[40:47], v70\n" \
"s_nop 0\n" \
"ds_read_b128 v[8:11], v69 offset:28672\n" \
"ds_read_b128 v[12:15], v69 offset:29696\n" \
"ds_read_b128 v[40:43], v69 offset:30720\n" \
"ds_read_b128 v[44:47], v69 offset:31744\n" \
"v_mov_b64 v[0:1], 0\n" \
"v_mov_b64 v[2:3], 0\n" \
"s_waitcnt lgkmcnt(6)\n" \
"v_pk_add_f32 v[4:5], v[4:5], v[6:7]\n" \
"v_pk_mul_f32 v[4:5], v[4:5], v[48:49]\n" \
"v_cvt_pk_bf16_f32 v79, v4, v5\n" \
"v_smfmac_f32_16x16x64_bf16 v[0:3], v[16:19], v[24:31], v70\n" \
"s_waitcnt lgkmcnt(4)\n" \
"global_store_dword v71, v79, s[46:47] offset:1280 sc1\n" \
"v_smfmac_f32_16x16x64_bf16 v[0:3], v[20:23], v[32:39], v70\n" \
"s_nop 0\n" \
"ds_read_b128 v[24:27], v69 offset:32768\n" \
"ds_read_b128 v[28:31], v69 offset:33792\n" \
"ds_read_b128 v[32:35], v69 offset:34816\n" \
"ds_read_b128 v[36:39], v69 offset:35840\n" \
"v_mov_b64 v[4:5], 0\n" \
"v_mov_b64 v[6:7], 0\n" \
"s_waitcnt lgkmcnt(6)\n" \
"v_pk_add_f32 v[0:1], v[0:1], v[2:3]\n" \
"v_pk_mul_f32 v[0:1], v[0:1], v[48:49]\n" \
"v_cvt_pk_bf16_f32 v78, v0, v1\n" \
"v_smfmac_f32_16x16x64_bf16 v[4:7], v[16:19], v[8:15], v70\n" \
"s_waitcnt lgkmcnt(4)\n" \
"global_store_dword v71, v78, s[46:47] offset:1536 sc1\n" \
"v_smfmac_f32_16x16x64_bf16 v[4:7], v[20:23], v[40:47], v70\n" \
"v_cmp_u_f32 vcc, v0, v1\n" \
"v_mov_b64 v[0:1], 0\n" \
"v_mov_b64 v[2:3], 0\n" \
"s_waitcnt lgkmcnt(2)\n" \
"v_mov_b32 v77, v77\n" \
"v_mov_b32 v77, v77\n" \
"v_pk_add_f32 v[4:5], v[4:5], v[6:7]\n" \
"v_pk_mul_f32 v[4:5], v[4:5], v[48:49]\n" \
"v_cvt_pk_bf16_f32 v79, v4, v5\n" \
"v_smfmac_f32_16x16x64_bf16 v[0:3], v[16:19], v[24:31], v70\n" \
"s_waitcnt lgkmcnt(0)\n" \
"global_store_dword v71, v79, s[46:47] offset:1792 sc1\n" \
"v_smfmac_f32_16x16x64_bf16 v[0:3], v[20:23], v[32:39], v70\n" \
"s_cbranch_vccnz .Lh_oslow%=\n" \
".Lh_ocont%=:\n" \
"v_cvt_pk_bf16_f32 v52, v52, v53\n" \
"v_add_f32 v56, v56, v58\n" \
"v_add_f32 v57, v57, v59\n" \
"v_cvt_pk_bf16_f32 v53, v56, v57\n" \
"v_mov_b32 v77, v77\n" \
"v_mov_b32 v77, v77\n" \
"v_pk_add_f32 v[0:1], v[0:1], v[2:3]\n" \
"v_pk_mul_f32 v[0:1], v[0:1], v[48:49]\n" \
"v_cvt_pk_bf16_f32 v78, v0, v1\n" \
"global_store_dword v71, v78, s[46:47] offset:2048 sc1\n" \
"global_store_dword v71, v52, s[50:51] offset:1024 sc1\n" \
"global_store_dword v71, v53, s[50:51] offset:1280 sc1\n" \
"s_mov_b32 s81, s10\n" \
"s_waitcnt vmcnt(11)\n" \
".Lh_beval%=:\n" \
"s_memtime s[4:5]\n" \
"s_memrealtime s[6:7]\n" \
"v_subrev_u32 v51, s82, v51\n" \
"v_and_b32 v56, 14, v51\n" \
"s_memtime s[8:9]\n" \
"s_memrealtime s[10:11]\n" \
"v_max_i32_dpp v51, v51, v51 row_shr:1 row_mask:0xf bank_mask:0xf\n" \
"v_cmp_ne_u32 vcc, 0, v56\n" \
"s_memtime s[12:13]\n" \
"s_memrealtime s[14:15]\n" \
"v_max_i32_dpp v51, v51, v51 row_shr:2 row_mask:0xf bank_mask:0xf\n" \
"v_mov_b32 v77, v77\n" \
"s_memtime s[16:17]\n" \
"s_memrealtime s[18:19]\n" \
"v_max_i32_dpp v51, v51, v51 row_shr:4 row_mask:0xf bank_mask:0xf\n" \
"v_mov_b32 v77, v77\n" \
"s_memtime s[20:21]\n" \
"s_memrealtime s[22:23]\n" \
"v_max_i32_dpp v51, v51, v51 row_shr:8 row_mask:0xf bank_mask:0xf\n" \
"v_mov_b32 v77, v77\n" \
"s_memtime s[24:25]\n" \
"s_memrealtime s[26:27]\n" \
"v_readlane_b32 s84, v51, 15\n" \
"v_readlane_b32 s85, v51, 31\n" \
"s_memtime s[28:29]\n" \
"s_memrealtime s[30:31]\n" \
"s_cbranch_vccnz .Lh_repoll%=\n" \
".Lh_bdone%=:\n" \
"s_max_i32 s84, s84, s85\n" \
"s_add_u32 s84, s84, s82\n" \
"v_mov_b32 v56, s84\n" \
"s_cmp_lt_u32 s81, s92\n" \
"ds_write_b32 v77, v56\n" \
"s_cbranch_scc0 .Lh_kvd%=\n" \
".Lh_kvc%=:\n" \
"v_readfirstlane_b32 s84, v54\n" \
"s_cmp_lt_u32 s84, s92\n" \
"s_cbranch_scc1 .Lh_kvs%=\n" \
"global_load_dwordx4 v[60:63], v68, s[44:45] sc1\n" \
"global_load_dwordx4 v[64:67], v68, s[44:45] offset:1024 sc1\n" \
"global_load_lds_dwordx4 v68, s[44:45] offset:2048 sc1\n" \
"global_load_lds_dwordx4 v68, s[44:45] offset:3072 sc1\n" \
".Lh_kvd%=:\n" \
"v_mov_b32 v77, v77\n" \
"s_waitcnt lgkmcnt(0)\n" \
"s_barrier\n" \
"ds_read_b128 v[56:59], v68 offset:4096\n" \
"v_mov_b32 v77, v77\n" \
"s_add_u32 s5, s28, 16\n" \
"s_mov_b32 s7, s8\n" \
"s_cmp_eq_u32 s6, s10\n" \
"s_cmov_b32 s4, s8\n" \
"s_cmov_b32 s7, s12\n" \
"s_cmp_eq_u32 s6, s14\n" \
"s_cmov_b32 s4, s12\n" \
"s_cmov_b32 s7, s16\n" \
"s_cmp_eq_u32 s6, s18\n" \
"s_cmov_b32 s4, s16\n" \
"s_cmov_b32 s7, s20\n" \
"s_cmp_eq_u32 s6, s22\n" \
"s_cmov_b32 s4, s20\n" \
"s_cmov_b32 s7, s24\n" \
"s_cmp_eq_u32 s6, s26\n" \
"s_cmov_b32 s4, s24\n" \
"s_cmov_b32 s7, s28\n" \
"s_cmp_eq_u32 s6, s30\n" \
"s_cmov_b32 s4, s28\n" \
"s_cmov_b32 s7, s5\n" \
"s_add_u32 s6, s6, 4\n" \
"s_lshl_b32 s6, s6, 16\n" \
"s_sub_u32 s5, s6, s66\n" \
"s_mov_b32 s66, s6\n" \
"s_mul_hi_i32 s5, s5, s58\n" \
"s_add_u32 s67, s5, s67\n" \
"s_sub_u32 s4, s4, s67\n" \
"s_sub_u32 s7, s7, s67\n" \
"s_sub_u32 s7, s7, 12\n" \
"s_max_i32 s4, s4, 0\n" \
"s_min_i32 s7, s7, s4\n" \
"s_add_u32 s67, s67, s7\n" \
"s_min_i32 s7, s7, 64\n" \
"s_max_i32 s7, s7, 0xffffffc0\n" \
"s_lshl_b32 s7, s7, 6\n" \
"s_add_u32 s58, s58, s7\n" \
"s_waitcnt lgkmcnt(0)\n" \
"v_subrev_u32 v56, s82, v56\n" \
"v_subrev_u32 v57, s82, v57\n" \
"v_subrev_u32 v58, s82, v58\n" \
"v_subrev_u32 v59, s82, v59\n" \
"v_max3_i32 v56, v56, v57, v58\n" \
"v_max_i32 v56, v56, v59\n" \
"v_mov_b32 v77, v77\n" \
"v_readfirstlane_b32 s84, v56\n" \
"s_min_i32 s84, s84, 0x1000000\n" \
"s_max_i32 s84, s84, 0xfc000000\n" \
"s_add_u32 s84, s84, s82\n" \
"s_add_u32 s83, s84, s69\n" \
"v_readfirstlane_b32 s10, v54\n" \
"s_sub_i32 s85, s83, s66\n" \
"s_mul_hi_i32 s85, s85, s58\n" \
"s_add_u32 s85, s85, s67\n" \
"s_sub_u32 s85, s85, s49\n" \
".Lh_w%=:\n" \
"s_memtime s[80:81]\n" \
"s_waitcnt lgkmcnt(0)\n" \
"s_sub_i32 s84, s85, s80\n" \
"s_cmp_gt_i32 s84, 256\n" \
"s_cbranch_scc1 .Lh_w%=\n" \
"s_max_i32 s84, s84, 0\n" \
"s_andn2_b32 s84, s84, 3\n" \
"s_sub_u32 s86, s90, s84\n" \
"s_subb_u32 s87, s91, 0\n" \
"s_setpc_b64 s[86:87]\n" \
".Lh_repoll%=:\n" \
"s_cmp_eq_u32 s73, 0\n" \
"s_cbranch_scc1 .Lh_bdone%=\n" \
"s_sub_u32 s73, s73, 1\n" \
"s_add_u32 s43, s43, 1\n" \
"global_load_dword v51, v50, s[60:61] sc1\n" \
"s_waitcnt vmcnt(0)\n" \
"s_branch .Lh_beval%=\n" \
".Lh_kvs%=:\n" \
"s_sub_u32 s93, s93, 1\n" \
"s_cbranch_scc1 .Lh_end%=\n" \
"global_load_dword v54, v55, s[56:57] sc1\n" \
"s_waitcnt vmcnt(0)\n" \
"s_branch .Lh_kvc%=\n" \
".Lh_rt0%=:\n" \
"s_cmp_eq_u32 s73, 0\n" \
"s_cbranch_scc1 .Lh_ct0%=\n" \
"s_sub_u32 s73, s73, 1\n" \
"s_add_u32 s41, s41, 1\n" \
"s_branch .Lh_ld0%=\n" \
".Lh_rt1%=:\n" \
"s_cmp_eq_u32 s73, 0\n" \
"s_cbranch_scc1 .Lh_ct1%=\n" \
"s_sub_u32 s73, s73, 1\n" \
"s_add_u32 s41, s41, 1\n" \
"s_branch .Lh_ld1%=\n" \
".Lh_rt2%=:\n" \
"s_cmp_eq_u32 s73, 0\n" \
"s_cbranch_scc1 .Lh_ct2%=\n" \
"s_sub_u32 s73, s73, 1\n" \
"s_add_u32 s41, s41, 1\n" \
"s_branch .Lh_ld2%=\n" \
".Lh_rt3%=:\n" \
"s_cmp_eq_u32 s73, 0\n" \
"s_cbranch_scc1 .Lh_ct3%=\n" \
"s_sub_u32 s73, s73, 1\n" \
"s_add_u32 s41, s41, 1\n" \
"s_branch .Lh_ld3%=\n" \
".Lh_rt4%=:\n" \
"s_cmp_eq_u32 s73, 0\n" \
"s_cbranch_scc1 .Lh_ct4%=\n" \
"s_sub_u32 s73, s73, 1\n" \
"s_add_u32 s41, s41, 1\n" \
"s_branch .Lh_ld4%=\n" \
".Lh_rt5%=:\n" \
"s_cmp_eq_u32 s73, 0\n" \
"s_cbranch_scc1 .Lh_ct5%=\n" \
"s_sub_u32 s73, s73, 1\n" \
"s_add_u32 s41, s41, 1\n" \
"s_branch .Lh_ld5%=\n" \
".Lh_rt6%=:\n" \
"s_cmp_eq_u32 s73, 0\n" \
"s_cbranch_scc1 .Lh_ct6%=\n" \
"s_sub_u32 s73, s73, 1\n" \
"s_add_u32 s41, s41, 1\n" \
"s_branch .Lh_ld6%=\n" \
".Lh_rt7%=:\n" \
"s_cmp_eq_u32 s73, 0\n" \
"s_cbranch_scc1 .Lh_ct7%=\n" \
"s_sub_u32 s73, s73, 1\n" \
"s_add_u32 s41, s41, 1\n" \
"s_branch .Lh_ld7%=\n" \
".Lh_ld0%=:\n" \
"global_load_dwordx4 v[0:3], v68, s[76:77] offset:-4096 sc1\n" \
"s_nop 3\n" \
".Lh_ld1%=:\n" \
"global_load_dwordx4 v[4:7], v68, s[76:77] offset:-3072 sc1\n" \
"s_nop 3\n" \
".Lh_ld2%=:\n" \
"global_load_dwordx4 v[8:11], v68, s[76:77] offset:-2048 sc1\n" \
"s_nop 3\n" \
".Lh_ld3%=:\n" \
"global_load_dwordx4 v[12:15], v68, s[76:77] offset:-1024 sc1\n" \
"s_nop 3\n" \
".Lh_ld4%=:\n" \
"global_load_dwordx4 v[16:19], v68, s[76:77] offset:0 sc1\n" \
"s_nop 3\n" \
".Lh_ld5%=:\n" \
"global_load_dwordx4 v[20:23], v68, s[76:77] offset:1024 sc1\n" \
"s_nop 3\n" \
".Lh_ld6%=:\n" \
"global_load_dwordx4 v[24:27], v68, s[76:77] offset:2048 sc1\n" \
"s_nop 3\n" \
".Lh_ld7%=:\n" \
".Lh_ld8%=:\n" \
"s_nop 7\n" \
"v_mov_b64 v[36:37], 0\n" \
"v_mov_b64 v[38:39], 0\n" \
"v_mov_b64 v[40:41], 0\n" \
"v_mov_b64 v[42:43], 0\n" \
"v_mov_b64 v[44:45], 0\n" \
"v_mov_b64 v[46:47], 0\n" \
"v_mov_b64 v[48:49], 0\n" \
"v_mov_b64 v[50:51], 0\n" \
"v_mov_b64 v[52:53], 0\n" \
"v_mov_b64 v[54:55], 0\n" \
"v_mov_b64 v[56:57], 0\n" \
"v_mov_b64 v[58:59], 0\n" \
"v_and_b32 v79, 0xe0, v71\n" \
"v_lshlrev_b32 v79, 9, v79\n" \
"v_writelane_b32 v79, s54, 63\n" \
"s_branch .Lh_qk%=\n" \
".Lh_slow%=:\n" \
"s_cmp_eq_u32 s73, 0\n" \
"s_cbranch_scc1 .Lh_resume%=\n" \
"v_max_u32 v78, v32, v33\n" \
"v_max3_u32 v78, v78, v34, v35\n" \
"v_cmp_eq_u32 s[84:85], -1, v78\n" \
"v_max_u32 v78, v0, v1\n" \
"v_max3_u32 v78, v78, v2, v3\n" \
"v_max3_u32 v78, v78, v4, v5\n" \
"v_max3_u32 v78, v78, v6, v7\n" \
"v_max3_u32 v78, v78, v8, v9\n" \
"v_max3_u32 v78, v78, v10, v11\n" \
"v_max3_u32 v78, v78, v12, v13\n" \
"v_max3_u32 v78, v78, v14, v15\n" \
"v_max3_u32 v78, v78, v16, v17\n" \
"v_max3_u32 v78, v78, v18, v19\n" \
"v_max3_u32 v78, v78, v20, v21\n" \
"v_max3_u32 v78, v78, v22, v23\n" \
"v_max3_u32 v78, v78, v24, v25\n" \
"v_max3_u32 v78, v78, v26, v27\n" \
"v_max3_u32 v78, v78, v28, v29\n" \
"v_max3_u32 v78, v78, v30, v31\n" \
"v_cmp_eq_u32 s[86:87], -1, v78\n" \
"v_mov_b32 v78, -1\n" \
"s_or_b64 s[84:85], s[84:85], s[86:87]\n" \
"s_cbranch_scc0 .Lh_resume%=\n" \
"s_sub_u32 s73, s73, 1\n" \
"s_add_u32 s41, s41, 1\n" \
"s_cmp_eq_u64 s[86:87], 0\n" \
"s_cbranch_scc1 .Lh_ld8%=\n" \
"s_branch .Lh_ld0%=\n" \
".Lh_oslow%=:\n" \
"s_cmp_eq_u32 s73, 0\n" \
"s_cbranch_scc1 .Lh_ocont%=\n" \
"v_max_u32 v45, v16, v17\n" \
"v_max3_u32 v45, v45, v18, v19\n" \
"v_max3_u32 v45, v45, v20, v21\n" \
"v_max3_u32 v45, v45, v22, v23\n" \
"v_cmp_eq_u32 vcc, -1, v45\n" \
"s_nop 4\n" \
"s_cbranch_vccz .Lh_ocont%=\n" \
"s_sub_u32 s73, s73, 1\n" \
"s_add_u32 s43, s43, 0x10000\n" \
"s_waitcnt lgkmcnt(0)\n" \
"ds_read_b128 v[24:27], v69 offset:0\n" \
"ds_read_b128 v[28:31], v69 offset:1024\n" \
"ds_read_b128 v[32:35], v69 offset:2048\n" \
"ds_read_b128 v[36:39], v69 offset:3072\n" \
"ds_read_b128 v[8:11], v69 offset:4096\n" \
"ds_read_b128 v[12:15], v69 offset:5120\n" \
"ds_read_b128 v[40:43], v69 offset:6144\n" \
"ds_read_b128 v[44:47], v69 offset:7168\n" \
"s_waitcnt lgkmcnt(0)\n" \
"ds_read_b128 v[16:19], v75\n" \
"ds_read_b128 v[20:23], v75 offset:1024\n" \
"v_mov_b64 v[4:5], 0\n" \
"v_mov_b64 v[6:7], 0\n" \
"v_mov_b64 v[0:1], 0\n" \
"v_mov_b64 v[2:3], 0\n" \
"s_waitcnt lgkmcnt(0)\n" \
"s_branch .Lh_wo%=\n" \
".Lh_last%=:\n" \
"global_store_dwordx4 v68, v[36:39], s[50:51] sc1\n" \
"v_smfmac_f32_16x16x64_bf16 v[56:59], v[0:3], v[184:191], v70\n" \
"v_smfmac_f32_16x16x64_bf16 v[52:55], v[4:7], v[120:127], v70\n" \
"v_smfmac_f32_16x16x64_bf16 v[56:59], v[4:7], v[192:199], v70\n" \
"v_smfmac_f32_16x16x64_bf16 v[52:55], v[8:11], v[128:135], v70\n" \
"v_smfmac_f32_16x16x64_bf16 v[56:59], v[8:11], v[200:207], v70\n" \
"v_smfmac_f32_16x16x64_bf16 v[52:55], v[12:15], v[136:143], v70\n" \
"v_smfmac_f32_16x16x64_bf16 v[56:59], v[12:15], v[208:215], v70\n" \
"v_smfmac_f32_16x16x64_bf16 v[52:55], v[16:19], v[144:151], v70\n" \
"v_smfmac_f32_16x16x64_bf16 v[56:59], v[16:19], v[216:223], v70\n" \
"v_smfmac_f32_16x16x64_bf16 v[52:55], v[20:23], v[152:159], v70\n" \
"v_smfmac_f32_16x16x64_bf16 v[56:59], v[20:23], v[224:231], v70\n" \
"v_smfmac_f32_16x16x64_bf16 v[52:55], v[24:27], v[160:167], v70\n" \
"v_smfmac_f32_16x16x64_bf16 v[56:59], v[24:27], v[232:239], v70\n" \
"v_smfmac_f32_16x16x64_bf16 v[52:55], v[28:31], v[168:175], v70\n" \
"v_smfmac_f32_16x16x64_bf16 v[56:59], v[28:31], v[240:247], v70\n" \
"v_smfmac_f32_16x16x64_bf16 v[52:55], v[32:35], v[176:183], v70\n" \
"v_smfmac_f32_16x16x64_bf16 v[56:59], v[32:35], v[248:255], v70\n" \
"v_mov_b32 v77, v77\n" \
"v_mov_b32 v77, v77\n" \
"v_pk_add_f32 v[52:53], v[52:53], v[54:55]\n" \
"v_mov_b32 v77, v77\n" \
"v_mov_b32 v77, v77\n" \
"v_mov_b32 v77, v77\n" \
"v_pk_add_f32 v[56:57], v[56:57], v[58:59]\n" \
"v_cvt_pk_bf16_f32 v52, v52, v53\n" \
"v_cvt_pk_bf16_f32 v53, v56, v57\n" \
"global_store_dword v71, v52, s[50:51] offset:1024 sc1\n" \
"global_store_dword v71, v53, s[50:51] offset:1280 sc1\n" \
".Lh_end%=:\n" \
"s_endpgm\n"

#define RED_ASM \
"s_setprio 3\n" \
"s_mov_b32 s72, 0\n" \
"s_mov_b32 s73, 1000000\n" \
"s_mov_b32 s60, 0\n" \
"s_mov_b32 s61, 0\n" \
"s_mov_b32 s77, s41\n" \
"s_mov_b32 s79, s41\n" \
"v_mov_b32 v44, -1\n" \
"v_mov_b32 v45, -1\n" \
"v_mov_b32 v46, -1\n" \
"v_mov_b32 v47, -1\n" \
"s_getpc_b64 s[90:91]\n" \
".Lr_pc%=:\n" \
"s_add_u32 s90, s90, .Lr_load%=-.Lr_pc%=\n" \
"s_addc_u32 s91, s91, 0\n" \
"s_getpc_b64 s[88:89]\n" \
".Lr_pd%=:\n" \
"s_add_u32 s88, s88, .Lr_poll%=-.Lr_pd%=\n" \
"s_addc_u32 s89, s89, 0\n" \
"s_add_u32 s84, s72, 1\n" \
"s_and_b32 s84, s84, 3\n" \
"s_lshl_b32 s85, s84, s46\n" \
"s_add_u32 s76, s44, s85\n" \
"s_xor_b32 s84, s84, 2\n" \
"s_lshl_b32 s85, s84, s46\n" \
"s_add_u32 s78, s44, s85\n" \
"s_branch .Lr_p0%=\n" \
"s_nop 0\n" \
"s_nop 0\n" \
"s_nop 0\n" \
"s_nop 0\n" \
"s_nop 0\n" \
"s_nop 0\n" \
"s_nop 0\n" \
"s_nop 0\n" \
"s_nop 0\n" \
"s_nop 0\n" \
"s_nop 0\n" \
"s_nop 0\n" \
"s_nop 0\n" \
"s_nop 0\n" \
"s_nop 0\n" \
"s_nop 0\n" \
"s_nop 0\n" \
"s_nop 0\n" \
"s_nop 0\n" \
"s_nop 0\n" \
"s_nop 0\n" \
"s_nop 0\n" \
"s_nop 0\n" \
"s_nop 0\n" \
"s_nop 0\n" \
"s_nop 0\n" \
"s_nop 0\n" \
"s_nop 0\n" \
"s_nop 0\n" \
"s_nop 0\n" \
"s_nop 0\n" \
"s_nop 0\n" \
"s_nop 0\n" \
"s_nop 0\n" \
"s_nop 0\n" \
"s_nop 0\n" \
"s_nop 0\n" \
"s_nop 0\n" \
"s_nop 0\n" \
"s_nop 0\n" \
"s_nop 0\n" \
"s_nop 0\n" \
"s_nop 0\n" \
"s_nop 0\n" \
"s_nop 0\n" \
"s_nop 0\n" \
"s_nop 0\n" \
"s_nop 0\n" \
"s_nop 0\n" \
"s_nop 0\n" \
"s_nop 0\n" \
"s_nop 0\n" \
"s_nop 0\n" \
"s_nop 0\n" \
"s_nop 0\n" \
"s_nop 0\n" \
"s_nop 0\n" \
"s_nop 0\n" \
"s_nop 0\n" \
"s_nop 0\n" \
"s_nop 0\n" \
"s_nop 0\n" \
"s_nop 0\n" \
"s_nop 0\n" \
".Lr_poll%=:\n" \
"global_load_dwordx4 v[24:27], v37, s[48:49] sc1\n" \
"s_nop 15\n" \
"s_nop 15\n" \
"s_nop 0\n" \
"s_memtime s[4:5]\n" \
"s_memrealtime s[6:7]\n" \
"s_nop 0\n" \
"s_nop 0\n" \
"s_memtime s[8:9]\n" \
"s_memrealtime s[10:11]\n" \
"s_nop 0\n" \
"s_nop 0\n" \
"s_memtime s[12:13]\n" \
"s_memrealtime s[14:15]\n" \
"s_nop 0\n" \
"s_nop 0\n" \
"s_memtime s[16:17]\n" \
"s_memrealtime s[18:19]\n" \
"s_nop 0\n" \
"s_nop 0\n" \
"s_memtime s[20:21]\n" \
"s_memrealtime s[22:23]\n" \
"s_nop 0\n" \
"s_nop 0\n" \
"s_memtime s[24:25]\n" \
"s_memrealtime s[26:27]\n" \
"s_nop 0\n" \
"s_nop 0\n" \
"s_memtime s[28:29]\n" \
"s_memrealtime s[30:31]\n" \
"s_waitcnt lgkmcnt(0)\n" \
"s_add_u32 s5, s28, 16\n" \
"s_mov_b32 s7, s8\n" \
"s_cmp_eq_u32 s6, s10\n" \
"s_cmov_b32 s4, s8\n" \
"s_cmov_b32 s7, s12\n" \
"s_cmp_eq_u32 s6, s14\n" \
"s_cmov_b32 s4, s12\n" \
"s_cmov_b32 s7, s16\n" \
"s_cmp_eq_u32 s6, s18\n" \
"s_cmov_b32 s4, s16\n" \
"s_cmov_b32 s7, s20\n" \
"s_cmp_eq_u32 s6, s22\n" \
"s_cmov_b32 s4, s20\n" \
"s_cmov_b32 s7, s24\n" \
"s_cmp_eq_u32 s6, s26\n" \
"s_cmov_b32 s4, s24\n" \
"s_cmov_b32 s7, s28\n" \
"s_cmp_eq_u32 s6, s30\n" \
"s_cmov_b32 s4, s28\n" \
"s_cmov_b32 s7, s5\n" \
"s_add_u32 s6, s6, 4\n" \
"s_lshl_b32 s6, s6, 16\n" \
"s_sub_u32 s5, s6, s66\n" \
"s_mov_b32 s66, s6\n" \
"s_mul_hi_i32 s5, s5, s58\n" \
"s_add_u32 s67, s5, s67\n" \
"s_sub_u32 s4, s4, s67\n" \
"s_sub_u32 s7, s7, s67\n" \
"s_sub_u32 s7, s7, 12\n" \
"s_max_i32 s4, s4, 0\n" \
"s_min_i32 s7, s7, s4\n" \
"s_add_u32 s67, s67, s7\n" \
"s_min_i32 s7, s7, 64\n" \
"s_max_i32 s7, s7, 0xffffffc0\n" \
"s_lshl_b32 s7, s7, 6\n" \
"s_add_u32 s58, s58, s7\n" \
"s_and_b32 s84, s72, 15\n" \
"s_or_b32 s82, s66, s84\n" \
"s_waitcnt vmcnt(0)\n" \
"v_subrev_u32 v24, s82, v24\n" \
"v_subrev_u32 v25, s82, v25\n" \
"v_subrev_u32 v26, s82, v26\n" \
"v_subrev_u32 v27, s82, v27\n" \
"v_or3_b32 v28, v24, v25, v26\n" \
"v_max3_i32 v24, v24, v25, v26\n" \
"v_or_b32 v28, v28, v27\n" \
"v_max_i32 v24, v24, v27\n" \
"s_nop 1\n" \
"v_max_i32_dpp v24, v24, v24 row_shr:1 row_mask:0xf bank_mask:0xf\n" \
"v_and_b32 v28, 14, v28\n" \
"s_nop 0\n" \
"v_max_i32_dpp v24, v24, v24 row_shr:2 row_mask:0xf bank_mask:0xf\n" \
"v_cmp_ne_u32 vcc, 0, v28\n" \
"s_nop 0\n" \
"v_max_i32_dpp v24, v24, v24 row_shr:4 row_mask:0xf bank_mask:0xf\n" \
"s_cbranch_vccnz .Lr_repoll%=\n" \
"s_nop 0\n" \
"v_max_i32_dpp v24, v24, v24 row_shr:8 row_mask:0xf bank_mask:0xf\n" \
".Lr_bdone%=:\n" \
"s_nop 0\n" \
"v_readlane_b32 s84, v24, 15\n" \
"v_readlane_b32 s85, v24, 31\n" \
"s_max_i32 s84, s84, s85\n" \
"s_min_i32 s84, s84, 0x1000000\n" \
"s_max_i32 s84, s84, 0xfc000000\n" \
"s_add_u32 s84, s84, s82\n" \
"s_add_u32 s83, s84, s47\n" \
"s_sub_i32 s85, s83, s66\n" \
"s_mul_hi_i32 s85, s85, s58\n" \
"s_add_u32 s85, s85, s67\n" \
"s_sub_u32 s85, s85, 100\n" \
".Lr_w%=:\n" \
"s_memtime s[80:81]\n" \
"s_waitcnt lgkmcnt(0)\n" \
"s_sub_i32 s84, s85, s80\n" \
"s_cmp_gt_i32 s84, 256\n" \
"s_cbranch_scc1 .Lr_w%=\n" \
"s_max_i32 s84, s84, 0\n" \
"s_andn2_b32 s84, s84, 3\n" \
"s_sub_u32 s86, s90, s84\n" \
"s_subb_u32 s87, s91, 0\n" \
"s_setpc_b64 s[86:87]\n" \
"s_nop 0\n" \
"s_nop 0\n" \
"s_nop 0\n" \
"s_nop 0\n" \
"s_nop 0\n" \
"s_nop 0\n" \
"s_nop 0\n" \
"s_nop 0\n" \
"s_nop 0\n" \
"s_nop 0\n" \
"s_nop 0\n" \
"s_nop 0\n" \
"s_nop 0\n" \
"s_nop 0\n" \
"s_nop 0\n" \
"s_nop 0\n" \
"s_nop 0\n" \
"s_nop 0\n" \
"s_nop 0\n" \
"s_nop 0\n" \
"s_nop 0\n" \
"s_nop 0\n" \
"s_nop 0\n" \
"s_nop 0\n" \
"s_nop 0\n" \
"s_nop 0\n" \
"s_nop 0\n" \
"s_nop 0\n" \
"s_nop 0\n" \
"s_nop 0\n" \
"s_nop 0\n" \
"s_nop 0\n" \
"s_nop 0\n" \
"s_nop 0\n" \
"s_nop 0\n" \
"s_nop 0\n" \
"s_nop 0\n" \
"s_nop 0\n" \
"s_nop 0\n" \
"s_nop 0\n" \
"s_nop 0\n" \
"s_nop 0\n" \
"s_nop 0\n" \
"s_nop 0\n" \
"s_nop 0\n" \
"s_nop 0\n" \
"s_nop 0\n" \
"s_nop 0\n" \
"s_nop 0\n" \
"s_nop 0\n" \
"s_nop 0\n" \
"s_nop 0\n" \
"s_nop 0\n" \
"s_nop 0\n" \
"s_nop 0\n" \
"s_nop 0\n" \
"s_nop 0\n" \
"s_nop 0\n" \
"s_nop 0\n" \
"s_nop 0\n" \
"s_nop 0\n" \
"s_nop 0\n" \
"s_nop 0\n" \
"s_nop 0\n" \
".Lr_load%=:\n" \
"global_load_dwordx4 v[0:3], v32, s[40:41] sc1\n" \
"global_load_dwordx4 v[4:7], v33, s[40:41] sc1\n" \
"global_load_dwordx4 v[8:11], v34, s[40:41] sc1\n" \
"global_load_dwordx4 v[12:15], v35, s[40:41] sc1\n" \
"s_waitcnt vmcnt(3)\n" \
"v_mfma_f32_16x16x32_bf16 v[16:19], v[40:43], v[0:3], 0\n" \
"s_waitcnt vmcnt(2)\n" \
"v_mfma_f32_16x16x32_bf16 v[16:19], v[40:43], v[4:7], v[16:19]\n" \
"s_waitcnt vmcnt(1)\n" \
"v_mfma_f32_16x16x32_bf16 v[16:19], v[40:43], v[8:11], v[16:19]\n" \
"s_waitcnt vmcnt(0)\n" \
"v_mfma_f32_16x16x32_bf16 v[16:19], v[40:43], v[12:15], v[16:19]\n" \
"s_nop 7\n" \
"v_cmp_u_f32 vcc, v16, v16\n" \
"v_add_f32_dpp v20, v16, v16 row_ror:8 row_mask:0xf bank_mask:0xf\n" \
"v_add_f32_dpp v21, v17, v17 row_ror:8 row_mask:0xf bank_mask:0xf\n" \
"v_add_f32_dpp v22, v18, v18 row_ror:8 row_mask:0xf bank_mask:0xf\n" \
"v_add_f32_dpp v23, v19, v19 row_ror:8 row_mask:0xf bank_mask:0xf\n" \
"v_cvt_pk_bf16_f32 v16, v20, v21\n" \
"v_cvt_pk_bf16_f32 v17, v22, v23\n" \
"s_cbranch_vccnz .Lr_slow%=\n" \
".Lr_resume%=:\n" \
"global_store_dwordx2 v36, v[16:17], s[76:77] sc1\n" \
"s_add_u32 s72, s72, 1\n" \
"s_cmp_eq_u32 s72, 63\n" \
"s_cbranch_scc1 .Lr_end%=\n" \
"s_add_u32 s83, s83, s52\n" \
"s_sub_i32 s85, s83, s66\n" \
"s_mul_hi_i32 s85, s85, s58\n" \
"s_add_u32 s85, s85, s67\n" \
"s_sub_u32 s85, s85, 100\n" \
".Lr_c%=:\n" \
"s_memtime s[80:81]\n" \
"s_waitcnt lgkmcnt(0)\n" \
"s_sub_i32 s84, s85, s80\n" \
"s_cmp_gt_i32 s84, 0\n" \
"s_cbranch_scc1 .Lr_c%=\n" \
"global_store_dwordx2 v36, v[44:45], s[78:79] sc1\n" \
"global_store_dwordx4 v32, v[44:47], s[40:41] sc1\n" \
"global_store_dwordx4 v33, v[44:47], s[40:41] sc1\n" \
"global_store_dwordx4 v34, v[44:47], s[40:41] sc1\n" \
"global_store_dwordx4 v35, v[44:47], s[40:41] sc1\n" \
"s_sub_u32 s40, s42, s40\n" \
"s_xor_b32 s48, s48, 0x10000\n" \
"s_add_u32 s83, s83, s53\n" \
"s_add_u32 s84, s72, 1\n" \
"s_and_b32 s84, s84, 3\n" \
"s_lshl_b32 s85, s84, s46\n" \
"s_add_u32 s76, s44, s85\n" \
"s_xor_b32 s84, s84, 2\n" \
"s_lshl_b32 s85, s84, s46\n" \
"s_add_u32 s78, s44, s85\n" \
"s_sub_i32 s85, s83, s66\n" \
"s_mul_hi_i32 s85, s85, s58\n" \
"s_add_u32 s85, s85, s67\n" \
"s_sub_u32 s85, s85, 100\n" \
".Lrb_w%=:\n" \
"s_memtime s[80:81]\n" \
"s_waitcnt lgkmcnt(0)\n" \
"s_sub_i32 s84, s85, s80\n" \
"s_cmp_gt_i32 s84, 256\n" \
"s_cbranch_scc1 .Lrb_w%=\n" \
"s_max_i32 s84, s84, 0\n" \
"s_andn2_b32 s84, s84, 3\n" \
"s_sub_u32 s86, s88, s84\n" \
"s_subb_u32 s87, s89, 0\n" \
"s_setpc_b64 s[86:87]\n" \
".Lr_repoll%=:\n" \
"s_cmp_eq_u32 s73, 0\n" \
"s_cbranch_scc1 .Lr_bdone%=\n" \
"s_sub_u32 s73, s73, 1\n" \
"s_add_u32 s61, s61, 1\n" \
"s_cmp_lg_u32 s72, 0\n" \
"s_cbranch_scc1 .Lr_poll%=\n" \
".Lr_p0%=:\n" \
"s_memtime s[4:5]\n" \
"s_memrealtime s[6:7]\n" \
"s_mov_b32 s58, 1533542\n" \
"s_waitcnt lgkmcnt(0)\n" \
"s_mov_b32 s67, s4\n" \
"s_lshl_b32 s66, s6, 16\n" \
"s_sub_u32 s83, s66, 0x2000000\n" \
"s_branch .Lr_poll%=\n" \
".Lr_slow%=:\n" \
"s_cmp_eq_u32 s73, 0\n" \
"s_cbranch_scc1 .Lr_resume%=\n" \
"s_sub_u32 s73, s73, 1\n" \
"s_add_u32 s60, s60, 1\n" \
"global_load_dwordx4 v[24:27], v37, s[48:49] sc1\n" \
"global_load_dwordx4 v[0:3], v32, s[40:41] sc1\n" \
"global_load_dwordx4 v[4:7], v33, s[40:41] sc1\n" \
"global_load_dwordx4 v[8:11], v34, s[40:41] sc1\n" \
"global_load_dwordx4 v[12:15], v35, s[40:41] sc1\n" \
"s_waitcnt vmcnt(4)\n" \
"v_subrev_u32 v24, s82, v24\n" \
"v_subrev_u32 v25, s82, v25\n" \
"v_subrev_u32 v26, s82, v26\n" \
"v_subrev_u32 v27, s82, v27\n" \
"v_or3_b32 v28, v24, v25, v26\n" \
"v_max3_i32 v24, v24, v25, v26\n" \
"v_or_b32 v28, v28, v27\n" \
"v_max_i32 v24, v24, v27\n" \
"s_nop 1\n" \
"v_max_i32_dpp v24, v24, v24 row_shr:1 row_mask:0xf bank_mask:0xf\n" \
"v_and_b32 v28, 15, v28\n" \
"s_nop 0\n" \
"v_max_i32_dpp v24, v24, v24 row_shr:2 row_mask:0xf bank_mask:0xf\n" \
"v_cmp_ne_u32 vcc, 0, v28\n" \
"s_nop 0\n" \
"v_max_i32_dpp v24, v24, v24 row_shr:4 row_mask:0xf bank_mask:0xf\n" \
"s_cbranch_vccnz .Lr_k4%=\n" \
"s_nop 0\n" \
"v_max_i32_dpp v24, v24, v24 row_shr:8 row_mask:0xf bank_mask:0xf\n" \
"s_nop 0\n" \
"v_readlane_b32 s84, v24, 15\n" \
"v_readlane_b32 s85, v24, 31\n" \
"s_max_i32 s84, s84, s85\n" \
"s_min_i32 s84, s84, 0x1000000\n" \
"s_max_i32 s84, s84, 0xfc000000\n" \
"s_add_u32 s84, s84, s82\n" \
"s_add_u32 s83, s84, s47\n" \
".Lr_k4%=:\n" \
"s_waitcnt vmcnt(3)\n" \
"v_mfma_f32_16x16x32_bf16 v[16:19], v[40:43], v[0:3], 0\n" \
"s_waitcnt vmcnt(2)\n" \
"v_mfma_f32_16x16x32_bf16 v[16:19], v[40:43], v[4:7], v[16:19]\n" \
"s_waitcnt vmcnt(1)\n" \
"v_mfma_f32_16x16x32_bf16 v[16:19], v[40:43], v[8:11], v[16:19]\n" \
"s_waitcnt vmcnt(0)\n" \
"v_mfma_f32_16x16x32_bf16 v[16:19], v[40:43], v[12:15], v[16:19]\n" \
"s_nop 7\n" \
"v_cmp_u_f32 vcc, v16, v16\n" \
"v_add_f32_dpp v20, v16, v16 row_ror:8 row_mask:0xf bank_mask:0xf\n" \
"v_add_f32_dpp v21, v17, v17 row_ror:8 row_mask:0xf bank_mask:0xf\n" \
"v_add_f32_dpp v22, v18, v18 row_ror:8 row_mask:0xf bank_mask:0xf\n" \
"v_add_f32_dpp v23, v19, v19 row_ror:8 row_mask:0xf bank_mask:0xf\n" \
"v_cvt_pk_bf16_f32 v16, v20, v21\n" \
"v_cvt_pk_bf16_f32 v17, v22, v23\n" \
"s_cbranch_vccz .Lr_resume%=\n" \
"s_sub_i32 s85, s83, s66\n" \
"s_mul_hi_i32 s85, s85, s58\n" \
"s_add_u32 s85, s85, s67\n" \
"s_sub_u32 s85, s85, 100\n" \
".Lrs_w%=:\n" \
"s_memtime s[80:81]\n" \
"s_waitcnt lgkmcnt(0)\n" \
"s_sub_i32 s84, s85, s80\n" \
"s_cmp_gt_i32 s84, 256\n" \
"s_cbranch_scc1 .Lrs_w%=\n" \
"s_max_i32 s84, s84, 0\n" \
"s_andn2_b32 s84, s84, 3\n" \
"s_sub_u32 s86, s90, s84\n" \
"s_subb_u32 s87, s91, 0\n" \
"s_setpc_b64 s[86:87]\n" \
".Lr_end%=:\n" \
"s_endpgm\n"
#define Q_CLOB "a0", "a1", "a2", "a3", "a4", "a5", "a6", "a7", "a8", "a9", "a10", "a11", "a12", "a13", "a14", "a15", "a16", "a17", "a18", "a19", "a20", "a21", "a22", "a23", "a24", "a25", "a26", "a27", "a28", "a29", "a30", "a31", "a32", "a33", "a34", "a35", "a36", "a37", "a38", "a39", "a40", "a41", "a42", "a43", "a44", "a45", "a46", "a47", "a48", "a49", "a50", "a51", "a52", "a53", "a54", "a55", "a56", "a57", "a58", "a59", "a60", "a61", "a62", "a63", "a64", "a65", "a66", "a67", "a68", "a69", "a70", "a71", "a72", "a73", "a74", "a75", "a76", "a77", "a78", "a79", "a80", "a81", "a82", "a83", "a84", "a85", "a86", "a87", "a88", "a89", "a90", "a91", "a92", "a93", "a94", "a95", "a96", "a97", "a98", "a99", "a100", "a101", "a102", "a103", "a104", "a105", "a106", "a107", "a108", "a109", "a110", "a111", "a112", "a113", "a114", "a115", "a116", "a117", "a118", "a119", "a120", "a121", "a122", "a123", "a124", "a125", "a126", "a127", "a128", "a129", "a130", "a131", "a132", "a133", "a134", "a135", "a136", "a137", "a138", "a139", "a140", "a141", "a142", "a143", "a144", "a145", "a146", "a147", "a148", "a149", "a150", "a151", "a152", "a153", "a154", "a155", "a156", "a157", "a158", "a159", "a160", "a161", "a162", "a163", "a164", "a165", "a166", "a167", "a168", "a169", "a170", "a171", "a172", "a173", "a174", "a175", "a176", "a177", "a178", "a179", "a180", "a181", "a182", "a183", "a184", "a185", "a186", "a187", "a188", "a189", "a190", "a191", "a192", "a193", "a194", "a195", "a196", "a197", "a198", "a199", "a200", "a201", "a202", "a203", "a204", "a205", "a206", "a207", "a208", "a209", "a210", "a211", "a212", "a213", "a214", "a215", "a216", "a217", "a218", "a219", "a220", "a221", "a222", "a223", "a224", "a225", "a226", "a227", "a228", "a229", "a230", "a231", "a232", "a233", "a234", "a235", "a236", "a237", "a238", "a239", "a240", "a241", "a242", "a243", "a244", "a245", "a246", "a247", "a248", "a249", "a250", "a251", "a252", "a253", "a254", "a255", "s73", "s75", "s76", "s77", "s78", "s79", "v0", "v1", "v2", "v3", "v4", "v5", "v6", "v7", "v8", "v9", "v10", "v11", "v12", "v13", "v14", "v15", "v16", "v17", "v18", "v19", "v20", "v21", "v22", "v23", "v24", "v25", "v26", "v27", "v28", "v29", "v30", "v31", "v32", "v33", "v34", "v35", "v36", "v37", "v38", "v39", "v40", "v41", "v42", "v43", "v44", "v45", "v46", "v47", "v48", "v49", "v50", "v51", "v52", "v53", "v54", "v55", "v56", "v57", "v58", "v59", "v60", "v61", "v62", "v63", "v64", "v65", "v66", "v67", "v68", "v69", "v70", "v71", "v72", "v73", "v74", "v75", "v76", "v77", "v78", "v79", "v80", "v112", "v113", "v114", "v115", "v116", "v117", "v118", "v119", "v120", "v121", "v122", "v123", "v124", "v125", "v126", "v127", "v128", "v129", "v130", "v131", "v132", "v133", "v134", "v135", "v136", "v137", "v138", "v139", "v140", "v141", "v142", "v143", "v144", "v145", "v146", "v147", "v148", "v149", "v150", "v151", "v152", "v224", "v225", "v226", "v227", "v228", "v229", "v230", "v231", "v232", "v233", "v234", "v235", "v236", "v237", "v238", "v239", "v240", "v241", "v242", "v243", "v244", "v245", "v246", "v247", "v248", "v249", "v250", "v251", "v252", "v253", "v254", "v255"
#define H_CLOB "a0", "a1", "a2", "a3", "a4", "a5", "a6", "a7", "a8", "a9", "a10", "a11", "a12", "a13", "a14", "a15", "a16", "a17", "a18", "a19", "a20", "a21", "a22", "a23", "a24", "a25", "a26", "a27", "a28", "a29", "a30", "a31", "a32", "a33", "a34", "a35", "a36", "a37", "a38", "a39", "a40", "a41", "a42", "a43", "a44", "a45", "a46", "a47", "a48", "a49", "a50", "a51", "a52", "a53", "a54", "a55", "a56", "a57", "a58", "a59", "a60", "a61", "a62", "a63", "a64", "a65", "a66", "a67", "a68", "a69", "a70", "a71", "a72", "a73", "a74", "a75", "a76", "a77", "a78", "a79", "a80", "a81", "a82", "a83", "a84", "a85", "a86", "a87", "a88", "a89", "a90", "a91", "a92", "a93", "a94", "a95", "a96", "a97", "a98", "a99", "a100", "a101", "a102", "a103", "a104", "a105", "a106", "a107", "a108", "a109", "a110", "a111", "a112", "a113", "a114", "a115", "a116", "a117", "a118", "a119", "a120", "a121", "a122", "a123", "a124", "a125", "a126", "a127", "a128", "a129", "a130", "a131", "a132", "a133", "a134", "a135", "a136", "a137", "a138", "a139", "a140", "a141", "a142", "a143", "a144", "a145", "a146", "a147", "a148", "a149", "a150", "a151", "a152", "a153", "a154", "a155", "a156", "a157", "a158", "a159", "a160", "a161", "a162", "a163", "a164", "a165", "a166", "a167", "a168", "a169", "a170", "a171", "a172", "a173", "a174", "a175", "a176", "a177", "a178", "a179", "a180", "a181", "a182", "a183", "a184", "a185", "a186", "a187", "a188", "a189", "a190", "a191", "a192", "a193", "a194", "a195", "a196", "a197", "a198", "a199", "a200", "a201", "a202", "a203", "a204", "a205", "a206", "a207", "a208", "a209", "a210", "a211", "a212", "a213", "a214", "a215", "a216", "a217", "a218", "a219", "a220", "a221", "a222", "a223", "a224", "a225", "a226", "a227", "a228", "a229", "a230", "a231", "a232", "a233", "a234", "a235", "a236", "a237", "a238", "a239", "a240", "a241", "a242", "a243", "a244", "a245", "a246", "a247", "a248", "a249", "a250", "a251", "a252", "a253", "a254", "a255", "s73", "s75", "s76", "s77", "s78", "s79", "v0", "v1", "v2", "v3", "v4", "v5", "v6", "v7", "v8", "v9", "v10", "v11", "v12", "v13", "v14", "v15", "v16", "v17", "v18", "v19", "v20", "v21", "v22", "v23", "v24", "v25", "v26", "v27", "v28", "v29", "v30", "v31", "v32", "v33", "v34", "v35", "v36", "v37", "v38", "v39", "v40", "v41", "v42", "v43", "v44", "v45", "v46", "v47", "v48", "v49", "v50", "v51", "v52", "v53", "v54", "v55", "v56", "v57", "v58", "v59", "v60", "v61", "v62", "v63", "v64", "v65", "v66", "v67", "v68", "v69", "v70", "v71", "v72", "v73", "v74", "v75", "v76", "v77", "v78", "v79", "v80", "v81", "v82", "v83", "v84", "v85", "v86", "v87", "v112", "v113", "v114", "v115", "v116", "v117", "v118", "v119", "v120", "v121", "v122", "v123", "v124", "v125", "v126", "v127", "v128", "v129", "v130", "v131", "v132", "v133", "v134", "v135", "v136", "v137", "v138", "v139", "v140", "v141", "v142", "v143", "v144", "v145", "v146", "v147", "v148", "v149", "v150", "v151", "v152", "v224", "v225", "v226", "v227", "v228", "v229", "v230", "v231", "v232", "v233", "v234", "v235", "v236", "v237", "v238", "v239", "v240", "v241", "v242", "v243", "v244", "v245", "v246", "v247", "v248", "v249", "v250", "v251", "v252", "v253", "v254", "v255"
#define KV_ASM \
"s_mov_b32 s73, 2000000\n" \
"s_mov_b32 s75, 0xbfb8aa3b\n" \
"v_mov_b32 v108, v100\n" \
"v_add_u32 v109, 0x10000, v100\n" \
"v_add_u32 v110, 0x20000, v100\n" \
"v_min_u32 v66, 68, v107\n" \
"v_add_u32 v66, 10368, v66\n" \
"v_mov_b32 v150, s65\n" \
"global_load_dword v151, v150, s[54:55] sc1\n" \
"s_memrealtime s[76:77]\n" \
"s_waitcnt lgkmcnt(0)\n" \
".Ldly%=:\n" \
"s_memrealtime s[78:79]\n" \
"s_waitcnt lgkmcnt(0)\n" \
"s_sub_u32 s78, s78, s76\n" \
"s_cmp_lt_u32 s78, s64\n" \
"s_cbranch_scc1 .Ldly%=\n" \
"s_add_u32 m0, s56, 0\n" \
"s_nop 0\n" \
"global_load_lds_dwordx4 v96, s[40:41] offset:0\n" \
"global_load_lds_dwordx4 v97, s[40:41] offset:1024\n" \
"global_load_lds_dwordx4 v98, s[40:41] offset:2048\n" \
"global_load_lds_dwordx4 v99, s[40:41] offset:3072\n" \
"s_add_u32 s40, s40, s44\n" \
"s_addc_u32 s41, s41, 0\n" \
"s_add_u32 m0, s56, 16384\n" \
"s_nop 0\n" \
"global_load_lds_dwordx4 v96, s[40:41] offset:0\n" \
"global_load_lds_dwordx4 v97, s[40:41] offset:1024\n" \
"global_load_lds_dwordx4 v98, s[40:41] offset:2048\n" \
"global_load_lds_dwordx4 v99, s[40:41] offset:3072\n" \
"s_add_u32 s40, s40, s44\n" \
"s_addc_u32 s41, s41, 0\n" \
"s_add_u32 m0, s56, 32768\n" \
"s_nop 0\n" \
"global_load_lds_dwordx4 v96, s[40:41] offset:0\n" \
"global_load_lds_dwordx4 v97, s[40:41] offset:1024\n" \
"global_load_lds_dwordx4 v98, s[40:41] offset:2048\n" \
"global_load_lds_dwordx4 v99, s[40:41] offset:3072\n" \
"s_add_u32 s40, s40, s44\n" \
"s_addc_u32 s41, s41, 0\n" \
"s_add_u32 m0, s56, 49152\n" \
"s_nop 0\n" \
"global_load_lds_dwordx4 v96, s[40:41] offset:0\n" \
"global_load_lds_dwordx4 v97, s[40:41] offset:1024\n" \
"global_load_lds_dwordx4 v98, s[40:41] offset:2048\n" \
"global_load_lds_dwordx4 v99, s[40:41] offset:3072\n" \
"s_add_u32 s40, s40, s44\n" \
"s_addc_u32 s41, s41, 0\n" \
"s_add_u32 m0, s56, 65536\n" \
"s_nop 0\n" \
"global_load_lds_dwordx4 v96, s[40:41] offset:0\n" \
"global_load_lds_dwordx4 v97, s[40:41] offset:1024\n" \
"global_load_lds_dwordx4 v98, s[40:41] offset:2048\n" \
"global_load_lds_dwordx4 v99, s[40:41] offset:3072\n" \
"s_add_u32 s40, s40, s44\n" \
"s_addc_u32 s41, s41, 0\n" \
"s_add_u32 m0, s56, 81920\n" \
"s_nop 0\n" \
"global_load_lds_dwordx4 v96, s[40:41] offset:0\n" \
"global_load_lds_dwordx4 v97, s[40:41] offset:1024\n" \
"global_load_lds_dwordx4 v98, s[40:41] offset:2048\n" \
"global_load_lds_dwordx4 v99, s[40:41] offset:3072\n" \
"s_add_u32 s40, s40, s44\n" \
"s_addc_u32 s41, s41, 0\n" \
"s_add_u32 m0, s56, 98304\n" \
"s_nop 0\n" \
"global_load_lds_dwordx4 v96, s[40:41] offset:0\n" \
"global_load_lds_dwordx4 v97, s[40:41] offset:1024\n" \
"global_load_lds_dwordx4 v98, s[40:41] offset:2048\n" \
"global_load_lds_dwordx4 v99, s[40:41] offset:3072\n" \
"s_add_u32 s40, s40, s44\n" \
"s_addc_u32 s41, s41, 0\n" \
"s_add_u32 m0, s56, 114688\n" \
"s_nop 0\n" \
"global_load_lds_dwordx4 v96, s[40:41] offset:0\n" \
"global_load_lds_dwordx4 v97, s[40:41] offset:1024\n" \
"global_load_lds_dwordx4 v98, s[40:41] offset:2048\n" \
"global_load_lds_dwordx4 v99, s[40:41] offset:3072\n" \
"s_add_u32 s40, s40, s44\n" \
"s_addc_u32 s41, s41, 0\n" \
"s_add_u32 m0, s56, 131072\n" \
"s_nop 0\n" \
"global_load_lds_dwordx4 v96, s[40:41] offset:0\n" \
"global_load_lds_dwordx4 v97, s[40:41] offset:1024\n" \
"global_load_lds_dwordx4 v98, s[40:41] offset:2048\n" \
"global_load_lds_dwordx4 v99, s[40:41] offset:3072\n" \
"s_add_u32 s40, s40, s44\n" \
"s_addc_u32 s41, s41, 0\n" \
"s_waitcnt vmcnt(32)\n" \
"s_barrier\n" \
"ds_read_b64_tr_b16 a[0:1], v108 offset:0\n" \
"ds_read_b64_tr_b16 a[2:3], v108 offset:1024\n" \
"ds_read_b64_tr_b16 a[4:5], v108 offset:4096\n" \
"ds_read_b64_tr_b16 a[6:7], v108 offset:5120\n" \
"ds_read_b64_tr_b16 a[8:9], v108 offset:8192\n" \
"ds_read_b64_tr_b16 a[10:11], v108 offset:9216\n" \
"ds_read_b64_tr_b16 a[12:13], v108 offset:12288\n" \
"ds_read_b64_tr_b16 a[14:15], v108 offset:13312\n" \
"s_waitcnt lgkmcnt(0)\n" \
"s_waitcnt vmcnt(28)\n" \
"s_barrier\n" \
"s_add_u32 m0, s56, 0\n" \
"s_nop 0\n" \
"global_load_lds_dwordx4 v96, s[42:43] offset:0\n" \
"global_load_lds_dwordx4 v97, s[42:43] offset:1024\n" \
"global_load_lds_dwordx4 v98, s[42:43] offset:2048\n" \
"global_load_lds_dwordx4 v99, s[42:43] offset:3072\n" \
"s_add_u32 s42, s42, s44\n" \
"s_addc_u32 s43, s43, 0\n" \
"ds_read_b64_tr_b16 a[16:17], v108 offset:16384\n" \
"ds_read_b64_tr_b16 a[18:19], v108 offset:17408\n" \
"ds_read_b64_tr_b16 a[20:21], v108 offset:20480\n" \
"ds_read_b64_tr_b16 a[22:23], v108 offset:21504\n" \
"ds_read_b64_tr_b16 a[24:25], v108 offset:24576\n" \
"ds_read_b64_tr_b16 a[26:27], v108 offset:25600\n" \
"ds_read_b64_tr_b16 a[28:29], v108 offset:28672\n" \
"ds_read_b64_tr_b16 a[30:31], v108 offset:29696\n" \
"s_waitcnt lgkmcnt(0)\n" \
"s_waitcnt vmcnt(28)\n" \
"s_barrier\n" \
"s_add_u32 m0, s56, 16384\n" \
"s_nop 0\n" \
"global_load_lds_dwordx4 v96, s[42:43] offset:0\n" \
"global_load_lds_dwordx4 v97, s[42:43] offset:1024\n" \
"global_load_lds_dwordx4 v98, s[42:43] offset:2048\n" \
"global_load_lds_dwordx4 v99, s[42:43] offset:3072\n" \
"s_add_u32 s42, s42, s44\n" \
"s_addc_u32 s43, s43, 0\n" \
"ds_read_b64_tr_b16 a[32:33], v108 offset:32768\n" \
"ds_read_b64_tr_b16 a[34:35], v108 offset:33792\n" \
"ds_read_b64_tr_b16 a[36:37], v108 offset:36864\n" \
"ds_read_b64_tr_b16 a[38:39], v108 offset:37888\n" \
"ds_read_b64_tr_b16 a[40:41], v108 offset:40960\n" \
"ds_read_b64_tr_b16 a[42:43], v108 offset:41984\n" \
"ds_read_b64_tr_b16 a[44:45], v108 offset:45056\n" \
"ds_read_b64_tr_b16 a[46:47], v108 offset:46080\n" \
"s_waitcnt lgkmcnt(0)\n" \
"s_waitcnt vmcnt(28)\n" \
"s_barrier\n" \
"s_add_u32 m0, s56, 32768\n" \
"s_nop 0\n" \
"global_load_lds_dwordx4 v96, s[42:43] offset:0\n" \
"global_load_lds_dwordx4 v97, s[42:43] offset:1024\n" \
"global_load_lds_dwordx4 v98, s[42:43] offset:2048\n" \
"global_load_lds_dwordx4 v99, s[42:43] offset:3072\n" \
"s_add_u32 s42, s42, s44\n" \
"s_addc_u32 s43, s43, 0\n" \
"ds_read_b64_tr_b16 a[48:49], v108 offset:49152\n" \
"ds_read_b64_tr_b16 a[50:51], v108 offset:50176\n" \
"ds_read_b64_tr_b16 a[52:53], v108 offset:53248\n" \
"ds_read_b64_tr_b16 a[54:55], v108 offset:54272\n" \
"ds_read_b64_tr_b16 a[56:57], v108 offset:57344\n" \
"ds_read_b64_tr_b16 a[58:59], v108 offset:58368\n" \
"ds_read_b64_tr_b16 a[60:61], v108 offset:61440\n" \
"ds_read_b64_tr_b16 a[62:63], v108 offset:62464\n" \
"s_waitcnt lgkmcnt(0)\n" \
"s_waitcnt vmcnt(28)\n" \
"s_barrier\n" \
"s_add_u32 m0, s56, 49152\n" \
"s_nop 0\n" \
"global_load_lds_dwordx4 v96, s[42:43] offset:0\n" \
"global_load_lds_dwordx4 v97, s[42:43] offset:1024\n" \
"global_load_lds_dwordx4 v98, s[42:43] offset:2048\n" \
"global_load_lds_dwordx4 v99, s[42:43] offset:3072\n" \
"s_add_u32 s42, s42, s44\n" \
"s_addc_u32 s43, s43, 0\n" \
"ds_read_b64_tr_b16 a[64:65], v109 offset:0\n" \
"ds_read_b64_tr_b16 a[66:67], v109 offset:1024\n" \
"ds_read_b64_tr_b16 a[68:69], v109 offset:4096\n" \
"ds_read_b64_tr_b16 a[70:71], v109 offset:5120\n" \
"ds_read_b64_tr_b16 a[72:73], v109 offset:8192\n" \
"ds_read_b64_tr_b16 a[74:75], v109 offset:9216\n" \
"ds_read_b64_tr_b16 a[76:77], v109 offset:12288\n" \
"ds_read_b64_tr_b16 a[78:79], v109 offset:13312\n" \
"s_waitcnt lgkmcnt(0)\n" \
"s_waitcnt vmcnt(28)\n" \
"s_barrier\n" \
"s_add_u32 m0, s56, 65536\n" \
"s_nop 0\n" \
"global_load_lds_dwordx4 v96, s[42:43] offset:0\n" \
"global_load_lds_dwordx4 v97, s[42:43] offset:1024\n" \
"global_load_lds_dwordx4 v98, s[42:43] offset:2048\n" \
"global_load_lds_dwordx4 v99, s[42:43] offset:3072\n" \
"s_add_u32 s42, s42, s44\n" \
"s_addc_u32 s43, s43, 0\n" \
"ds_read_b64_tr_b16 a[80:81], v109 offset:16384\n" \
"ds_read_b64_tr_b16 a[82:83], v109 offset:17408\n" \
"ds_read_b64_tr_b16 a[84:85], v109 offset:20480\n" \
"ds_read_b64_tr_b16 a[86:87], v109 offset:21504\n" \
"ds_read_b64_tr_b16 a[88:89], v109 offset:24576\n" \
"ds_read_b64_tr_b16 a[90:91], v109 offset:25600\n" \
"ds_read_b64_tr_b16 a[92:93], v109 offset:28672\n" \
"ds_read_b64_tr_b16 a[94:95], v109 offset:29696\n" \
"s_waitcnt lgkmcnt(0)\n" \
"s_waitcnt vmcnt(28)\n" \
"s_barrier\n" \
"s_add_u32 m0, s56, 81920\n" \
"s_nop 0\n" \
"global_load_lds_dwordx4 v96, s[42:43] offset:0\n" \
"global_load_lds_dwordx4 v97, s[42:43] offset:1024\n" \
"global_load_lds_dwordx4 v98, s[42:43] offset:2048\n" \
"global_load_lds_dwordx4 v99, s[42:43] offset:3072\n" \
"s_add_u32 s42, s42, s44\n" \
"s_addc_u32 s43, s43, 0\n" \
"ds_read_b64_tr_b16 a[96:97], v109 offset:32768\n" \
"ds_read_b64_tr_b16 a[98:99], v109 offset:33792\n" \
"ds_read_b64_tr_b16 a[100:101], v109 offset:36864\n" \
"ds_read_b64_tr_b16 a[102:103], v109 offset:37888\n" \
"ds_read_b64_tr_b16 a[104:105], v109 offset:40960\n" \
"ds_read_b64_tr_b16 a[106:107], v109 offset:41984\n" \
"ds_read_b64_tr_b16 a[108:109], v109 offset:45056\n" \
"ds_read_b64_tr_b16 a[110:111], v109 offset:46080\n" \
"s_waitcnt lgkmcnt(0)\n" \
"s_waitcnt vmcnt(28)\n" \
"s_barrier\n" \
"s_add_u32 m0, s56, 98304\n" \
"s_nop 0\n" \
"global_load_lds_dwordx4 v96, s[42:43] offset:0\n" \
"global_load_lds_dwordx4 v97, s[42:43] offset:1024\n" \
"global_load_lds_dwordx4 v98, s[42:43] offset:2048\n" \
"global_load_lds_dwordx4 v99, s[42:43] offset:3072\n" \
"s_add_u32 s42, s42, s44\n" \
"s_addc_u32 s43, s43, 0\n" \
"ds_read_b64_tr_b16 a[112:113], v109 offset:49152\n" \
"ds_read_b64_tr_b16 a[114:115], v109 offset:50176\n" \
"ds_read_b64_tr_b16 a[116:117], v109 offset:53248\n" \
"ds_read_b64_tr_b16 a[118:119], v109 offset:54272\n" \
"ds_read_b64_tr_b16 a[120:121], v109 offset:57344\n" \
"ds_read_b64_tr_b16 a[122:123], v109 offset:58368\n" \
"ds_read_b64_tr_b16 a[124:125], v109 offset:61440\n" \
"ds_read_b64_tr_b16 a[126:127], v109 offset:62464\n" \
"s_waitcnt lgkmcnt(0)\n" \
"s_waitcnt vmcnt(28)\n" \
"s_barrier\n" \
"global_load_dword v152, v150, s[54:55] sc1\n" \
"s_add_u32 m0, s56, 114688\n" \
"s_nop 0\n" \
"global_load_lds_dwordx4 v96, s[42:43] offset:0\n" \
"global_load_lds_dwordx4 v97, s[42:43] offset:1024\n" \
"global_load_lds_dwordx4 v98, s[42:43] offset:2048\n" \
"global_load_lds_dwordx4 v99, s[42:43] offset:3072\n" \
"s_add_u32 s42, s42, s44\n" \
"s_addc_u32 s43, s43, 0\n" \
"ds_read_b64_tr_b16 a[128:129], v110 offset:0\n" \
"ds_read_b64_tr_b16 a[130:131], v110 offset:1024\n" \
"ds_read_b64_tr_b16 a[132:133], v110 offset:4096\n" \
"ds_read_b64_tr_b16 a[134:135], v110 offset:5120\n" \
"ds_read_b64_tr_b16 a[136:137], v110 offset:8192\n" \
"ds_read_b64_tr_b16 a[138:139], v110 offset:9216\n" \
"ds_read_b64_tr_b16 a[140:141], v110 offset:12288\n" \
"ds_read_b64_tr_b16 a[142:143], v110 offset:13312\n" \
"s_waitcnt lgkmcnt(0)\n" \
"s_waitcnt vmcnt(29)\n" \
"s_barrier\n" \
"s_add_u32 m0, s56, 131072\n" \
"s_nop 0\n" \
"global_load_lds_dwordx4 v96, s[42:43] offset:0\n" \
"global_load_lds_dwordx4 v97, s[42:43] offset:1024\n" \
"global_load_lds_dwordx4 v98, s[42:43] offset:2048\n" \
"global_load_lds_dwordx4 v99, s[42:43] offset:3072\n" \
"s_add_u32 s42, s42, s44\n" \
"s_addc_u32 s43, s43, 0\n" \
"ds_read_b64_tr_b16 a[144:145], v108 offset:0\n" \
"ds_read_b64_tr_b16 a[146:147], v108 offset:1024\n" \
"ds_read_b64_tr_b16 a[148:149], v108 offset:4096\n" \
"ds_read_b64_tr_b16 a[150:151], v108 offset:5120\n" \
"ds_read_b64_tr_b16 a[152:153], v108 offset:8192\n" \
"ds_read_b64_tr_b16 a[154:155], v108 offset:9216\n" \
"ds_read_b64_tr_b16 a[156:157], v108 offset:12288\n" \
"ds_read_b64_tr_b16 a[158:159], v108 offset:13312\n" \
"s_waitcnt lgkmcnt(0)\n" \
"s_waitcnt vmcnt(29)\n" \
"s_barrier\n" \
"ds_read_b64_tr_b16 a[160:161], v108 offset:16384\n" \
"ds_read_b64_tr_b16 a[162:163], v108 offset:17408\n" \
"ds_read_b64_tr_b16 a[164:165], v108 offset:20480\n" \
"ds_read_b64_tr_b16 a[166:167], v108 offset:21504\n" \
"ds_read_b64_tr_b16 a[168:169], v108 offset:24576\n" \
"ds_read_b64_tr_b16 a[170:171], v108 offset:25600\n" \
"ds_read_b64_tr_b16 a[172:173], v108 offset:28672\n" \
"ds_read_b64_tr_b16 a[174:175], v108 offset:29696\n" \
"s_waitcnt lgkmcnt(0)\n" \
"s_waitcnt vmcnt(25)\n" \
"s_barrier\n" \
"ds_read_b64_tr_b16 a[176:177], v108 offset:32768\n" \
"ds_read_b64_tr_b16 a[178:179], v108 offset:33792\n" \
"ds_read_b64_tr_b16 a[180:181], v108 offset:36864\n" \
"ds_read_b64_tr_b16 a[182:183], v108 offset:37888\n" \
"ds_read_b64_tr_b16 a[184:185], v108 offset:40960\n" \
"ds_read_b64_tr_b16 a[186:187], v108 offset:41984\n" \
"ds_read_b64_tr_b16 a[188:189], v108 offset:45056\n" \
"ds_read_b64_tr_b16 a[190:191], v108 offset:46080\n" \
"s_waitcnt lgkmcnt(0)\n" \
"s_waitcnt vmcnt(21)\n" \
"s_barrier\n" \
"ds_read_b64_tr_b16 a[192:193], v108 offset:49152\n" \
"ds_read_b64_tr_b16 a[194:195], v108 offset:50176\n" \
"ds_read_b64_tr_b16 a[196:197], v108 offset:53248\n" \
"ds_read_b64_tr_b16 a[198:199], v108 offset:54272\n" \
"ds_read_b64_tr_b16 a[200:201], v108 offset:57344\n" \
"ds_read_b64_tr_b16 a[202:203], v108 offset:58368\n" \
"ds_read_b64_tr_b16 a[204:205], v108 offset:61440\n" \
"ds_read_b64_tr_b16 a[206:207], v108 offset:62464\n" \
"s_waitcnt lgkmcnt(0)\n" \
"s_waitcnt vmcnt(17)\n" \
"s_barrier\n" \
"ds_read_b64_tr_b16 a[208:209], v109 offset:0\n" \
"ds_read_b64_tr_b16 a[210:211], v109 offset:1024\n" \
"ds_read_b64_tr_b16 a[212:213], v109 offset:4096\n" \
"ds_read_b64_tr_b16 a[214:215], v109 offset:5120\n" \
"ds_read_b64_tr_b16 a[216:217], v109 offset:8192\n" \
"ds_read_b64_tr_b16 a[218:219], v109 offset:9216\n" \
"ds_read_b64_tr_b16 a[220:221], v109 offset:12288\n" \
"ds_read_b64_tr_b16 a[222:223], v109 offset:13312\n" \
"s_waitcnt lgkmcnt(0)\n" \
"s_waitcnt vmcnt(13)\n" \
"s_barrier\n" \
"ds_read_b64_tr_b16 a[224:225], v109 offset:16384\n" \
"ds_read_b64_tr_b16 a[226:227], v109 offset:17408\n" \
"ds_read_b64_tr_b16 a[228:229], v109 offset:20480\n" \
"ds_read_b64_tr_b16 a[230:231], v109 offset:21504\n" \
"ds_read_b64_tr_b16 a[232:233], v109 offset:24576\n" \
"ds_read_b64_tr_b16 a[234:235], v109 offset:25600\n" \
"ds_read_b64_tr_b16 a[236:237], v109 offset:28672\n" \
"ds_read_b64_tr_b16 a[238:239], v109 offset:29696\n" \
"s_waitcnt lgkmcnt(0)\n" \
"s_waitcnt vmcnt(9)\n" \
"s_barrier\n" \
"ds_read_b64_tr_b16 a[240:241], v109 offset:32768\n" \
"ds_read_b64_tr_b16 a[242:243], v109 offset:33792\n" \
"ds_read_b64_tr_b16 a[244:245], v109 offset:36864\n" \
"ds_read_b64_tr_b16 a[246:247], v109 offset:37888\n" \
"ds_read_b64_tr_b16 a[248:249], v109 offset:40960\n" \
"ds_read_b64_tr_b16 a[250:251], v109 offset:41984\n" \
"ds_read_b64_tr_b16 a[252:253], v109 offset:45056\n" \
"ds_read_b64_tr_b16 a[254:255], v109 offset:46080\n" \
"s_waitcnt lgkmcnt(0)\n" \
"s_waitcnt vmcnt(4)\n" \
"s_barrier\n" \
".Lhb_c%=:\n" \
"v_cmp_eq_u32 vcc, v151, v152\n" \
"s_nop 4\n" \
"s_cbranch_vccnz .Lhb_s%=\n" \
"global_load_dword v64, v107, s[54:55] sc1\n" \
"global_load_dword v65, v66, s[54:55] sc1\n" \
"ds_read_b64_tr_b16 v[224:225], v109 offset:49152\n" \
"ds_read_b64_tr_b16 v[226:227], v109 offset:50176\n" \
"ds_read_b64_tr_b16 v[228:229], v109 offset:53248\n" \
"ds_read_b64_tr_b16 v[230:231], v109 offset:54272\n" \
"ds_read_b64_tr_b16 v[232:233], v109 offset:57344\n" \
"ds_read_b64_tr_b16 v[234:235], v109 offset:58368\n" \
"ds_read_b64_tr_b16 v[236:237], v109 offset:61440\n" \
"ds_read_b64_tr_b16 v[238:239], v109 offset:62464\n" \
"s_waitcnt lgkmcnt(0)\n" \
"s_waitcnt vmcnt(2)\n" \
"s_barrier\n" \
"ds_read_b64_tr_b16 v[240:241], v110 offset:0\n" \
"ds_read_b64_tr_b16 v[242:243], v110 offset:1024\n" \
"ds_read_b64_tr_b16 v[244:245], v110 offset:4096\n" \
"ds_read_b64_tr_b16 v[246:247], v110 offset:5120\n" \
"ds_read_b64_tr_b16 v[248:249], v110 offset:8192\n" \
"ds_read_b64_tr_b16 v[250:251], v110 offset:9216\n" \
"ds_read_b64_tr_b16 v[252:253], v110 offset:12288\n" \
"ds_read_b64_tr_b16 v[254:255], v110 offset:13312\n" \
"s_waitcnt lgkmcnt(0)\n" \
"s_barrier\n" \
"s_waitcnt vmcnt(0)\n" \
".Lln_c%=:\n" \
"v_cmp_gt_u32 vcc, 1, v64\n" \
"s_cbranch_vccnz .Lln_s%=\n" \
".Lrdy_c%=:\n" \
"v_cmp_gt_u32 vcc, 1, v65\n" \
"s_cbranch_vccnz .Lrdy_s%=\n" \
"v_min_u32_dpp v65, v65, v65 row_shr:1 row_mask:0xf bank_mask:0xf\n" \
"s_nop 1\n" \
"v_min_u32_dpp v65, v65, v65 row_shr:2 row_mask:0xf bank_mask:0xf\n" \
"s_nop 1\n" \
"v_min_u32_dpp v65, v65, v65 row_shr:4 row_mask:0xf bank_mask:0xf\n" \
"s_nop 1\n" \
"v_min_u32_dpp v65, v65, v65 row_shr:8 row_mask:0xf bank_mask:0xf\n" \
"s_nop 0\n" \
"v_readlane_b32 s76, v65, 15\n" \
"v_readlane_b32 s77, v65, 31\n" \
"s_min_u32 s76, s76, s77\n" \
"s_and_b32 s76, s76, 1\n" \
"s_lshl_b32 s76, s76, 13\n" \
"s_xor_b32 s48, s48, s76\n" \
"s_xor_b32 s66, s66, s76\n" \
"s_xor_b32 s68, s48, 0x2000\n" \
"s_mov_b32 s69, s49\n" \
"s_mov_b32 s60, s57\n" \
"s_add_u32 s61, s57, 147456\n" \
"s_mov_b32 s62, 0\n" \
"s_mov_b32 s63, 0\n" \
"s_mov_b32 s64, 0\n" \
"s_add_u32 m0, s60, 0\n" \
"s_nop 0\n" \
"global_load_lds_dwordx4 v102, s[46:47] offset:0 sc1\n" \
"global_load_lds_dwordx4 v102, s[46:47] offset:1024 sc1\n" \
"global_load_lds_dwordx4 v102, s[46:47] offset:2048 sc1\n" \
"global_load_lds_dwordx4 v102, s[46:47] offset:3072 sc1\n" \
"s_add_u32 m0, s60, 4096\n" \
"s_nop 0\n" \
"global_load_lds_dwordx4 v103, s[46:47] offset:0 sc1\n" \
"global_load_lds_dwordx4 v103, s[46:47] offset:1024 sc1\n" \
"global_load_lds_dwordx4 v103, s[46:47] offset:2048 sc1\n" \
"global_load_lds_dwordx4 v103, s[46:47] offset:3072 sc1\n" \
"s_add_u32 m0, s60, 8192\n" \
"s_nop 0\n" \
"global_load_lds_dwordx4 v104, s[46:47] offset:0 sc1\n" \
"s_add_u32 s46, s46, s45\n" \
"s_addc_u32 s47, s47, 0\n" \
"s_add_u32 s60, s60, 36864\n" \
"s_cmp_eq_u32 s60, s61\n" \
"s_cselect_b32 s60, s57, s60\n" \
"s_add_u32 m0, s60, 0\n" \
"s_nop 0\n" \
"global_load_lds_dwordx4 v102, s[46:47] offset:0 sc1\n" \
"global_load_lds_dwordx4 v102, s[46:47] offset:1024 sc1\n" \
"global_load_lds_dwordx4 v102, s[46:47] offset:2048 sc1\n" \
"global_load_lds_dwordx4 v102, s[46:47] offset:3072 sc1\n" \
"s_add_u32 m0, s60, 4096\n" \
"s_nop 0\n" \
"global_load_lds_dwordx4 v103, s[46:47] offset:0 sc1\n" \
"global_load_lds_dwordx4 v103, s[46:47] offset:1024 sc1\n" \
"global_load_lds_dwordx4 v103, s[46:47] offset:2048 sc1\n" \
"global_load_lds_dwordx4 v103, s[46:47] offset:3072 sc1\n" \
"s_add_u32 m0, s60, 8192\n" \
"s_nop 0\n" \
"global_load_lds_dwordx4 v104, s[46:47] offset:0 sc1\n" \
"s_add_u32 s46, s46, s45\n" \
"s_addc_u32 s47, s47, 0\n" \
"s_add_u32 s60, s60, 36864\n" \
"s_cmp_eq_u32 s60, s61\n" \
"s_cselect_b32 s60, s57, s60\n" \
"s_add_u32 m0, s60, 0\n" \
"s_nop 0\n" \
"global_load_lds_dwordx4 v102, s[46:47] offset:0 sc1\n" \
"global_load_lds_dwordx4 v102, s[46:47] offset:1024 sc1\n" \
"global_load_lds_dwordx4 v102, s[46:47] offset:2048 sc1\n" \
"global_load_lds_dwordx4 v102, s[46:47] offset:3072 sc1\n" \
"s_add_u32 m0, s60, 4096\n" \
"s_nop 0\n" \
"global_load_lds_dwordx4 v103, s[46:47] offset:0 sc1\n" \
"global_load_lds_dwordx4 v103, s[46:47] offset:1024 sc1\n" \
"global_load_lds_dwordx4 v103, s[46:47] offset:2048 sc1\n" \
"global_load_lds_dwordx4 v103, s[46:47] offset:3072 sc1\n" \
"s_add_u32 m0, s60, 8192\n" \
"s_nop 0\n" \
"global_load_lds_dwordx4 v104, s[46:47] offset:0 sc1\n" \
"s_add_u32 s46, s46, s45\n" \
"s_addc_u32 s47, s47, 0\n" \
"s_add_u32 s60, s60, 36864\n" \
"s_cmp_eq_u32 s60, s61\n" \
"s_cselect_b32 s60, s57, s60\n" \
"v_mov_b32 v111, v101\n" \
"v_lshlrev_b32 v148, 1, v107\n" \
"v_mov_b32 v144, -1\n" \
"v_mov_b32 v145, -1\n" \
"v_mov_b32 v146, -1\n" \
"v_mov_b32 v147, -1\n" \
"s_waitcnt vmcnt(18)\n" \
"s_barrier\n" \
"ds_read_b128 v[32:35], v111 offset:0\n" \
"ds_read_b128 v[36:39], v111 offset:1024\n" \
"ds_read_b128 v[40:43], v111 offset:2048\n" \
"ds_read_b128 v[44:47], v111 offset:3072\n" \
"ds_read_b128 v[48:51], v111 offset:4096\n" \
"ds_read_b128 v[52:55], v111 offset:5120\n" \
"ds_read_b128 v[56:59], v111 offset:6144\n" \
"ds_read_b128 v[60:63], v111 offset:7168\n" \
"s_waitcnt lgkmcnt(7)\n" \
"v_mfma_f32_32x32x16_bf16 v[0:15], a[0:3], v[32:35], 0\n" \
"v_mfma_f32_32x32x16_bf16 v[16:31], v[32:35], a[144:147], 0\n" \
"s_waitcnt lgkmcnt(6)\n" \
"ds_read_b128 v[32:35], v111 offset:8192\n" \
"v_mfma_f32_32x32x16_bf16 v[0:15], a[4:7], v[36:39], v[0:15]\n" \
"v_mfma_f32_32x32x16_bf16 v[16:31], v[36:39], a[148:151], v[16:31]\n" \
"s_waitcnt lgkmcnt(6)\n" \
"ds_read_b128 v[36:39], v111 offset:9216\n" \
"v_mfma_f32_32x32x16_bf16 v[0:15], a[8:11], v[40:43], v[0:15]\n" \
"v_mfma_f32_32x32x16_bf16 v[16:31], v[40:43], a[152:155], v[16:31]\n" \
"s_waitcnt lgkmcnt(6)\n" \
"ds_read_b128 v[40:43], v111 offset:10240\n" \
"v_mfma_f32_32x32x16_bf16 v[0:15], a[12:15], v[44:47], v[0:15]\n" \
"v_mfma_f32_32x32x16_bf16 v[16:31], v[44:47], a[156:159], v[16:31]\n" \
"s_waitcnt lgkmcnt(6)\n" \
"ds_read_b128 v[44:47], v111 offset:11264\n" \
"v_mfma_f32_32x32x16_bf16 v[0:15], a[16:19], v[48:51], v[0:15]\n" \
"v_mfma_f32_32x32x16_bf16 v[16:31], v[48:51], a[160:163], v[16:31]\n" \
"s_waitcnt lgkmcnt(6)\n" \
"ds_read_b128 v[48:51], v111 offset:12288\n" \
"v_mfma_f32_32x32x16_bf16 v[0:15], a[20:23], v[52:55], v[0:15]\n" \
"v_mfma_f32_32x32x16_bf16 v[16:31], v[52:55], a[164:167], v[16:31]\n" \
"s_waitcnt lgkmcnt(6)\n" \
"ds_read_b128 v[52:55], v111 offset:13312\n" \
"v_mfma_f32_32x32x16_bf16 v[0:15], a[24:27], v[56:59], v[0:15]\n" \
"v_mfma_f32_32x32x16_bf16 v[16:31], v[56:59], a[168:171], v[16:31]\n" \
"s_waitcnt lgkmcnt(6)\n" \
"ds_read_b128 v[56:59], v111 offset:14336\n" \
"v_mfma_f32_32x32x16_bf16 v[0:15], a[28:31], v[60:63], v[0:15]\n" \
"s_add_u32 m0, s60, 0\n" \
"s_nop 0\n" \
"global_load_lds_dwordx4 v102, s[46:47] offset:0 sc1\n" \
"v_mfma_f32_32x32x16_bf16 v[16:31], v[60:63], a[172:175], v[16:31]\n" \
"s_waitcnt lgkmcnt(6)\n" \
"ds_read_b128 v[60:63], v111 offset:15360\n" \
"v_mfma_f32_32x32x16_bf16 v[0:15], a[32:35], v[32:35], v[0:15]\n" \
"v_mfma_f32_32x32x16_bf16 v[16:31], v[32:35], a[176:179], v[16:31]\n" \
"s_waitcnt lgkmcnt(6)\n" \
"ds_read_b128 v[32:35], v111 offset:16384\n" \
"v_mfma_f32_32x32x16_bf16 v[0:15], a[36:39], v[36:39], v[0:15]\n" \
"global_load_lds_dwordx4 v102, s[46:47] offset:1024 sc1\n" \
"v_mfma_f32_32x32x16_bf16 v[16:31], v[36:39], a[180:183], v[16:31]\n" \
"s_waitcnt lgkmcnt(6)\n" \
"ds_read_b128 v[36:39], v111 offset:17408\n" \
"v_mfma_f32_32x32x16_bf16 v[0:15], a[40:43], v[40:43], v[0:15]\n" \
"v_mfma_f32_32x32x16_bf16 v[16:31], v[40:43], a[184:187], v[16:31]\n" \
"s_waitcnt lgkmcnt(6)\n" \
"ds_read_b128 v[40:43], v111 offset:18432\n" \
"v_mfma_f32_32x32x16_bf16 v[0:15], a[44:47], v[44:47], v[0:15]\n" \
"global_load_lds_dwordx4 v102, s[46:47] offset:2048 sc1\n" \
"v_mfma_f32_32x32x16_bf16 v[16:31], v[44:47], a[188:191], v[16:31]\n" \
"s_waitcnt lgkmcnt(6)\n" \
"ds_read_b128 v[44:47], v111 offset:19456\n" \
"v_mfma_f32_32x32x16_bf16 v[0:15], a[48:51], v[48:51], v[0:15]\n" \
"v_mfma_f32_32x32x16_bf16 v[16:31], v[48:51], a[192:195], v[16:31]\n" \
"s_waitcnt lgkmcnt(6)\n" \
"ds_read_b128 v[48:51], v111 offset:20480\n" \
"v_mfma_f32_32x32x16_bf16 v[0:15], a[52:55], v[52:55], v[0:15]\n" \
"global_load_lds_dwordx4 v102, s[46:47] offset:3072 sc1\n" \
"v_mfma_f32_32x32x16_bf16 v[16:31], v[52:55], a[196:199], v[16:31]\n" \
"s_waitcnt lgkmcnt(6)\n" \
"ds_read_b128 v[52:55], v111 offset:21504\n" \
"v_mfma_f32_32x32x16_bf16 v[0:15], a[56:59], v[56:59], v[0:15]\n" \
"v_mfma_f32_32x32x16_bf16 v[16:31], v[56:59], a[200:203], v[16:31]\n" \
"s_waitcnt lgkmcnt(6)\n" \
"ds_read_b128 v[56:59], v111 offset:22528\n" \
"v_mfma_f32_32x32x16_bf16 v[0:15], a[60:63], v[60:63], v[0:15]\n" \
"s_add_u32 m0, s60, 4096\n" \
"s_nop 0\n" \
"global_load_lds_dwordx4 v103, s[46:47] offset:0 sc1\n" \
"v_mfma_f32_32x32x16_bf16 v[16:31], v[60:63], a[204:207], v[16:31]\n" \
"s_waitcnt lgkmcnt(6)\n" \
"ds_read_b128 v[60:63], v111 offset:23552\n" \
"v_mfma_f32_32x32x16_bf16 v[0:15], a[64:67], v[32:35], v[0:15]\n" \
"v_mfma_f32_32x32x16_bf16 v[16:31], v[32:35], a[208:211], v[16:31]\n" \
"s_waitcnt lgkmcnt(6)\n" \
"ds_read_b128 v[32:35], v111 offset:24576\n" \
"v_mfma_f32_32x32x16_bf16 v[0:15], a[68:71], v[36:39], v[0:15]\n" \
"global_load_lds_dwordx4 v103, s[46:47] offset:1024 sc1\n" \
"v_mfma_f32_32x32x16_bf16 v[16:31], v[36:39], a[212:215], v[16:31]\n" \
"s_waitcnt lgkmcnt(6)\n" \
"ds_read_b128 v[36:39], v111 offset:25600\n" \
"v_mfma_f32_32x32x16_bf16 v[0:15], a[72:75], v[40:43], v[0:15]\n" \
"v_mfma_f32_32x32x16_bf16 v[16:31], v[40:43], a[216:219], v[16:31]\n" \
"s_waitcnt lgkmcnt(6)\n" \
"ds_read_b128 v[40:43], v111 offset:26624\n" \
"v_mfma_f32_32x32x16_bf16 v[0:15], a[76:79], v[44:47], v[0:15]\n" \
"global_load_lds_dwordx4 v103, s[46:47] offset:2048 sc1\n" \
"v_mfma_f32_32x32x16_bf16 v[16:31], v[44:47], a[220:223], v[16:31]\n" \
"s_waitcnt lgkmcnt(6)\n" \
"ds_read_b128 v[44:47], v111 offset:27648\n" \
"v_mfma_f32_32x32x16_bf16 v[0:15], a[80:83], v[48:51], v[0:15]\n" \
"s_waitcnt vmcnt(7)\n" \
"s_barrier\n" \
"v_mfma_f32_32x32x16_bf16 v[16:31], v[48:51], a[224:227], v[16:31]\n" \
"s_waitcnt lgkmcnt(6)\n" \
"ds_read_b128 v[48:51], v111 offset:28672\n" \
"v_mfma_f32_32x32x16_bf16 v[0:15], a[84:87], v[52:55], v[0:15]\n" \
"global_load_lds_dwordx4 v103, s[46:47] offset:3072 sc1\n" \
"v_mfma_f32_32x32x16_bf16 v[16:31], v[52:55], a[228:231], v[16:31]\n" \
"s_waitcnt lgkmcnt(6)\n" \
"ds_read_b128 v[52:55], v111 offset:29696\n" \
"v_mfma_f32_32x32x16_bf16 v[0:15], a[88:91], v[56:59], v[0:15]\n" \
"v_mfma_f32_32x32x16_bf16 v[16:31], v[56:59], a[232:235], v[16:31]\n" \
"s_waitcnt lgkmcnt(6)\n" \
"ds_read_b128 v[56:59], v111 offset:30720\n" \
"v_mfma_f32_32x32x16_bf16 v[0:15], a[92:95], v[60:63], v[0:15]\n" \
"s_add_u32 m0, s60, 8192\n" \
"s_nop 0\n" \
"global_load_lds_dwordx4 v104, s[46:47] offset:0 sc1\n" \
"s_add_u32 s46, s46, s45\n" \
"s_addc_u32 s47, s47, 0\n" \
"v_mfma_f32_32x32x16_bf16 v[16:31], v[60:63], a[236:239], v[16:31]\n" \
"s_waitcnt lgkmcnt(6)\n" \
"ds_read_b128 v[60:63], v111 offset:31744\n" \
"v_mfma_f32_32x32x16_bf16 v[0:15], a[96:99], v[32:35], v[0:15]\n" \
"global_store_dwordx4 v101, v[144:147], s[66:67] sc1\n" \
"s_add_u32 s60, s60, 36864\n" \
"v_mfma_f32_32x32x16_bf16 v[16:31], v[32:35], a[240:243], v[16:31]\n" \
"s_waitcnt lgkmcnt(6)\n" \
"ds_read_b128 v[32:35], v111 offset:32768\n" \
"v_mfma_f32_32x32x16_bf16 v[0:15], a[100:103], v[36:39], v[0:15]\n" \
"global_store_dwordx2 v148, v[144:145], s[66:67] offset:1024 sc1\n" \
"s_cmp_eq_u32 s60, s61\n" \
"s_cselect_b32 s60, s57, s60\n" \
"s_add_u32 s62, s62, 36864\n" \
"s_cmp_eq_u32 s62, 147456\n" \
"s_cselect_b32 s62, 0, s62\n" \
"v_mfma_f32_32x32x16_bf16 v[16:31], v[36:39], a[244:247], v[16:31]\n" \
"s_waitcnt lgkmcnt(6)\n" \
"ds_read_b128 v[36:39], v111 offset:33792\n" \
"v_mfma_f32_32x32x16_bf16 v[0:15], a[104:107], v[40:43], v[0:15]\n" \
"s_add_u32 s66, s66, 16384\n" \
"s_addc_u32 s67, s67, 0\n" \
"v_mfma_f32_32x32x16_bf16 v[16:31], v[40:43], a[248:251], v[16:31]\n" \
"s_waitcnt lgkmcnt(6)\n" \
"ds_read_b128 v[40:43], v111 offset:34816\n" \
"v_mfma_f32_32x32x16_bf16 v[0:15], a[108:111], v[44:47], v[0:15]\n" \
"v_mfma_f32_32x32x16_bf16 v[16:31], v[44:47], a[252:255], v[16:31]\n" \
"s_waitcnt lgkmcnt(6)\n" \
"ds_read_b128 v[44:47], v111 offset:35840\n" \
"v_add_u32 v111, s62, v101\n" \
"v_mfma_f32_32x32x16_bf16 v[0:15], a[112:115], v[48:51], v[0:15]\n" \
"v_mfma_f32_32x32x16_bf16 v[16:31], v[48:51], v[224:227], v[16:31]\n" \
"s_waitcnt lgkmcnt(6)\n" \
"ds_read_b128 v[48:51], v111 offset:0\n" \
"v_mfma_f32_32x32x16_bf16 v[0:15], a[116:119], v[52:55], v[0:15]\n" \
"v_mfma_f32_32x32x16_bf16 v[16:31], v[52:55], v[228:231], v[16:31]\n" \
"s_waitcnt lgkmcnt(6)\n" \
"ds_read_b128 v[52:55], v111 offset:1024\n" \
"v_mfma_f32_32x32x16_bf16 v[0:15], a[120:123], v[56:59], v[0:15]\n" \
"v_mfma_f32_32x32x16_bf16 v[16:31], v[56:59], v[232:235], v[16:31]\n" \
"s_waitcnt lgkmcnt(6)\n" \
"ds_read_b128 v[56:59], v111 offset:2048\n" \
"v_mfma_f32_32x32x16_bf16 v[0:15], a[124:127], v[60:63], v[0:15]\n" \
"v_mfma_f32_32x32x16_bf16 v[16:31], v[60:63], v[236:239], v[16:31]\n" \
"s_waitcnt lgkmcnt(6)\n" \
"ds_read_b128 v[60:63], v111 offset:3072\n" \
"v_mfma_f32_32x32x16_bf16 v[0:15], a[128:131], v[32:35], v[0:15]\n" \
"v_mfma_f32_32x32x16_bf16 v[16:31], v[32:35], v[240:243], v[16:31]\n" \
"s_waitcnt lgkmcnt(6)\n" \
"ds_read_b128 v[32:35], v111 offset:4096\n" \
"v_mfma_f32_32x32x16_bf16 v[0:15], a[132:135], v[36:39], v[0:15]\n" \
"v_mfma_f32_32x32x16_bf16 v[16:31], v[36:39], v[244:247], v[16:31]\n" \
"s_waitcnt lgkmcnt(6)\n" \
"ds_read_b128 v[36:39], v111 offset:5120\n" \
"v_mfma_f32_32x32x16_bf16 v[0:15], a[136:139], v[40:43], v[0:15]\n" \
"v_mfma_f32_32x32x16_bf16 v[16:31], v[40:43], v[248:251], v[16:31]\n" \
"s_waitcnt lgkmcnt(6)\n" \
"ds_read_b128 v[40:43], v111 offset:6144\n" \
"v_mfma_f32_32x32x16_bf16 v[0:15], a[140:143], v[44:47], v[0:15]\n" \
"v_mfma_f32_32x32x16_bf16 v[16:31], v[44:47], v[252:255], v[16:31]\n" \
"s_waitcnt lgkmcnt(6)\n" \
"ds_read_b128 v[44:47], v111 offset:7168\n" \
".Ltop%=:\n" \
"v_mfma_f32_32x32x16_bf16 v[112:127], a[0:3], v[48:51], 0\n" \
"s_barrier\n" \
"v_mfma_f32_32x32x16_bf16 v[128:143], v[48:51], a[144:147], 0\n" \
"s_waitcnt lgkmcnt(6)\n" \
"ds_read_b128 v[48:51], v111 offset:8192\n" \
"v_mfma_f32_32x32x16_bf16 v[112:127], a[4:7], v[52:55], v[112:127]\n" \
"v_cvt_pk_bf16_f32 v64, v0, v1\n" \
"v_cvt_pk_bf16_f32 v65, v2, v3\n" \
"v_cvt_pk_bf16_f32 v66, v8, v9\n" \
"v_cvt_pk_bf16_f32 v67, v10, v11\n" \
"v_mfma_f32_32x32x16_bf16 v[128:143], v[52:55], a[148:151], v[128:143]\n" \
"s_waitcnt lgkmcnt(6)\n" \
"ds_read_b128 v[52:55], v111 offset:9216\n" \
"v_mfma_f32_32x32x16_bf16 v[112:127], a[8:11], v[56:59], v[112:127]\n" \
"global_store_dwordx4 v105, v[64:67], s[48:49] offset:0 sc1\n" \
"v_cvt_pk_bf16_f32 v68, v4, v5\n" \
"v_cvt_pk_bf16_f32 v69, v6, v7\n" \
"v_cvt_pk_bf16_f32 v70, v12, v13\n" \
"v_cvt_pk_bf16_f32 v71, v14, v15\n" \
"v_mfma_f32_32x32x16_bf16 v[128:143], v[56:59], a[152:155], v[128:143]\n" \
"s_waitcnt lgkmcnt(6)\n" \
"ds_read_b128 v[56:59], v111 offset:10240\n" \
"v_mfma_f32_32x32x16_bf16 v[112:127], a[12:15], v[60:63], v[112:127]\n" \
"global_store_dwordx4 v105, v[68:71], s[48:49] offset:512 sc1\n" \
"v_cvt_pk_bf16_f32 v72, v16, v17\n" \
"v_cvt_pk_bf16_f32 v73, v18, v19\n" \
"v_cvt_pk_bf16_f32 v74, v20, v21\n" \
"v_cvt_pk_bf16_f32 v75, v22, v23\n" \
"v_mfma_f32_32x32x16_bf16 v[128:143], v[60:63], a[156:159], v[128:143]\n" \
"s_waitcnt lgkmcnt(6)\n" \
"ds_read_b128 v[60:63], v111 offset:11264\n" \
"v_mfma_f32_32x32x16_bf16 v[112:127], a[16:19], v[32:35], v[112:127]\n" \
"global_store_dwordx4 v106, v[72:75], s[48:49] offset:0 sc1\n" \
"v_cvt_pk_bf16_f32 v76, v24, v25\n" \
"v_cvt_pk_bf16_f32 v77, v26, v27\n" \
"v_cvt_pk_bf16_f32 v78, v28, v29\n" \
"v_cvt_pk_bf16_f32 v79, v30, v31\n" \
"v_mfma_f32_32x32x16_bf16 v[128:143], v[32:35], a[160:163], v[128:143]\n" \
"s_waitcnt lgkmcnt(6)\n" \
"ds_read_b128 v[32:35], v111 offset:12288\n" \
"v_mfma_f32_32x32x16_bf16 v[112:127], a[20:23], v[36:39], v[112:127]\n" \
"global_store_dwordx4 v106, v[76:79], s[48:49] offset:512 sc1\n" \
"v_mfma_f32_32x32x16_bf16 v[128:143], v[36:39], a[164:167], v[128:143]\n" \
"s_waitcnt lgkmcnt(6)\n" \
"ds_read_b128 v[36:39], v111 offset:13312\n" \
"v_mfma_f32_32x32x16_bf16 v[112:127], a[24:27], v[40:43], v[112:127]\n" \
"global_store_dwordx4 v105, v[64:67], s[68:69] offset:0 sc1\n" \
"v_mfma_f32_32x32x16_bf16 v[128:143], v[40:43], a[168:171], v[128:143]\n" \
"s_waitcnt lgkmcnt(6)\n" \
"ds_read_b128 v[40:43], v111 offset:14336\n" \
"v_mfma_f32_32x32x16_bf16 v[112:127], a[28:31], v[44:47], v[112:127]\n" \
"global_store_dwordx4 v105, v[68:71], s[68:69] offset:512 sc1\n" \
"v_mfma_f32_32x32x16_bf16 v[128:143], v[44:47], a[172:175], v[128:143]\n" \
"s_waitcnt lgkmcnt(6)\n" \
"ds_read_b128 v[44:47], v111 offset:15360\n" \
"v_mfma_f32_32x32x16_bf16 v[112:127], a[32:35], v[48:51], v[112:127]\n" \
"global_store_dwordx4 v106, v[72:75], s[68:69] offset:0 sc1\n" \
"v_mfma_f32_32x32x16_bf16 v[128:143], v[48:51], a[176:179], v[128:143]\n" \
"s_waitcnt lgkmcnt(6)\n" \
"ds_read_b128 v[48:51], v111 offset:16384\n" \
"v_mfma_f32_32x32x16_bf16 v[112:127], a[36:39], v[52:55], v[112:127]\n" \
"global_store_dwordx4 v106, v[76:79], s[68:69] offset:512 sc1\n" \
"s_add_u32 s48, s48, s50\n" \
"s_addc_u32 s49, s49, 0\n" \
"v_mfma_f32_32x32x16_bf16 v[128:143], v[52:55], a[180:183], v[128:143]\n" \
"s_waitcnt lgkmcnt(6)\n" \
"ds_read_b128 v[52:55], v111 offset:17408\n" \
"v_mfma_f32_32x32x16_bf16 v[112:127], a[40:43], v[56:59], v[112:127]\n" \
"s_add_u32 m0, s60, 0\n" \
"s_nop 0\n" \
"global_load_lds_dwordx4 v102, s[46:47] offset:0 sc1\n" \
"s_xor_b32 s68, s48, 0x2000\n" \
"s_mov_b32 s69, s49\n" \
"v_mfma_f32_32x32x16_bf16 v[128:143], v[56:59], a[184:187], v[128:143]\n" \
"s_waitcnt lgkmcnt(6)\n" \
"ds_read_b128 v[56:59], v111 offset:18432\n" \
"v_mfma_f32_32x32x16_bf16 v[112:127], a[44:47], v[60:63], v[112:127]\n" \
"global_load_lds_dwordx4 v102, s[46:47] offset:1024 sc1\n" \
"v_mfma_f32_32x32x16_bf16 v[128:143], v[60:63], a[188:191], v[128:143]\n" \
"s_waitcnt lgkmcnt(6)\n" \
"ds_read_b128 v[60:63], v111 offset:19456\n" \
"v_mfma_f32_32x32x16_bf16 v[112:127], a[48:51], v[32:35], v[112:127]\n" \
"global_load_lds_dwordx4 v102, s[46:47] offset:2048 sc1\n" \
"v_mfma_f32_32x32x16_bf16 v[128:143], v[32:35], a[192:195], v[128:143]\n" \
"s_waitcnt lgkmcnt(6)\n" \
"ds_read_b128 v[32:35], v111 offset:20480\n" \
"v_mfma_f32_32x32x16_bf16 v[112:127], a[52:55], v[36:39], v[112:127]\n" \
"global_load_lds_dwordx4 v102, s[46:47] offset:3072 sc1\n" \
"v_mfma_f32_32x32x16_bf16 v[128:143], v[36:39], a[196:199], v[128:143]\n" \
"s_waitcnt lgkmcnt(6)\n" \
"ds_read_b128 v[36:39], v111 offset:21504\n" \
"v_mfma_f32_32x32x16_bf16 v[112:127], a[56:59], v[40:43], v[112:127]\n" \
"v_mfma_f32_32x32x16_bf16 v[128:143], v[40:43], a[200:203], v[128:143]\n" \
"s_waitcnt lgkmcnt(6)\n" \
"ds_read_b128 v[40:43], v111 offset:22528\n" \
"v_mfma_f32_32x32x16_bf16 v[112:127], a[60:63], v[44:47], v[112:127]\n" \
"s_add_u32 m0, s60, 4096\n" \
"s_nop 0\n" \
"global_load_lds_dwordx4 v103, s[46:47] offset:0 sc1\n" \
"v_mfma_f32_32x32x16_bf16 v[128:143], v[44:47], a[204:207], v[128:143]\n" \
"s_waitcnt lgkmcnt(6)\n" \
"ds_read_b128 v[44:47], v111 offset:23552\n" \
"v_mfma_f32_32x32x16_bf16 v[112:127], a[64:67], v[48:51], v[112:127]\n" \
"v_mfma_f32_32x32x16_bf16 v[128:143], v[48:51], a[208:211], v[128:143]\n" \
"s_waitcnt lgkmcnt(6)\n" \
"ds_read_b128 v[48:51], v111 offset:24576\n" \
"v_mfma_f32_32x32x16_bf16 v[112:127], a[68:71], v[52:55], v[112:127]\n" \
"global_load_lds_dwordx4 v103, s[46:47] offset:1024 sc1\n" \
"v_mfma_f32_32x32x16_bf16 v[128:143], v[52:55], a[212:215], v[128:143]\n" \
"s_waitcnt lgkmcnt(6)\n" \
"ds_read_b128 v[52:55], v111 offset:25600\n" \
"v_mfma_f32_32x32x16_bf16 v[112:127], a[72:75], v[56:59], v[112:127]\n" \
"v_mfma_f32_32x32x16_bf16 v[128:143], v[56:59], a[216:219], v[128:143]\n" \
"s_waitcnt lgkmcnt(6)\n" \
"ds_read_b128 v[56:59], v111 offset:26624\n" \
"v_mfma_f32_32x32x16_bf16 v[112:127], a[76:79], v[60:63], v[112:127]\n" \
"global_load_lds_dwordx4 v103, s[46:47] offset:2048 sc1\n" \
"v_mfma_f32_32x32x16_bf16 v[128:143], v[60:63], a[220:223], v[128:143]\n" \
"s_waitcnt lgkmcnt(6)\n" \
"ds_read_b128 v[60:63], v111 offset:27648\n" \
"v_mfma_f32_32x32x16_bf16 v[112:127], a[80:83], v[32:35], v[112:127]\n" \
"v_mfma_f32_32x32x16_bf16 v[128:143], v[32:35], a[224:227], v[128:143]\n" \
"s_waitcnt lgkmcnt(6)\n" \
"ds_read_b128 v[32:35], v111 offset:28672\n" \
"v_mfma_f32_32x32x16_bf16 v[112:127], a[84:87], v[36:39], v[112:127]\n" \
"global_load_lds_dwordx4 v103, s[46:47] offset:3072 sc1\n" \
"v_mfma_f32_32x32x16_bf16 v[128:143], v[36:39], a[228:231], v[128:143]\n" \
"s_waitcnt lgkmcnt(6)\n" \
"ds_read_b128 v[36:39], v111 offset:29696\n" \
"v_mfma_f32_32x32x16_bf16 v[112:127], a[88:91], v[40:43], v[112:127]\n" \
"v_mfma_f32_32x32x16_bf16 v[128:143], v[40:43], a[232:235], v[128:143]\n" \
"s_waitcnt lgkmcnt(6)\n" \
"ds_read_b128 v[40:43], v111 offset:30720\n" \
"v_mfma_f32_32x32x16_bf16 v[112:127], a[92:95], v[44:47], v[112:127]\n" \
"s_add_u32 m0, s60, 8192\n" \
"s_nop 0\n" \
"global_load_lds_dwordx4 v104, s[46:47] offset:0 sc1\n" \
"s_add_u32 s46, s46, s45\n" \
"s_addc_u32 s47, s47, 0\n" \
"v_mfma_f32_32x32x16_bf16 v[128:143], v[44:47], a[236:239], v[128:143]\n" \
"s_waitcnt lgkmcnt(6)\n" \
"ds_read_b128 v[44:47], v111 offset:31744\n" \
"v_mfma_f32_32x32x16_bf16 v[112:127], a[96:99], v[48:51], v[112:127]\n" \
"global_store_dwordx4 v101, v[144:147], s[66:67] sc1\n" \
"s_add_u32 s60, s60, 36864\n" \
"v_mfma_f32_32x32x16_bf16 v[128:143], v[48:51], a[240:243], v[128:143]\n" \
"s_waitcnt lgkmcnt(6)\n" \
"ds_read_b128 v[48:51], v111 offset:32768\n" \
"v_mfma_f32_32x32x16_bf16 v[112:127], a[100:103], v[52:55], v[112:127]\n" \
"global_store_dwordx2 v148, v[144:145], s[66:67] offset:1024 sc1\n" \
"s_cmp_eq_u32 s60, s61\n" \
"s_cselect_b32 s60, s57, s60\n" \
"s_add_u32 s62, s62, 36864\n" \
"s_cmp_eq_u32 s62, 147456\n" \
"s_cselect_b32 s62, 0, s62\n" \
"v_mfma_f32_32x32x16_bf16 v[128:143], v[52:55], a[244:247], v[128:143]\n" \
"s_waitcnt lgkmcnt(6)\n" \
"ds_read_b128 v[52:55], v111 offset:33792\n" \
"v_mfma_f32_32x32x16_bf16 v[112:127], a[104:107], v[56:59], v[112:127]\n" \
"s_add_u32 s66, s66, 16384\n" \
"s_addc_u32 s67, s67, 0\n" \
"v_mfma_f32_32x32x16_bf16 v[128:143], v[56:59], a[248:251], v[128:143]\n" \
"s_waitcnt lgkmcnt(6)\n" \
"ds_read_b128 v[56:59], v111 offset:34816\n" \
"v_mfma_f32_32x32x16_bf16 v[112:127], a[108:111], v[60:63], v[112:127]\n" \
"v_mfma_f32_32x32x16_bf16 v[128:143], v[60:63], a[252:255], v[128:143]\n" \
"s_waitcnt lgkmcnt(6)\n" \
"ds_read_b128 v[60:63], v111 offset:35840\n" \
"v_add_u32 v111, s62, v101\n" \
"v_mfma_f32_32x32x16_bf16 v[112:127], a[112:115], v[32:35], v[112:127]\n" \
"v_mfma_f32_32x32x16_bf16 v[128:143], v[32:35], v[224:227], v[128:143]\n" \
"s_waitcnt lgkmcnt(6)\n" \
"ds_read_b128 v[32:35], v111 offset:0\n" \
"v_mfma_f32_32x32x16_bf16 v[112:127], a[116:119], v[36:39], v[112:127]\n" \
"v_mfma_f32_32x32x16_bf16 v[128:143], v[36:39], v[228:231], v[128:143]\n" \
"s_waitcnt lgkmcnt(6)\n" \
"ds_read_b128 v[36:39], v111 offset:1024\n" \
"v_mov_b32 v149, s58\n" \
"v_mfma_f32_32x32x16_bf16 v[112:127], a[120:123], v[40:43], v[112:127]\n" \
"s_waitcnt vmcnt(11)\n" \
"s_mov_b64 exec, 1\n" \
"global_store_dword v107, v149, s[52:53] sc1\n" \
"s_mov_b64 exec, -1\n" \
"v_mfma_f32_32x32x16_bf16 v[128:143], v[40:43], v[232:235], v[128:143]\n" \
"s_waitcnt lgkmcnt(6)\n" \
"ds_read_b128 v[40:43], v111 offset:2048\n" \
"v_mfma_f32_32x32x16_bf16 v[112:127], a[124:127], v[44:47], v[112:127]\n" \
"s_mov_b32 s63, s58\n" \
"s_add_u32 s58, s58, s59\n" \
"v_mfma_f32_32x32x16_bf16 v[128:143], v[44:47], v[236:239], v[128:143]\n" \
"s_waitcnt lgkmcnt(6)\n" \
"ds_read_b128 v[44:47], v111 offset:3072\n" \
"v_mfma_f32_32x32x16_bf16 v[112:127], a[128:131], v[48:51], v[112:127]\n" \
"v_mfma_f32_32x32x16_bf16 v[128:143], v[48:51], v[240:243], v[128:143]\n" \
"s_waitcnt lgkmcnt(6)\n" \
"ds_read_b128 v[48:51], v111 offset:4096\n" \
"v_mfma_f32_32x32x16_bf16 v[112:127], a[132:135], v[52:55], v[112:127]\n" \
"v_mfma_f32_32x32x16_bf16 v[128:143], v[52:55], v[244:247], v[128:143]\n" \
"s_waitcnt lgkmcnt(6)\n" \
"ds_read_b128 v[52:55], v111 offset:5120\n" \
"v_mfma_f32_32x32x16_bf16 v[112:127], a[136:139], v[56:59], v[112:127]\n" \
"v_mfma_f32_32x32x16_bf16 v[128:143], v[56:59], v[248:251], v[128:143]\n" \
"s_waitcnt lgkmcnt(6)\n" \
"ds_read_b128 v[56:59], v111 offset:6144\n" \
"v_mfma_f32_32x32x16_bf16 v[112:127], a[140:143], v[60:63], v[112:127]\n" \
"s_sub_u32 s51, s51, 1\n" \
"s_cmp_eq_u32 s51, 0\n" \
"v_mfma_f32_32x32x16_bf16 v[128:143], v[60:63], v[252:255], v[128:143]\n" \
"s_waitcnt lgkmcnt(6)\n" \
"ds_read_b128 v[60:63], v111 offset:7168\n" \
"s_cbranch_scc1 .Lfin%=\n" \
"v_mfma_f32_32x32x16_bf16 v[0:15], a[0:3], v[32:35], 0\n" \
"s_barrier\n" \
"v_mfma_f32_32x32x16_bf16 v[16:31], v[32:35], a[144:147], 0\n" \
"s_waitcnt lgkmcnt(6)\n" \
"ds_read_b128 v[32:35], v111 offset:8192\n" \
"v_mfma_f32_32x32x16_bf16 v[0:15], a[4:7], v[36:39], v[0:15]\n" \
"v_cvt_pk_bf16_f32 v64, v112, v113\n" \
"v_cvt_pk_bf16_f32 v65, v114, v115\n" \
"v_cvt_pk_bf16_f32 v66, v120, v121\n" \
"v_cvt_pk_bf16_f32 v67, v122, v123\n" \
"v_mfma_f32_32x32x16_bf16 v[16:31], v[36:39], a[148:151], v[16:31]\n" \
"s_waitcnt lgkmcnt(6)\n" \
"ds_read_b128 v[36:39], v111 offset:9216\n" \
"v_mfma_f32_32x32x16_bf16 v[0:15], a[8:11], v[40:43], v[0:15]\n" \
"global_store_dwordx4 v105, v[64:67], s[48:49] offset:0 sc1\n" \
"v_cvt_pk_bf16_f32 v68, v116, v117\n" \
"v_cvt_pk_bf16_f32 v69, v118, v119\n" \
"v_cvt_pk_bf16_f32 v70, v124, v125\n" \
"v_cvt_pk_bf16_f32 v71, v126, v127\n" \
"v_mfma_f32_32x32x16_bf16 v[16:31], v[40:43], a[152:155], v[16:31]\n" \
"s_waitcnt lgkmcnt(6)\n" \
"ds_read_b128 v[40:43], v111 offset:10240\n" \
"v_mfma_f32_32x32x16_bf16 v[0:15], a[12:15], v[44:47], v[0:15]\n" \
"global_store_dwordx4 v105, v[68:71], s[48:49] offset:512 sc1\n" \
"v_cvt_pk_bf16_f32 v72, v128, v129\n" \
"v_cvt_pk_bf16_f32 v73, v130, v131\n" \
"v_cvt_pk_bf16_f32 v74, v132, v133\n" \
"v_cvt_pk_bf16_f32 v75, v134, v135\n" \
"v_mfma_f32_32x32x16_bf16 v[16:31], v[44:47], a[156:159], v[16:31]\n" \
"s_waitcnt lgkmcnt(6)\n" \
"ds_read_b128 v[44:47], v111 offset:11264\n" \
"v_mfma_f32_32x32x16_bf16 v[0:15], a[16:19], v[48:51], v[0:15]\n" \
"global_store_dwordx4 v106, v[72:75], s[48:49] offset:0 sc1\n" \
"v_cvt_pk_bf16_f32 v76, v136, v137\n" \
"v_cvt_pk_bf16_f32 v77, v138, v139\n" \
"v_cvt_pk_bf16_f32 v78, v140, v141\n" \
"v_cvt_pk_bf16_f32 v79, v142, v143\n" \
"v_mfma_f32_32x32x16_bf16 v[16:31], v[48:51], a[160:163], v[16:31]\n" \
"s_waitcnt lgkmcnt(6)\n" \
"ds_read_b128 v[48:51], v111 offset:12288\n" \
"v_mfma_f32_32x32x16_bf16 v[0:15], a[20:23], v[52:55], v[0:15]\n" \
"global_store_dwordx4 v106, v[76:79], s[48:49] offset:512 sc1\n" \
"v_mfma_f32_32x32x16_bf16 v[16:31], v[52:55], a[164:167], v[16:31]\n" \
"s_waitcnt lgkmcnt(6)\n" \
"ds_read_b128 v[52:55], v111 offset:13312\n" \
"v_mfma_f32_32x32x16_bf16 v[0:15], a[24:27], v[56:59], v[0:15]\n" \
"global_store_dwordx4 v105, v[64:67], s[68:69] offset:0 sc1\n" \
"v_mfma_f32_32x32x16_bf16 v[16:31], v[56:59], a[168:171], v[16:31]\n" \
"s_waitcnt lgkmcnt(6)\n" \
"ds_read_b128 v[56:59], v111 offset:14336\n" \
"v_mfma_f32_32x32x16_bf16 v[0:15], a[28:31], v[60:63], v[0:15]\n" \
"global_store_dwordx4 v105, v[68:71], s[68:69] offset:512 sc1\n" \
"v_mfma_f32_32x32x16_bf16 v[16:31], v[60:63], a[172:175], v[16:31]\n" \
"s_waitcnt lgkmcnt(6)\n" \
"ds_read_b128 v[60:63], v111 offset:15360\n" \
"v_mfma_f32_32x32x16_bf16 v[0:15], a[32:35], v[32:35], v[0:15]\n" \
"global_store_dwordx4 v106, v[72:75], s[68:69] offset:0 sc1\n" \
"v_mfma_f32_32x32x16_bf16 v[16:31], v[32:35], a[176:179], v[16:31]\n" \
"s_waitcnt lgkmcnt(6)\n" \
"ds_read_b128 v[32:35], v111 offset:16384\n" \
"v_mfma_f32_32x32x16_bf16 v[0:15], a[36:39], v[36:39], v[0:15]\n" \
"global_store_dwordx4 v106, v[76:79], s[68:69] offset:512 sc1\n" \
"s_add_u32 s48, s48, s50\n" \
"s_addc_u32 s49, s49, 0\n" \
"v_mfma_f32_32x32x16_bf16 v[16:31], v[36:39], a[180:183], v[16:31]\n" \
"s_waitcnt lgkmcnt(6)\n" \
"ds_read_b128 v[36:39], v111 offset:17408\n" \
"v_mfma_f32_32x32x16_bf16 v[0:15], a[40:43], v[40:43], v[0:15]\n" \
"s_add_u32 m0, s60, 0\n" \
"s_nop 0\n" \
"global_load_lds_dwordx4 v102, s[46:47] offset:0 sc1\n" \
"s_xor_b32 s68, s48, 0x2000\n" \
"s_mov_b32 s69, s49\n" \
"v_mfma_f32_32x32x16_bf16 v[16:31], v[40:43], a[184:187], v[16:31]\n" \
"s_waitcnt lgkmcnt(6)\n" \
"ds_read_b128 v[40:43], v111 offset:18432\n" \
"v_mfma_f32_32x32x16_bf16 v[0:15], a[44:47], v[44:47], v[0:15]\n" \
"global_load_lds_dwordx4 v102, s[46:47] offset:1024 sc1\n" \
"v_mfma_f32_32x32x16_bf16 v[16:31], v[44:47], a[188:191], v[16:31]\n" \
"s_waitcnt lgkmcnt(6)\n" \
"ds_read_b128 v[44:47], v111 offset:19456\n" \
"v_mfma_f32_32x32x16_bf16 v[0:15], a[48:51], v[48:51], v[0:15]\n" \
"global_load_lds_dwordx4 v102, s[46:47] offset:2048 sc1\n" \
"v_mfma_f32_32x32x16_bf16 v[16:31], v[48:51], a[192:195], v[16:31]\n" \
"s_waitcnt lgkmcnt(6)\n" \
"ds_read_b128 v[48:51], v111 offset:20480\n" \
"v_mfma_f32_32x32x16_bf16 v[0:15], a[52:55], v[52:55], v[0:15]\n" \
"global_load_lds_dwordx4 v102, s[46:47] offset:3072 sc1\n" \
"v_mfma_f32_32x32x16_bf16 v[16:31], v[52:55], a[196:199], v[16:31]\n" \
"s_waitcnt lgkmcnt(6)\n" \
"ds_read_b128 v[52:55], v111 offset:21504\n" \
"v_mfma_f32_32x32x16_bf16 v[0:15], a[56:59], v[56:59], v[0:15]\n" \
"v_mfma_f32_32x32x16_bf16 v[16:31], v[56:59], a[200:203], v[16:31]\n" \
"s_waitcnt lgkmcnt(6)\n" \
"ds_read_b128 v[56:59], v111 offset:22528\n" \
"v_mfma_f32_32x32x16_bf16 v[0:15], a[60:63], v[60:63], v[0:15]\n" \
"s_add_u32 m0, s60, 4096\n" \
"s_nop 0\n" \
"global_load_lds_dwordx4 v103, s[46:47] offset:0 sc1\n" \
"v_mfma_f32_32x32x16_bf16 v[16:31], v[60:63], a[204:207], v[16:31]\n" \
"s_waitcnt lgkmcnt(6)\n" \
"ds_read_b128 v[60:63], v111 offset:23552\n" \
"v_mfma_f32_32x32x16_bf16 v[0:15], a[64:67], v[32:35], v[0:15]\n" \
"v_mfma_f32_32x32x16_bf16 v[16:31], v[32:35], a[208:211], v[16:31]\n" \
"s_waitcnt lgkmcnt(6)\n" \
"ds_read_b128 v[32:35], v111 offset:24576\n" \
"v_mfma_f32_32x32x16_bf16 v[0:15], a[68:71], v[36:39], v[0:15]\n" \
"global_load_lds_dwordx4 v103, s[46:47] offset:1024 sc1\n" \
"v_mfma_f32_32x32x16_bf16 v[16:31], v[36:39], a[212:215], v[16:31]\n" \
"s_waitcnt lgkmcnt(6)\n" \
"ds_read_b128 v[36:39], v111 offset:25600\n" \
"v_mfma_f32_32x32x16_bf16 v[0:15], a[72:75], v[40:43], v[0:15]\n" \
"v_mfma_f32_32x32x16_bf16 v[16:31], v[40:43], a[216:219], v[16:31]\n" \
"s_waitcnt lgkmcnt(6)\n" \
"ds_read_b128 v[40:43], v111 offset:26624\n" \
"v_mfma_f32_32x32x16_bf16 v[0:15], a[76:79], v[44:47], v[0:15]\n" \
"global_load_lds_dwordx4 v103, s[46:47] offset:2048 sc1\n" \
"v_mfma_f32_32x32x16_bf16 v[16:31], v[44:47], a[220:223], v[16:31]\n" \
"s_waitcnt lgkmcnt(6)\n" \
"ds_read_b128 v[44:47], v111 offset:27648\n" \
"v_mfma_f32_32x32x16_bf16 v[0:15], a[80:83], v[48:51], v[0:15]\n" \
"v_mfma_f32_32x32x16_bf16 v[16:31], v[48:51], a[224:227], v[16:31]\n" \
"s_waitcnt lgkmcnt(6)\n" \
"ds_read_b128 v[48:51], v111 offset:28672\n" \
"v_mfma_f32_32x32x16_bf16 v[0:15], a[84:87], v[52:55], v[0:15]\n" \
"global_load_lds_dwordx4 v103, s[46:47] offset:3072 sc1\n" \
"v_mfma_f32_32x32x16_bf16 v[16:31], v[52:55], a[228:231], v[16:31]\n" \
"s_waitcnt lgkmcnt(6)\n" \
"ds_read_b128 v[52:55], v111 offset:29696\n" \
"v_mfma_f32_32x32x16_bf16 v[0:15], a[88:91], v[56:59], v[0:15]\n" \
"v_mfma_f32_32x32x16_bf16 v[16:31], v[56:59], a[232:235], v[16:31]\n" \
"s_waitcnt lgkmcnt(6)\n" \
"ds_read_b128 v[56:59], v111 offset:30720\n" \
"v_mfma_f32_32x32x16_bf16 v[0:15], a[92:95], v[60:63], v[0:15]\n" \
"s_add_u32 m0, s60, 8192\n" \
"s_nop 0\n" \
"global_load_lds_dwordx4 v104, s[46:47] offset:0 sc1\n" \
"s_add_u32 s46, s46, s45\n" \
"s_addc_u32 s47, s47, 0\n" \
"v_mfma_f32_32x32x16_bf16 v[16:31], v[60:63], a[236:239], v[16:31]\n" \
"s_waitcnt lgkmcnt(6)\n" \
"ds_read_b128 v[60:63], v111 offset:31744\n" \
"v_mfma_f32_32x32x16_bf16 v[0:15], a[96:99], v[32:35], v[0:15]\n" \
"global_store_dwordx4 v101, v[144:147], s[66:67] sc1\n" \
"s_add_u32 s60, s60, 36864\n" \
"v_mfma_f32_32x32x16_bf16 v[16:31], v[32:35], a[240:243], v[16:31]\n" \
"s_waitcnt lgkmcnt(6)\n" \
"ds_read_b128 v[32:35], v111 offset:32768\n" \
"v_mfma_f32_32x32x16_bf16 v[0:15], a[100:103], v[36:39], v[0:15]\n" \
"global_store_dwordx2 v148, v[144:145], s[66:67] offset:1024 sc1\n" \
"s_cmp_eq_u32 s60, s61\n" \
"s_cselect_b32 s60, s57, s60\n" \
"s_add_u32 s62, s62, 36864\n" \
"s_cmp_eq_u32 s62, 147456\n" \
"s_cselect_b32 s62, 0, s62\n" \
"v_mfma_f32_32x32x16_bf16 v[16:31], v[36:39], a[244:247], v[16:31]\n" \
"s_waitcnt lgkmcnt(6)\n" \
"ds_read_b128 v[36:39], v111 offset:33792\n" \
"v_mfma_f32_32x32x16_bf16 v[0:15], a[104:107], v[40:43], v[0:15]\n" \
"s_add_u32 s66, s66, 16384\n" \
"s_addc_u32 s67, s67, 0\n" \
"v_mfma_f32_32x32x16_bf16 v[16:31], v[40:43], a[248:251], v[16:31]\n" \
"s_waitcnt lgkmcnt(6)\n" \
"ds_read_b128 v[40:43], v111 offset:34816\n" \
"v_mfma_f32_32x32x16_bf16 v[0:15], a[108:111], v[44:47], v[0:15]\n" \
"v_mfma_f32_32x32x16_bf16 v[16:31], v[44:47], a[252:255], v[16:31]\n" \
"s_waitcnt lgkmcnt(6)\n" \
"ds_read_b128 v[44:47], v111 offset:35840\n" \
"v_add_u32 v111, s62, v101\n" \
"v_mfma_f32_32x32x16_bf16 v[0:15], a[112:115], v[48:51], v[0:15]\n" \
"v_mfma_f32_32x32x16_bf16 v[16:31], v[48:51], v[224:227], v[16:31]\n" \
"s_waitcnt lgkmcnt(6)\n" \
"ds_read_b128 v[48:51], v111 offset:0\n" \
"v_mfma_f32_32x32x16_bf16 v[0:15], a[116:119], v[52:55], v[0:15]\n" \
"v_mfma_f32_32x32x16_bf16 v[16:31], v[52:55], v[228:231], v[16:31]\n" \
"s_waitcnt lgkmcnt(6)\n" \
"ds_read_b128 v[52:55], v111 offset:1024\n" \
"v_mov_b32 v149, s58\n" \
"v_mfma_f32_32x32x16_bf16 v[0:15], a[120:123], v[56:59], v[0:15]\n" \
"s_waitcnt vmcnt(11)\n" \
"s_mov_b64 exec, 1\n" \
"global_store_dword v107, v149, s[52:53] sc1\n" \
"s_mov_b64 exec, -1\n" \
"v_mfma_f32_32x32x16_bf16 v[16:31], v[56:59], v[232:235], v[16:31]\n" \
"s_waitcnt lgkmcnt(6)\n" \
"ds_read_b128 v[56:59], v111 offset:2048\n" \
"v_mfma_f32_32x32x16_bf16 v[0:15], a[124:127], v[60:63], v[0:15]\n" \
"s_mov_b32 s63, s58\n" \
"s_add_u32 s58, s58, s59\n" \
"v_mfma_f32_32x32x16_bf16 v[16:31], v[60:63], v[236:239], v[16:31]\n" \
"s_waitcnt lgkmcnt(6)\n" \
"ds_read_b128 v[60:63], v111 offset:3072\n" \
"v_mfma_f32_32x32x16_bf16 v[0:15], a[128:131], v[32:35], v[0:15]\n" \
"v_mfma_f32_32x32x16_bf16 v[16:31], v[32:35], v[240:243], v[16:31]\n" \
"s_waitcnt lgkmcnt(6)\n" \
"ds_read_b128 v[32:35], v111 offset:4096\n" \
"v_mfma_f32_32x32x16_bf16 v[0:15], a[132:135], v[36:39], v[0:15]\n" \
"v_mfma_f32_32x32x16_bf16 v[16:31], v[36:39], v[244:247], v[16:31]\n" \
"s_waitcnt lgkmcnt(6)\n" \
"ds_read_b128 v[36:39], v111 offset:5120\n" \
"v_mfma_f32_32x32x16_bf16 v[0:15], a[136:139], v[40:43], v[0:15]\n" \
"v_mfma_f32_32x32x16_bf16 v[16:31], v[40:43], v[248:251], v[16:31]\n" \
"s_waitcnt lgkmcnt(6)\n" \
"ds_read_b128 v[40:43], v111 offset:6144\n" \
"v_mfma_f32_32x32x16_bf16 v[0:15], a[140:143], v[44:47], v[0:15]\n" \
"v_mfma_f32_32x32x16_bf16 v[16:31], v[44:47], v[252:255], v[16:31]\n" \
"s_waitcnt lgkmcnt(6)\n" \
"ds_read_b128 v[44:47], v111 offset:7168\n" \
"s_branch .Ltop%=\n" \
".Lfin%=:\n" \
"s_nop 15\n" \
"v_cvt_pk_bf16_f32 v64, v112, v113\n" \
"v_cvt_pk_bf16_f32 v65, v114, v115\n" \
"v_cvt_pk_bf16_f32 v66, v120, v121\n" \
"v_cvt_pk_bf16_f32 v67, v122, v123\n" \
"global_store_dwordx4 v105, v[64:67], s[48:49] offset:0 sc1\n" \
"v_cvt_pk_bf16_f32 v68, v116, v117\n" \
"v_cvt_pk_bf16_f32 v69, v118, v119\n" \
"v_cvt_pk_bf16_f32 v70, v124, v125\n" \
"v_cvt_pk_bf16_f32 v71, v126, v127\n" \
"global_store_dwordx4 v105, v[68:71], s[48:49] offset:512 sc1\n" \
"v_cvt_pk_bf16_f32 v72, v128, v129\n" \
"v_cvt_pk_bf16_f32 v73, v130, v131\n" \
"v_cvt_pk_bf16_f32 v74, v132, v133\n" \
"v_cvt_pk_bf16_f32 v75, v134, v135\n" \
"global_store_dwordx4 v106, v[72:75], s[48:49] offset:0 sc1\n" \
"v_cvt_pk_bf16_f32 v76, v136, v137\n" \
"v_cvt_pk_bf16_f32 v77, v138, v139\n" \
"v_cvt_pk_bf16_f32 v78, v140, v141\n" \
"v_cvt_pk_bf16_f32 v79, v142, v143\n" \
"global_store_dwordx4 v106, v[76:79], s[48:49] offset:512 sc1\n" \
"global_store_dwordx4 v105, v[64:67], s[68:69] offset:0 sc1\n" \
"global_store_dwordx4 v105, v[68:71], s[68:69] offset:512 sc1\n" \
"global_store_dwordx4 v106, v[72:75], s[68:69] offset:0 sc1\n" \
"global_store_dwordx4 v106, v[76:79], s[68:69] offset:512 sc1\n" \
"s_waitcnt vmcnt(0)\n" \
"v_mov_b32 v80, 64\n" \
"s_mov_b64 exec, 1\n" \
"global_store_dword v107, v80, s[52:53] sc1\n" \
"s_mov_b64 exec, -1\n" \
".Lend%=:\n" \
"s_endpgm\n" \
".Lhb_s%=:\n" \
"s_sub_u32 s73, s73, 1\n" \
"s_cbranch_scc1 .Lend%=\n" \
"global_load_dword v152, v150, s[54:55] sc1\n" \
"s_waitcnt vmcnt(0)\n" \
"s_branch .Lhb_c%=\n" \
".Lln_s%=:\n" \
"s_sub_u32 s73, s73, 1\n" \
"s_cbranch_scc1 .Lend%=\n" \
"s_sleep 4\n" \
"global_load_dword v64, v107, s[54:55] sc1\n" \
"s_waitcnt vmcnt(0)\n" \
"s_branch .Lln_c%=\n" \
".Lrdy_s%=:\n" \
"s_sub_u32 s73, s73, 1\n" \
"s_cbranch_scc1 .Lend%=\n" \
"s_sleep 4\n" \
"global_load_dword v65, v66, s[54:55] sc1\n" \
"s_waitcnt vmcnt(0)\n" \
"s_branch .Lrdy_c%=\n" \
".Lout%=:\n"

#define Q_ASM \
"s_mov_b32 s73, 2000000\n" \
"s_mov_b32 s75, 0xbfb8aa3b\n" \
"v_mov_b32 v108, v100\n" \
"v_add_u32 v109, 0x10000, v100\n" \
"v_add_u32 v110, 0x20000, v100\n" \
"v_min_u32 v66, 68, v107\n" \
"v_add_u32 v66, 10368, v66\n" \
"v_mov_b32 v150, s65\n" \
"global_load_dword v151, v150, s[54:55] sc1\n" \
"s_memrealtime s[76:77]\n" \
"s_waitcnt lgkmcnt(0)\n" \
".Ldly%=:\n" \
"s_memrealtime s[78:79]\n" \
"s_waitcnt lgkmcnt(0)\n" \
"s_sub_u32 s78, s78, s76\n" \
"s_cmp_lt_u32 s78, s64\n" \
"s_cbranch_scc1 .Ldly%=\n" \
"s_add_u32 m0, s56, 0\n" \
"s_nop 0\n" \
"global_load_lds_dwordx4 v96, s[40:41] offset:0\n" \
"global_load_lds_dwordx4 v97, s[40:41] offset:1024\n" \
"global_load_lds_dwordx4 v98, s[40:41] offset:2048\n" \
"global_load_lds_dwordx4 v99, s[40:41] offset:3072\n" \
"s_add_u32 s40, s40, s44\n" \
"s_addc_u32 s41, s41, 0\n" \
"s_add_u32 m0, s56, 16384\n" \
"s_nop 0\n" \
"global_load_lds_dwordx4 v96, s[40:41] offset:0\n" \
"global_load_lds_dwordx4 v97, s[40:41] offset:1024\n" \
"global_load_lds_dwordx4 v98, s[40:41] offset:2048\n" \
"global_load_lds_dwordx4 v99, s[40:41] offset:3072\n" \
"s_add_u32 s40, s40, s44\n" \
"s_addc_u32 s41, s41, 0\n" \
"s_add_u32 m0, s56, 32768\n" \
"s_nop 0\n" \
"global_load_lds_dwordx4 v96, s[40:41] offset:0\n" \
"global_load_lds_dwordx4 v97, s[40:41] offset:1024\n" \
"global_load_lds_dwordx4 v98, s[40:41] offset:2048\n" \
"global_load_lds_dwordx4 v99, s[40:41] offset:3072\n" \
"s_add_u32 s40, s40, s44\n" \
"s_addc_u32 s41, s41, 0\n" \
"s_add_u32 m0, s56, 49152\n" \
"s_nop 0\n" \
"global_load_lds_dwordx4 v96, s[40:41] offset:0\n" \
"global_load_lds_dwordx4 v97, s[40:41] offset:1024\n" \
"global_load_lds_dwordx4 v98, s[40:41] offset:2048\n" \
"global_load_lds_dwordx4 v99, s[40:41] offset:3072\n" \
"s_add_u32 s40, s40, s44\n" \
"s_addc_u32 s41, s41, 0\n" \
"s_add_u32 m0, s56, 65536\n" \
"s_nop 0\n" \
"global_load_lds_dwordx4 v96, s[40:41] offset:0\n" \
"global_load_lds_dwordx4 v97, s[40:41] offset:1024\n" \
"global_load_lds_dwordx4 v98, s[40:41] offset:2048\n" \
"global_load_lds_dwordx4 v99, s[40:41] offset:3072\n" \
"s_add_u32 s40, s40, s44\n" \
"s_addc_u32 s41, s41, 0\n" \
"s_add_u32 m0, s56, 81920\n" \
"s_nop 0\n" \
"global_load_lds_dwordx4 v96, s[40:41] offset:0\n" \
"global_load_lds_dwordx4 v97, s[40:41] offset:1024\n" \
"global_load_lds_dwordx4 v98, s[40:41] offset:2048\n" \
"global_load_lds_dwordx4 v99, s[40:41] offset:3072\n" \
"s_add_u32 s40, s40, s44\n" \
"s_addc_u32 s41, s41, 0\n" \
"s_add_u32 m0, s56, 98304\n" \
"s_nop 0\n" \
"global_load_lds_dwordx4 v96, s[40:41] offset:0\n" \
"global_load_lds_dwordx4 v97, s[40:41] offset:1024\n" \
"global_load_lds_dwordx4 v98, s[40:41] offset:2048\n" \
"global_load_lds_dwordx4 v99, s[40:41] offset:3072\n" \
"s_add_u32 s40, s40, s44\n" \
"s_addc_u32 s41, s41, 0\n" \
"s_add_u32 m0, s56, 114688\n" \
"s_nop 0\n" \
"global_load_lds_dwordx4 v96, s[40:41] offset:0\n" \
"global_load_lds_dwordx4 v97, s[40:41] offset:1024\n" \
"global_load_lds_dwordx4 v98, s[40:41] offset:2048\n" \
"global_load_lds_dwordx4 v99, s[40:41] offset:3072\n" \
"s_add_u32 s40, s40, s44\n" \
"s_addc_u32 s41, s41, 0\n" \
"s_add_u32 m0, s56, 131072\n" \
"s_nop 0\n" \
"global_load_lds_dwordx4 v96, s[40:41] offset:0\n" \
"global_load_lds_dwordx4 v97, s[40:41] offset:1024\n" \
"global_load_lds_dwordx4 v98, s[40:41] offset:2048\n" \
"global_load_lds_dwordx4 v99, s[40:41] offset:3072\n" \
"s_add_u32 s40, s40, s44\n" \
"s_addc_u32 s41, s41, 0\n" \
"s_waitcnt vmcnt(32)\n" \
"s_barrier\n" \
"ds_read_b64_tr_b16 a[0:1], v108 offset:0\n" \
"ds_read_b64_tr_b16 a[2:3], v108 offset:1024\n" \
"ds_read_b64_tr_b16 a[4:5], v108 offset:4096\n" \
"ds_read_b64_tr_b16 a[6:7], v108 offset:5120\n" \
"ds_read_b64_tr_b16 a[8:9], v108 offset:8192\n" \
"ds_read_b64_tr_b16 a[10:11], v108 offset:9216\n" \
"ds_read_b64_tr_b16 a[12:13], v108 offset:12288\n" \
"ds_read_b64_tr_b16 a[14:15], v108 offset:13312\n" \
"s_waitcnt lgkmcnt(0)\n" \
"s_waitcnt vmcnt(28)\n" \
"s_barrier\n" \
"s_add_u32 m0, s56, 0\n" \
"s_nop 0\n" \
"global_load_lds_dwordx4 v96, s[42:43] offset:0\n" \
"global_load_lds_dwordx4 v97, s[42:43] offset:1024\n" \
"global_load_lds_dwordx4 v98, s[42:43] offset:2048\n" \
"global_load_lds_dwordx4 v99, s[42:43] offset:3072\n" \
"s_add_u32 s42, s42, s44\n" \
"s_addc_u32 s43, s43, 0\n" \
"ds_read_b64_tr_b16 a[16:17], v108 offset:16384\n" \
"ds_read_b64_tr_b16 a[18:19], v108 offset:17408\n" \
"ds_read_b64_tr_b16 a[20:21], v108 offset:20480\n" \
"ds_read_b64_tr_b16 a[22:23], v108 offset:21504\n" \
"ds_read_b64_tr_b16 a[24:25], v108 offset:24576\n" \
"ds_read_b64_tr_b16 a[26:27], v108 offset:25600\n" \
"ds_read_b64_tr_b16 a[28:29], v108 offset:28672\n" \
"ds_read_b64_tr_b16 a[30:31], v108 offset:29696\n" \
"s_waitcnt lgkmcnt(0)\n" \
"s_waitcnt vmcnt(28)\n" \
"s_barrier\n" \
"s_add_u32 m0, s56, 16384\n" \
"s_nop 0\n" \
"global_load_lds_dwordx4 v96, s[42:43] offset:0\n" \
"global_load_lds_dwordx4 v97, s[42:43] offset:1024\n" \
"global_load_lds_dwordx4 v98, s[42:43] offset:2048\n" \
"global_load_lds_dwordx4 v99, s[42:43] offset:3072\n" \
"s_add_u32 s42, s42, s44\n" \
"s_addc_u32 s43, s43, 0\n" \
"ds_read_b64_tr_b16 a[32:33], v108 offset:32768\n" \
"ds_read_b64_tr_b16 a[34:35], v108 offset:33792\n" \
"ds_read_b64_tr_b16 a[36:37], v108 offset:36864\n" \
"ds_read_b64_tr_b16 a[38:39], v108 offset:37888\n" \
"ds_read_b64_tr_b16 a[40:41], v108 offset:40960\n" \
"ds_read_b64_tr_b16 a[42:43], v108 offset:41984\n" \
"ds_read_b64_tr_b16 a[44:45], v108 offset:45056\n" \
"ds_read_b64_tr_b16 a[46:47], v108 offset:46080\n" \
"s_waitcnt lgkmcnt(0)\n" \
"s_waitcnt vmcnt(28)\n" \
"s_barrier\n" \
"s_add_u32 m0, s56, 32768\n" \
"s_nop 0\n" \
"global_load_lds_dwordx4 v96, s[42:43] offset:0\n" \
"global_load_lds_dwordx4 v97, s[42:43] offset:1024\n" \
"global_load_lds_dwordx4 v98, s[42:43] offset:2048\n" \
"global_load_lds_dwordx4 v99, s[42:43] offset:3072\n" \
"s_add_u32 s42, s42, s44\n" \
"s_addc_u32 s43, s43, 0\n" \
"ds_read_b64_tr_b16 a[48:49], v108 offset:49152\n" \
"ds_read_b64_tr_b16 a[50:51], v108 offset:50176\n" \
"ds_read_b64_tr_b16 a[52:53], v108 offset:53248\n" \
"ds_read_b64_tr_b16 a[54:55], v108 offset:54272\n" \
"ds_read_b64_tr_b16 a[56:57], v108 offset:57344\n" \
"ds_read_b64_tr_b16 a[58:59], v108 offset:58368\n" \
"ds_read_b64_tr_b16 a[60:61], v108 offset:61440\n" \
"ds_read_b64_tr_b16 a[62:63], v108 offset:62464\n" \
"s_waitcnt lgkmcnt(0)\n" \
"s_waitcnt vmcnt(28)\n" \
"s_barrier\n" \
"s_add_u32 m0, s56, 49152\n" \
"s_nop 0\n" \
"global_load_lds_dwordx4 v96, s[42:43] offset:0\n" \
"global_load_lds_dwordx4 v97, s[42:43] offset:1024\n" \
"global_load_lds_dwordx4 v98, s[42:43] offset:2048\n" \
"global_load_lds_dwordx4 v99, s[42:43] offset:3072\n" \
"s_add_u32 s42, s42, s44\n" \
"s_addc_u32 s43, s43, 0\n" \
"ds_read_b64_tr_b16 a[64:65], v109 offset:0\n" \
"ds_read_b64_tr_b16 a[66:67], v109 offset:1024\n" \
"ds_read_b64_tr_b16 a[68:69], v109 offset:4096\n" \
"ds_read_b64_tr_b16 a[70:71], v109 offset:5120\n" \
"ds_read_b64_tr_b16 a[72:73], v109 offset:8192\n" \
"ds_read_b64_tr_b16 a[74:75], v109 offset:9216\n" \
"ds_read_b64_tr_b16 a[76:77], v109 offset:12288\n" \
"ds_read_b64_tr_b16 a[78:79], v109 offset:13312\n" \
"s_waitcnt lgkmcnt(0)\n" \
"s_waitcnt vmcnt(28)\n" \
"s_barrier\n" \
"s_add_u32 m0, s56, 65536\n" \
"s_nop 0\n" \
"global_load_lds_dwordx4 v96, s[42:43] offset:0\n" \
"global_load_lds_dwordx4 v97, s[42:43] offset:1024\n" \
"global_load_lds_dwordx4 v98, s[42:43] offset:2048\n" \
"global_load_lds_dwordx4 v99, s[42:43] offset:3072\n" \
"s_add_u32 s42, s42, s44\n" \
"s_addc_u32 s43, s43, 0\n" \
"ds_read_b64_tr_b16 a[80:81], v109 offset:16384\n" \
"ds_read_b64_tr_b16 a[82:83], v109 offset:17408\n" \
"ds_read_b64_tr_b16 a[84:85], v109 offset:20480\n" \
"ds_read_b64_tr_b16 a[86:87], v109 offset:21504\n" \
"ds_read_b64_tr_b16 a[88:89], v109 offset:24576\n" \
"ds_read_b64_tr_b16 a[90:91], v109 offset:25600\n" \
"ds_read_b64_tr_b16 a[92:93], v109 offset:28672\n" \
"ds_read_b64_tr_b16 a[94:95], v109 offset:29696\n" \
"s_waitcnt lgkmcnt(0)\n" \
"s_waitcnt vmcnt(28)\n" \
"s_barrier\n" \
"s_add_u32 m0, s56, 81920\n" \
"s_nop 0\n" \
"global_load_lds_dwordx4 v96, s[42:43] offset:0\n" \
"global_load_lds_dwordx4 v97, s[42:43] offset:1024\n" \
"global_load_lds_dwordx4 v98, s[42:43] offset:2048\n" \
"global_load_lds_dwordx4 v99, s[42:43] offset:3072\n" \
"s_add_u32 s42, s42, s44\n" \
"s_addc_u32 s43, s43, 0\n" \
"ds_read_b64_tr_b16 a[96:97], v109 offset:32768\n" \
"ds_read_b64_tr_b16 a[98:99], v109 offset:33792\n" \
"ds_read_b64_tr_b16 a[100:101], v109 offset:36864\n" \
"ds_read_b64_tr_b16 a[102:103], v109 offset:37888\n" \
"ds_read_b64_tr_b16 a[104:105], v109 offset:40960\n" \
"ds_read_b64_tr_b16 a[106:107], v109 offset:41984\n" \
"ds_read_b64_tr_b16 a[108:109], v109 offset:45056\n" \
"ds_read_b64_tr_b16 a[110:111], v109 offset:46080\n" \
"s_waitcnt lgkmcnt(0)\n" \
"s_waitcnt vmcnt(28)\n" \
"s_barrier\n" \
"s_add_u32 m0, s56, 98304\n" \
"s_nop 0\n" \
"global_load_lds_dwordx4 v96, s[42:43] offset:0\n" \
"global_load_lds_dwordx4 v97, s[42:43] offset:1024\n" \
"global_load_lds_dwordx4 v98, s[42:43] offset:2048\n" \
"global_load_lds_dwordx4 v99, s[42:43] offset:3072\n" \
"s_add_u32 s42, s42, s44\n" \
"s_addc_u32 s43, s43, 0\n" \
"ds_read_b64_tr_b16 a[112:113], v109 offset:49152\n" \
"ds_read_b64_tr_b16 a[114:115], v109 offset:50176\n" \
"ds_read_b64_tr_b16 a[116:117], v109 offset:53248\n" \
"ds_read_b64_tr_b16 a[118:119], v109 offset:54272\n" \
"ds_read_b64_tr_b16 a[120:121], v109 offset:57344\n" \
"ds_read_b64_tr_b16 a[122:123], v109 offset:58368\n" \
"ds_read_b64_tr_b16 a[124:125], v109 offset:61440\n" \
"ds_read_b64_tr_b16 a[126:127], v109 offset:62464\n" \
"s_waitcnt lgkmcnt(0)\n" \
"s_waitcnt vmcnt(28)\n" \
"s_barrier\n" \
"global_load_dword v152, v150, s[54:55] sc1\n" \
"s_add_u32 m0, s56, 114688\n" \
"s_nop 0\n" \
"global_load_lds_dwordx4 v96, s[42:43] offset:0\n" \
"global_load_lds_dwordx4 v97, s[42:43] offset:1024\n" \
"global_load_lds_dwordx4 v98, s[42:43] offset:2048\n" \
"global_load_lds_dwordx4 v99, s[42:43] offset:3072\n" \
"s_add_u32 s42, s42, s44\n" \
"s_addc_u32 s43, s43, 0\n" \
"ds_read_b64_tr_b16 a[128:129], v110 offset:0\n" \
"ds_read_b64_tr_b16 a[130:131], v110 offset:1024\n" \
"ds_read_b64_tr_b16 a[132:133], v110 offset:4096\n" \
"ds_read_b64_tr_b16 a[134:135], v110 offset:5120\n" \
"ds_read_b64_tr_b16 a[136:137], v110 offset:8192\n" \
"ds_read_b64_tr_b16 a[138:139], v110 offset:9216\n" \
"ds_read_b64_tr_b16 a[140:141], v110 offset:12288\n" \
"ds_read_b64_tr_b16 a[142:143], v110 offset:13312\n" \
"s_waitcnt lgkmcnt(0)\n" \
"s_waitcnt vmcnt(29)\n" \
"s_barrier\n" \
"s_add_u32 m0, s56, 131072\n" \
"s_nop 0\n" \
"global_load_lds_dwordx4 v96, s[42:43] offset:0\n" \
"global_load_lds_dwordx4 v97, s[42:43] offset:1024\n" \
"global_load_lds_dwordx4 v98, s[42:43] offset:2048\n" \
"global_load_lds_dwordx4 v99, s[42:43] offset:3072\n" \
"s_add_u32 s42, s42, s44\n" \
"s_addc_u32 s43, s43, 0\n" \
"ds_read_b64_tr_b16 a[144:145], v108 offset:0\n" \
"ds_read_b64_tr_b16 a[146:147], v108 offset:1024\n" \
"ds_read_b64_tr_b16 a[148:149], v108 offset:4096\n" \
"ds_read_b64_tr_b16 a[150:151], v108 offset:5120\n" \
"ds_read_b64_tr_b16 a[152:153], v108 offset:8192\n" \
"ds_read_b64_tr_b16 a[154:155], v108 offset:9216\n" \
"ds_read_b64_tr_b16 a[156:157], v108 offset:12288\n" \
"ds_read_b64_tr_b16 a[158:159], v108 offset:13312\n" \
"s_waitcnt lgkmcnt(0)\n" \
"s_waitcnt vmcnt(29)\n" \
"s_barrier\n" \
"ds_read_b64_tr_b16 a[160:161], v108 offset:16384\n" \
"ds_read_b64_tr_b16 a[162:163], v108 offset:17408\n" \
"ds_read_b64_tr_b16 a[164:165], v108 offset:20480\n" \
"ds_read_b64_tr_b16 a[166:167], v108 offset:21504\n" \
"ds_read_b64_tr_b16 a[168:169], v108 offset:24576\n" \
"ds_read_b64_tr_b16 a[170:171], v108 offset:25600\n" \
"ds_read_b64_tr_b16 a[172:173], v108 offset:28672\n" \
"ds_read_b64_tr_b16 a[174:175], v108 offset:29696\n" \
"s_waitcnt lgkmcnt(0)\n" \
"s_waitcnt vmcnt(25)\n" \
"s_barrier\n" \
"ds_read_b64_tr_b16 a[176:177], v108 offset:32768\n" \
"ds_read_b64_tr_b16 a[178:179], v108 offset:33792\n" \
"ds_read_b64_tr_b16 a[180:181], v108 offset:36864\n" \
"ds_read_b64_tr_b16 a[182:183], v108 offset:37888\n" \
"ds_read_b64_tr_b16 a[184:185], v108 offset:40960\n" \
"ds_read_b64_tr_b16 a[186:187], v108 offset:41984\n" \
"ds_read_b64_tr_b16 a[188:189], v108 offset:45056\n" \
"ds_read_b64_tr_b16 a[190:191], v108 offset:46080\n" \
"s_waitcnt lgkmcnt(0)\n" \
"s_waitcnt vmcnt(21)\n" \
"s_barrier\n" \
"ds_read_b64_tr_b16 a[192:193], v108 offset:49152\n" \
"ds_read_b64_tr_b16 a[194:195], v108 offset:50176\n" \
"ds_read_b64_tr_b16 a[196:197], v108 offset:53248\n" \
"ds_read_b64_tr_b16 a[198:199], v108 offset:54272\n" \
"ds_read_b64_tr_b16 a[200:201], v108 offset:57344\n" \
"ds_read_b64_tr_b16 a[202:203], v108 offset:58368\n" \
"ds_read_b64_tr_b16 a[204:205], v108 offset:61440\n" \
"ds_read_b64_tr_b16 a[206:207], v108 offset:62464\n" \
"s_waitcnt lgkmcnt(0)\n" \
"s_waitcnt vmcnt(17)\n" \
"s_barrier\n" \
"ds_read_b64_tr_b16 a[208:209], v109 offset:0\n" \
"ds_read_b64_tr_b16 a[210:211], v109 offset:1024\n" \
"ds_read_b64_tr_b16 a[212:213], v109 offset:4096\n" \
"ds_read_b64_tr_b16 a[214:215], v109 offset:5120\n" \
"ds_read_b64_tr_b16 a[216:217], v109 offset:8192\n" \
"ds_read_b64_tr_b16 a[218:219], v109 offset:9216\n" \
"ds_read_b64_tr_b16 a[220:221], v109 offset:12288\n" \
"ds_read_b64_tr_b16 a[222:223], v109 offset:13312\n" \
"s_waitcnt lgkmcnt(0)\n" \
"s_waitcnt vmcnt(13)\n" \
"s_barrier\n" \
"ds_read_b64_tr_b16 a[224:225], v109 offset:16384\n" \
"ds_read_b64_tr_b16 a[226:227], v109 offset:17408\n" \
"ds_read_b64_tr_b16 a[228:229], v109 offset:20480\n" \
"ds_read_b64_tr_b16 a[230:231], v109 offset:21504\n" \
"ds_read_b64_tr_b16 a[232:233], v109 offset:24576\n" \
"ds_read_b64_tr_b16 a[234:235], v109 offset:25600\n" \
"ds_read_b64_tr_b16 a[236:237], v109 offset:28672\n" \
"ds_read_b64_tr_b16 a[238:239], v109 offset:29696\n" \
"s_waitcnt lgkmcnt(0)\n" \
"s_waitcnt vmcnt(9)\n" \
"s_barrier\n" \
"ds_read_b64_tr_b16 a[240:241], v109 offset:32768\n" \
"ds_read_b64_tr_b16 a[242:243], v109 offset:33792\n" \
"ds_read_b64_tr_b16 a[244:245], v109 offset:36864\n" \
"ds_read_b64_tr_b16 a[246:247], v109 offset:37888\n" \
"ds_read_b64_tr_b16 a[248:249], v109 offset:40960\n" \
"ds_read_b64_tr_b16 a[250:251], v109 offset:41984\n" \
"ds_read_b64_tr_b16 a[252:253], v109 offset:45056\n" \
"ds_read_b64_tr_b16 a[254:255], v109 offset:46080\n" \
"s_waitcnt lgkmcnt(0)\n" \
"s_waitcnt vmcnt(4)\n" \
"s_barrier\n" \
".Lhb_c%=:\n" \
"v_cmp_eq_u32 vcc, v151, v152\n" \
"s_nop 4\n" \
"s_cbranch_vccnz .Lhb_s%=\n" \
"global_load_dword v64, v107, s[54:55] sc1\n" \
"global_load_dword v65, v66, s[54:55] sc1\n" \
"ds_read_b64_tr_b16 v[224:225], v109 offset:49152\n" \
"ds_read_b64_tr_b16 v[226:227], v109 offset:50176\n" \
"ds_read_b64_tr_b16 v[228:229], v109 offset:53248\n" \
"ds_read_b64_tr_b16 v[230:231], v109 offset:54272\n" \
"ds_read_b64_tr_b16 v[232:233], v109 offset:57344\n" \
"ds_read_b64_tr_b16 v[234:235], v109 offset:58368\n" \
"ds_read_b64_tr_b16 v[236:237], v109 offset:61440\n" \
"ds_read_b64_tr_b16 v[238:239], v109 offset:62464\n" \
"s_waitcnt lgkmcnt(0)\n" \
"s_waitcnt vmcnt(2)\n" \
"s_barrier\n" \
"ds_read_b64_tr_b16 v[240:241], v110 offset:0\n" \
"ds_read_b64_tr_b16 v[242:243], v110 offset:1024\n" \
"ds_read_b64_tr_b16 v[244:245], v110 offset:4096\n" \
"ds_read_b64_tr_b16 v[246:247], v110 offset:5120\n" \
"ds_read_b64_tr_b16 v[248:249], v110 offset:8192\n" \
"ds_read_b64_tr_b16 v[250:251], v110 offset:9216\n" \
"ds_read_b64_tr_b16 v[252:253], v110 offset:12288\n" \
"ds_read_b64_tr_b16 v[254:255], v110 offset:13312\n" \
"s_waitcnt lgkmcnt(0)\n" \
"s_barrier\n" \
"s_waitcnt vmcnt(0)\n" \
".Lln_c%=:\n" \
"v_cmp_gt_u32 vcc, 1, v64\n" \
"s_cbranch_vccnz .Lln_s%=\n" \
".Lrdy_c%=:\n" \
"v_cmp_gt_u32 vcc, 1, v65\n" \
"s_cbranch_vccnz .Lrdy_s%=\n" \
"v_min_u32_dpp v65, v65, v65 row_shr:1 row_mask:0xf bank_mask:0xf\n" \
"s_nop 1\n" \
"v_min_u32_dpp v65, v65, v65 row_shr:2 row_mask:0xf bank_mask:0xf\n" \
"s_nop 1\n" \
"v_min_u32_dpp v65, v65, v65 row_shr:4 row_mask:0xf bank_mask:0xf\n" \
"s_nop 1\n" \
"v_min_u32_dpp v65, v65, v65 row_shr:8 row_mask:0xf bank_mask:0xf\n" \
"s_nop 0\n" \
"v_readlane_b32 s76, v65, 15\n" \
"v_readlane_b32 s77, v65, 31\n" \
"s_min_u32 s76, s76, s77\n" \
"s_and_b32 s76, s76, 1\n" \
"s_lshl_b32 s76, s76, 13\n" \
"s_xor_b32 s48, s48, s76\n" \
"s_xor_b32 s66, s66, s76\n" \
"s_xor_b32 s68, s48, 0x2000\n" \
"s_mov_b32 s69, s49\n" \
"s_mov_b32 s60, s57\n" \
"s_add_u32 s61, s57, 147456\n" \
"s_mov_b32 s62, 0\n" \
"s_mov_b32 s63, 0\n" \
"s_mov_b32 s64, 0\n" \
"s_add_u32 m0, s60, 0\n" \
"s_nop 0\n" \
"global_load_lds_dwordx4 v102, s[46:47] offset:0 sc1\n" \
"global_load_lds_dwordx4 v102, s[46:47] offset:1024 sc1\n" \
"global_load_lds_dwordx4 v102, s[46:47] offset:2048 sc1\n" \
"global_load_lds_dwordx4 v102, s[46:47] offset:3072 sc1\n" \
"s_add_u32 m0, s60, 4096\n" \
"s_nop 0\n" \
"global_load_lds_dwordx4 v103, s[46:47] offset:0 sc1\n" \
"global_load_lds_dwordx4 v103, s[46:47] offset:1024 sc1\n" \
"global_load_lds_dwordx4 v103, s[46:47] offset:2048 sc1\n" \
"global_load_lds_dwordx4 v103, s[46:47] offset:3072 sc1\n" \
"s_add_u32 m0, s60, 8192\n" \
"s_nop 0\n" \
"global_load_lds_dwordx4 v104, s[46:47] offset:0 sc1\n" \
"s_add_u32 s46, s46, s45\n" \
"s_addc_u32 s47, s47, 0\n" \
"s_add_u32 s60, s60, 36864\n" \
"s_cmp_eq_u32 s60, s61\n" \
"s_cselect_b32 s60, s57, s60\n" \
"s_add_u32 m0, s60, 0\n" \
"s_nop 0\n" \
"global_load_lds_dwordx4 v102, s[46:47] offset:0 sc1\n" \
"global_load_lds_dwordx4 v102, s[46:47] offset:1024 sc1\n" \
"global_load_lds_dwordx4 v102, s[46:47] offset:2048 sc1\n" \
"global_load_lds_dwordx4 v102, s[46:47] offset:3072 sc1\n" \
"s_add_u32 m0, s60, 4096\n" \
"s_nop 0\n" \
"global_load_lds_dwordx4 v103, s[46:47] offset:0 sc1\n" \
"global_load_lds_dwordx4 v103, s[46:47] offset:1024 sc1\n" \
"global_load_lds_dwordx4 v103, s[46:47] offset:2048 sc1\n" \
"global_load_lds_dwordx4 v103, s[46:47] offset:3072 sc1\n" \
"s_add_u32 m0, s60, 8192\n" \
"s_nop 0\n" \
"global_load_lds_dwordx4 v104, s[46:47] offset:0 sc1\n" \
"s_add_u32 s46, s46, s45\n" \
"s_addc_u32 s47, s47, 0\n" \
"s_add_u32 s60, s60, 36864\n" \
"s_cmp_eq_u32 s60, s61\n" \
"s_cselect_b32 s60, s57, s60\n" \
"s_add_u32 m0, s60, 0\n" \
"s_nop 0\n" \
"global_load_lds_dwordx4 v102, s[46:47] offset:0 sc1\n" \
"global_load_lds_dwordx4 v102, s[46:47] offset:1024 sc1\n" \
"global_load_lds_dwordx4 v102, s[46:47] offset:2048 sc1\n" \
"global_load_lds_dwordx4 v102, s[46:47] offset:3072 sc1\n" \
"s_add_u32 m0, s60, 4096\n" \
"s_nop 0\n" \
"global_load_lds_dwordx4 v103, s[46:47] offset:0 sc1\n" \
"global_load_lds_dwordx4 v103, s[46:47] offset:1024 sc1\n" \
"global_load_lds_dwordx4 v103, s[46:47] offset:2048 sc1\n" \
"global_load_lds_dwordx4 v103, s[46:47] offset:3072 sc1\n" \
"s_add_u32 m0, s60, 8192\n" \
"s_nop 0\n" \
"global_load_lds_dwordx4 v104, s[46:47] offset:0 sc1\n" \
"s_add_u32 s46, s46, s45\n" \
"s_addc_u32 s47, s47, 0\n" \
"s_add_u32 s60, s60, 36864\n" \
"s_cmp_eq_u32 s60, s61\n" \
"s_cselect_b32 s60, s57, s60\n" \
"v_mov_b32 v111, v101\n" \
"v_lshlrev_b32 v148, 1, v107\n" \
"v_mov_b32 v144, -1\n" \
"v_mov_b32 v145, -1\n" \
"v_mov_b32 v146, -1\n" \
"v_mov_b32 v147, -1\n" \
"s_waitcnt vmcnt(18)\n" \
"s_barrier\n" \
"ds_read_b128 v[32:35], v111 offset:0\n" \
"ds_read_b128 v[36:39], v111 offset:1024\n" \
"ds_read_b128 v[40:43], v111 offset:2048\n" \
"ds_read_b128 v[44:47], v111 offset:3072\n" \
"ds_read_b128 v[48:51], v111 offset:4096\n" \
"ds_read_b128 v[52:55], v111 offset:5120\n" \
"ds_read_b128 v[56:59], v111 offset:6144\n" \
"ds_read_b128 v[60:63], v111 offset:7168\n" \
"s_waitcnt lgkmcnt(7)\n" \
"v_mfma_f32_32x32x16_bf16 v[0:15], a[0:3], v[32:35], 0\n" \
"v_mfma_f32_32x32x16_bf16 v[16:31], a[144:147], v[32:35], 0\n" \
"s_waitcnt lgkmcnt(6)\n" \
"ds_read_b128 v[32:35], v111 offset:8192\n" \
"v_mfma_f32_32x32x16_bf16 v[0:15], a[4:7], v[36:39], v[0:15]\n" \
"v_mfma_f32_32x32x16_bf16 v[16:31], a[148:151], v[36:39], v[16:31]\n" \
"s_waitcnt lgkmcnt(6)\n" \
"ds_read_b128 v[36:39], v111 offset:9216\n" \
"v_mfma_f32_32x32x16_bf16 v[0:15], a[8:11], v[40:43], v[0:15]\n" \
"v_mfma_f32_32x32x16_bf16 v[16:31], a[152:155], v[40:43], v[16:31]\n" \
"s_waitcnt lgkmcnt(6)\n" \
"ds_read_b128 v[40:43], v111 offset:10240\n" \
"v_mfma_f32_32x32x16_bf16 v[0:15], a[12:15], v[44:47], v[0:15]\n" \
"v_mfma_f32_32x32x16_bf16 v[16:31], a[156:159], v[44:47], v[16:31]\n" \
"s_waitcnt lgkmcnt(6)\n" \
"ds_read_b128 v[44:47], v111 offset:11264\n" \
"v_mfma_f32_32x32x16_bf16 v[0:15], a[16:19], v[48:51], v[0:15]\n" \
"v_mfma_f32_32x32x16_bf16 v[16:31], a[160:163], v[48:51], v[16:31]\n" \
"s_waitcnt lgkmcnt(6)\n" \
"ds_read_b128 v[48:51], v111 offset:12288\n" \
"v_mfma_f32_32x32x16_bf16 v[0:15], a[20:23], v[52:55], v[0:15]\n" \
"v_mfma_f32_32x32x16_bf16 v[16:31], a[164:167], v[52:55], v[16:31]\n" \
"s_waitcnt lgkmcnt(6)\n" \
"ds_read_b128 v[52:55], v111 offset:13312\n" \
"v_mfma_f32_32x32x16_bf16 v[0:15], a[24:27], v[56:59], v[0:15]\n" \
"v_mfma_f32_32x32x16_bf16 v[16:31], a[168:171], v[56:59], v[16:31]\n" \
"s_waitcnt lgkmcnt(6)\n" \
"ds_read_b128 v[56:59], v111 offset:14336\n" \
"v_mfma_f32_32x32x16_bf16 v[0:15], a[28:31], v[60:63], v[0:15]\n" \
"s_add_u32 m0, s60, 0\n" \
"s_nop 0\n" \
"global_load_lds_dwordx4 v102, s[46:47] offset:0 sc1\n" \
"v_mfma_f32_32x32x16_bf16 v[16:31], a[172:175], v[60:63], v[16:31]\n" \
"s_waitcnt lgkmcnt(6)\n" \
"ds_read_b128 v[60:63], v111 offset:15360\n" \
"v_mfma_f32_32x32x16_bf16 v[0:15], a[32:35], v[32:35], v[0:15]\n" \
"v_mfma_f32_32x32x16_bf16 v[16:31], a[176:179], v[32:35], v[16:31]\n" \
"s_waitcnt lgkmcnt(6)\n" \
"ds_read_b128 v[32:35], v111 offset:16384\n" \
"v_mfma_f32_32x32x16_bf16 v[0:15], a[36:39], v[36:39], v[0:15]\n" \
"global_load_lds_dwordx4 v102, s[46:47] offset:1024 sc1\n" \
"v_mfma_f32_32x32x16_bf16 v[16:31], a[180:183], v[36:39], v[16:31]\n" \
"s_waitcnt lgkmcnt(6)\n" \
"ds_read_b128 v[36:39], v111 offset:17408\n" \
"v_mfma_f32_32x32x16_bf16 v[0:15], a[40:43], v[40:43], v[0:15]\n" \
"v_mfma_f32_32x32x16_bf16 v[16:31], a[184:187], v[40:43], v[16:31]\n" \
"s_waitcnt lgkmcnt(6)\n" \
"ds_read_b128 v[40:43], v111 offset:18432\n" \
"v_mfma_f32_32x32x16_bf16 v[0:15], a[44:47], v[44:47], v[0:15]\n" \
"global_load_lds_dwordx4 v102, s[46:47] offset:2048 sc1\n" \
"v_mfma_f32_32x32x16_bf16 v[16:31], a[188:191], v[44:47], v[16:31]\n" \
"s_waitcnt lgkmcnt(6)\n" \
"ds_read_b128 v[44:47], v111 offset:19456\n" \
"v_mfma_f32_32x32x16_bf16 v[0:15], a[48:51], v[48:51], v[0:15]\n" \
"v_mfma_f32_32x32x16_bf16 v[16:31], a[192:195], v[48:51], v[16:31]\n" \
"s_waitcnt lgkmcnt(6)\n" \
"ds_read_b128 v[48:51], v111 offset:20480\n" \
"v_mfma_f32_32x32x16_bf16 v[0:15], a[52:55], v[52:55], v[0:15]\n" \
"global_load_lds_dwordx4 v102, s[46:47] offset:3072 sc1\n" \
"v_mfma_f32_32x32x16_bf16 v[16:31], a[196:199], v[52:55], v[16:31]\n" \
"s_waitcnt lgkmcnt(6)\n" \
"ds_read_b128 v[52:55], v111 offset:21504\n" \
"v_mfma_f32_32x32x16_bf16 v[0:15], a[56:59], v[56:59], v[0:15]\n" \
"v_mfma_f32_32x32x16_bf16 v[16:31], a[200:203], v[56:59], v[16:31]\n" \
"s_waitcnt lgkmcnt(6)\n" \
"ds_read_b128 v[56:59], v111 offset:22528\n" \
"v_mfma_f32_32x32x16_bf16 v[0:15], a[60:63], v[60:63], v[0:15]\n" \
"s_add_u32 m0, s60, 4096\n" \
"s_nop 0\n" \
"global_load_lds_dwordx4 v103, s[46:47] offset:0 sc1\n" \
"v_mfma_f32_32x32x16_bf16 v[16:31], a[204:207], v[60:63], v[16:31]\n" \
"s_waitcnt lgkmcnt(6)\n" \
"ds_read_b128 v[60:63], v111 offset:23552\n" \
"v_mfma_f32_32x32x16_bf16 v[0:15], a[64:67], v[32:35], v[0:15]\n" \
"v_mfma_f32_32x32x16_bf16 v[16:31], a[208:211], v[32:35], v[16:31]\n" \
"s_waitcnt lgkmcnt(6)\n" \
"ds_read_b128 v[32:35], v111 offset:24576\n" \
"v_mfma_f32_32x32x16_bf16 v[0:15], a[68:71], v[36:39], v[0:15]\n" \
"global_load_lds_dwordx4 v103, s[46:47] offset:1024 sc1\n" \
"v_mfma_f32_32x32x16_bf16 v[16:31], a[212:215], v[36:39], v[16:31]\n" \
"s_waitcnt lgkmcnt(6)\n" \
"ds_read_b128 v[36:39], v111 offset:25600\n" \
"v_mfma_f32_32x32x16_bf16 v[0:15], a[72:75], v[40:43], v[0:15]\n" \
"v_mfma_f32_32x32x16_bf16 v[16:31], a[216:219], v[40:43], v[16:31]\n" \
"s_waitcnt lgkmcnt(6)\n" \
"ds_read_b128 v[40:43], v111 offset:26624\n" \
"v_mfma_f32_32x32x16_bf16 v[0:15], a[76:79], v[44:47], v[0:15]\n" \
"global_load_lds_dwordx4 v103, s[46:47] offset:2048 sc1\n" \
"v_mfma_f32_32x32x16_bf16 v[16:31], a[220:223], v[44:47], v[16:31]\n" \
"s_waitcnt lgkmcnt(6)\n" \
"ds_read_b128 v[44:47], v111 offset:27648\n" \
"v_mfma_f32_32x32x16_bf16 v[0:15], a[80:83], v[48:51], v[0:15]\n" \
"s_waitcnt vmcnt(7)\n" \
"s_barrier\n" \
"v_mfma_f32_32x32x16_bf16 v[16:31], a[224:227], v[48:51], v[16:31]\n" \
"s_waitcnt lgkmcnt(6)\n" \
"ds_read_b128 v[48:51], v111 offset:28672\n" \
"v_mfma_f32_32x32x16_bf16 v[0:15], a[84:87], v[52:55], v[0:15]\n" \
"global_load_lds_dwordx4 v103, s[46:47] offset:3072 sc1\n" \
"v_mfma_f32_32x32x16_bf16 v[16:31], a[228:231], v[52:55], v[16:31]\n" \
"s_waitcnt lgkmcnt(6)\n" \
"ds_read_b128 v[52:55], v111 offset:29696\n" \
"v_mfma_f32_32x32x16_bf16 v[0:15], a[88:91], v[56:59], v[0:15]\n" \
"v_mfma_f32_32x32x16_bf16 v[16:31], a[232:235], v[56:59], v[16:31]\n" \
"s_waitcnt lgkmcnt(6)\n" \
"ds_read_b128 v[56:59], v111 offset:30720\n" \
"v_mfma_f32_32x32x16_bf16 v[0:15], a[92:95], v[60:63], v[0:15]\n" \
"s_add_u32 m0, s60, 8192\n" \
"s_nop 0\n" \
"global_load_lds_dwordx4 v104, s[46:47] offset:0 sc1\n" \
"s_add_u32 s46, s46, s45\n" \
"s_addc_u32 s47, s47, 0\n" \
"v_mfma_f32_32x32x16_bf16 v[16:31], a[236:239], v[60:63], v[16:31]\n" \
"s_waitcnt lgkmcnt(6)\n" \
"ds_read_b128 v[60:63], v111 offset:31744\n" \
"v_mfma_f32_32x32x16_bf16 v[0:15], a[96:99], v[32:35], v[0:15]\n" \
"s_add_u32 s60, s60, 36864\n" \
"v_mfma_f32_32x32x16_bf16 v[16:31], a[240:243], v[32:35], v[16:31]\n" \
"s_waitcnt lgkmcnt(6)\n" \
"ds_read_b128 v[32:35], v111 offset:32768\n" \
"v_mfma_f32_32x32x16_bf16 v[0:15], a[100:103], v[36:39], v[0:15]\n" \
"s_cmp_eq_u32 s60, s61\n" \
"s_cselect_b32 s60, s57, s60\n" \
"s_add_u32 s62, s62, 36864\n" \
"s_cmp_eq_u32 s62, 147456\n" \
"s_cselect_b32 s62, 0, s62\n" \
"v_mfma_f32_32x32x16_bf16 v[16:31], a[244:247], v[36:39], v[16:31]\n" \
"s_waitcnt lgkmcnt(6)\n" \
"ds_read_b128 v[36:39], v111 offset:33792\n" \
"v_mfma_f32_32x32x16_bf16 v[0:15], a[104:107], v[40:43], v[0:15]\n" \
"v_mfma_f32_32x32x16_bf16 v[16:31], a[248:251], v[40:43], v[16:31]\n" \
"s_waitcnt lgkmcnt(6)\n" \
"ds_read_b128 v[40:43], v111 offset:34816\n" \
"v_mfma_f32_32x32x16_bf16 v[0:15], a[108:111], v[44:47], v[0:15]\n" \
"v_mfma_f32_32x32x16_bf16 v[16:31], a[252:255], v[44:47], v[16:31]\n" \
"s_waitcnt lgkmcnt(6)\n" \
"ds_read_b128 v[44:47], v111 offset:35840\n" \
"v_add_u32 v111, s62, v101\n" \
"v_mfma_f32_32x32x16_bf16 v[0:15], a[112:115], v[48:51], v[0:15]\n" \
"v_mfma_f32_32x32x16_bf16 v[16:31], v[224:227], v[48:51], v[16:31]\n" \
"s_waitcnt lgkmcnt(6)\n" \
"ds_read_b128 v[48:51], v111 offset:0\n" \
"v_mfma_f32_32x32x16_bf16 v[0:15], a[116:119], v[52:55], v[0:15]\n" \
"v_mfma_f32_32x32x16_bf16 v[16:31], v[228:231], v[52:55], v[16:31]\n" \
"s_waitcnt lgkmcnt(6)\n" \
"ds_read_b128 v[52:55], v111 offset:1024\n" \
"v_mfma_f32_32x32x16_bf16 v[0:15], a[120:123], v[56:59], v[0:15]\n" \
"v_mfma_f32_32x32x16_bf16 v[16:31], v[232:235], v[56:59], v[16:31]\n" \
"s_waitcnt lgkmcnt(6)\n" \
"ds_read_b128 v[56:59], v111 offset:2048\n" \
"v_mfma_f32_32x32x16_bf16 v[0:15], a[124:127], v[60:63], v[0:15]\n" \
"v_mfma_f32_32x32x16_bf16 v[16:31], v[236:239], v[60:63], v[16:31]\n" \
"s_waitcnt lgkmcnt(6)\n" \
"ds_read_b128 v[60:63], v111 offset:3072\n" \
"v_mfma_f32_32x32x16_bf16 v[0:15], a[128:131], v[32:35], v[0:15]\n" \
"v_mfma_f32_32x32x16_bf16 v[16:31], v[240:243], v[32:35], v[16:31]\n" \
"s_waitcnt lgkmcnt(6)\n" \
"ds_read_b128 v[32:35], v111 offset:4096\n" \
"v_mfma_f32_32x32x16_bf16 v[0:15], a[132:135], v[36:39], v[0:15]\n" \
"v_mfma_f32_32x32x16_bf16 v[16:31], v[244:247], v[36:39], v[16:31]\n" \
"s_waitcnt lgkmcnt(6)\n" \
"ds_read_b128 v[36:39], v111 offset:5120\n" \
"v_mfma_f32_32x32x16_bf16 v[0:15], a[136:139], v[40:43], v[0:15]\n" \
"v_mfma_f32_32x32x16_bf16 v[16:31], v[248:251], v[40:43], v[16:31]\n" \
"s_waitcnt lgkmcnt(6)\n" \
"ds_read_b128 v[40:43], v111 offset:6144\n" \
"v_mfma_f32_32x32x16_bf16 v[0:15], a[140:143], v[44:47], v[0:15]\n" \
"v_mfma_f32_32x32x16_bf16 v[16:31], v[252:255], v[44:47], v[16:31]\n" \
"s_waitcnt lgkmcnt(6)\n" \
"ds_read_b128 v[44:47], v111 offset:7168\n" \
".Ltop%=:\n" \
"v_mfma_f32_32x32x16_bf16 v[112:127], a[0:3], v[48:51], 0\n" \
"s_barrier\n" \
"v_mfma_f32_32x32x16_bf16 v[128:143], a[144:147], v[48:51], 0\n" \
"s_waitcnt lgkmcnt(6)\n" \
"ds_read_b128 v[48:51], v111 offset:8192\n" \
"v_mfma_f32_32x32x16_bf16 v[112:127], a[4:7], v[52:55], v[112:127]\n" \
"v_cvt_pk_bf16_f32 v64, v0, v1\n" \
"v_cvt_pk_bf16_f32 v65, v2, v3\n" \
"v_cvt_pk_bf16_f32 v66, v8, v9\n" \
"v_cvt_pk_bf16_f32 v67, v10, v11\n" \
"v_mfma_f32_32x32x16_bf16 v[128:143], a[148:151], v[52:55], v[128:143]\n" \
"s_waitcnt lgkmcnt(6)\n" \
"ds_read_b128 v[52:55], v111 offset:9216\n" \
"v_mfma_f32_32x32x16_bf16 v[112:127], a[8:11], v[56:59], v[112:127]\n" \
"global_store_dwordx4 v105, v[64:67], s[48:49] offset:0 sc1\n" \
"v_cvt_pk_bf16_f32 v68, v4, v5\n" \
"v_cvt_pk_bf16_f32 v69, v6, v7\n" \
"v_cvt_pk_bf16_f32 v70, v12, v13\n" \
"v_cvt_pk_bf16_f32 v71, v14, v15\n" \
"v_mfma_f32_32x32x16_bf16 v[128:143], a[152:155], v[56:59], v[128:143]\n" \
"s_waitcnt lgkmcnt(6)\n" \
"ds_read_b128 v[56:59], v111 offset:10240\n" \
"v_mfma_f32_32x32x16_bf16 v[112:127], a[12:15], v[60:63], v[112:127]\n" \
"global_store_dwordx4 v105, v[68:71], s[48:49] offset:512 sc1\n" \
"v_cvt_pk_bf16_f32 v72, v16, v17\n" \
"v_cvt_pk_bf16_f32 v73, v18, v19\n" \
"v_cvt_pk_bf16_f32 v74, v24, v25\n" \
"v_cvt_pk_bf16_f32 v75, v26, v27\n" \
"v_mfma_f32_32x32x16_bf16 v[128:143], a[156:159], v[60:63], v[128:143]\n" \
"s_waitcnt lgkmcnt(6)\n" \
"ds_read_b128 v[60:63], v111 offset:11264\n" \
"v_mfma_f32_32x32x16_bf16 v[112:127], a[16:19], v[32:35], v[112:127]\n" \
"global_store_dwordx4 v106, v[72:75], s[48:49] offset:0 sc1\n" \
"v_cvt_pk_bf16_f32 v76, v20, v21\n" \
"v_cvt_pk_bf16_f32 v77, v22, v23\n" \
"v_cvt_pk_bf16_f32 v78, v28, v29\n" \
"v_cvt_pk_bf16_f32 v79, v30, v31\n" \
"v_mfma_f32_32x32x16_bf16 v[128:143], a[160:163], v[32:35], v[128:143]\n" \
"s_waitcnt lgkmcnt(6)\n" \
"ds_read_b128 v[32:35], v111 offset:12288\n" \
"v_mfma_f32_32x32x16_bf16 v[112:127], a[20:23], v[36:39], v[112:127]\n" \
"global_store_dwordx4 v106, v[76:79], s[48:49] offset:512 sc1\n" \
"s_add_u32 s48, s48, s50\n" \
"s_addc_u32 s49, s49, 0\n" \
"v_mfma_f32_32x32x16_bf16 v[128:143], a[164:167], v[36:39], v[128:143]\n" \
"s_waitcnt lgkmcnt(6)\n" \
"ds_read_b128 v[36:39], v111 offset:13312\n" \
"v_mfma_f32_32x32x16_bf16 v[112:127], a[24:27], v[40:43], v[112:127]\n" \
"v_mfma_f32_32x32x16_bf16 v[128:143], a[168:171], v[40:43], v[128:143]\n" \
"s_waitcnt lgkmcnt(6)\n" \
"ds_read_b128 v[40:43], v111 offset:14336\n" \
"v_mfma_f32_32x32x16_bf16 v[112:127], a[28:31], v[44:47], v[112:127]\n" \
"s_add_u32 m0, s60, 0\n" \
"s_nop 0\n" \
"global_load_lds_dwordx4 v102, s[46:47] offset:0 sc1\n" \
"v_mfma_f32_32x32x16_bf16 v[128:143], a[172:175], v[44:47], v[128:143]\n" \
"s_waitcnt lgkmcnt(6)\n" \
"ds_read_b128 v[44:47], v111 offset:15360\n" \
"v_mfma_f32_32x32x16_bf16 v[112:127], a[32:35], v[48:51], v[112:127]\n" \
"v_mfma_f32_32x32x16_bf16 v[128:143], a[176:179], v[48:51], v[128:143]\n" \
"s_waitcnt lgkmcnt(6)\n" \
"ds_read_b128 v[48:51], v111 offset:16384\n" \
"v_mfma_f32_32x32x16_bf16 v[112:127], a[36:39], v[52:55], v[112:127]\n" \
"global_load_lds_dwordx4 v102, s[46:47] offset:1024 sc1\n" \
"v_mfma_f32_32x32x16_bf16 v[128:143], a[180:183], v[52:55], v[128:143]\n" \
"s_waitcnt lgkmcnt(6)\n" \
"ds_read_b128 v[52:55], v111 offset:17408\n" \
"v_mfma_f32_32x32x16_bf16 v[112:127], a[40:43], v[56:59], v[112:127]\n" \
"v_mfma_f32_32x32x16_bf16 v[128:143], a[184:187], v[56:59], v[128:143]\n" \
"s_waitcnt lgkmcnt(6)\n" \
"ds_read_b128 v[56:59], v111 offset:18432\n" \
"v_mfma_f32_32x32x16_bf16 v[112:127], a[44:47], v[60:63], v[112:127]\n" \
"global_load_lds_dwordx4 v102, s[46:47] offset:2048 sc1\n" \
"v_mfma_f32_32x32x16_bf16 v[128:143], a[188:191], v[60:63], v[128:143]\n" \
"s_waitcnt lgkmcnt(6)\n" \
"ds_read_b128 v[60:63], v111 offset:19456\n" \
"v_mfma_f32_32x32x16_bf16 v[112:127], a[48:51], v[32:35], v[112:127]\n" \
"v_mfma_f32_32x32x16_bf16 v[128:143], a[192:195], v[32:35], v[128:143]\n" \
"s_waitcnt lgkmcnt(6)\n" \
"ds_read_b128 v[32:35], v111 offset:20480\n" \
"v_mfma_f32_32x32x16_bf16 v[112:127], a[52:55], v[36:39], v[112:127]\n" \
"global_load_lds_dwordx4 v102, s[46:47] offset:3072 sc1\n" \
"v_mfma_f32_32x32x16_bf16 v[128:143], a[196:199], v[36:39], v[128:143]\n" \
"s_waitcnt lgkmcnt(6)\n" \
"ds_read_b128 v[36:39], v111 offset:21504\n" \
"v_mfma_f32_32x32x16_bf16 v[112:127], a[56:59], v[40:43], v[112:127]\n" \
"v_mfma_f32_32x32x16_bf16 v[128:143], a[200:203], v[40:43], v[128:143]\n" \
"s_waitcnt lgkmcnt(6)\n" \
"ds_read_b128 v[40:43], v111 offset:22528\n" \
"v_mfma_f32_32x32x16_bf16 v[112:127], a[60:63], v[44:47], v[112:127]\n" \
"s_add_u32 m0, s60, 4096\n" \
"s_nop 0\n" \
"global_load_lds_dwordx4 v103, s[46:47] offset:0 sc1\n" \
"v_mfma_f32_32x32x16_bf16 v[128:143], a[204:207], v[44:47], v[128:143]\n" \
"s_waitcnt lgkmcnt(6)\n" \
"ds_read_b128 v[44:47], v111 offset:23552\n" \
"v_mfma_f32_32x32x16_bf16 v[112:127], a[64:67], v[48:51], v[112:127]\n" \
"v_mfma_f32_32x32x16_bf16 v[128:143], a[208:211], v[48:51], v[128:143]\n" \
"s_waitcnt lgkmcnt(6)\n" \
"ds_read_b128 v[48:51], v111 offset:24576\n" \
"v_mfma_f32_32x32x16_bf16 v[112:127], a[68:71], v[52:55], v[112:127]\n" \
"global_load_lds_dwordx4 v103, s[46:47] offset:1024 sc1\n" \
"v_mfma_f32_32x32x16_bf16 v[128:143], a[212:215], v[52:55], v[128:143]\n" \
"s_waitcnt lgkmcnt(6)\n" \
"ds_read_b128 v[52:55], v111 offset:25600\n" \
"v_mfma_f32_32x32x16_bf16 v[112:127], a[72:75], v[56:59], v[112:127]\n" \
"v_mfma_f32_32x32x16_bf16 v[128:143], a[216:219], v[56:59], v[128:143]\n" \
"s_waitcnt lgkmcnt(6)\n" \
"ds_read_b128 v[56:59], v111 offset:26624\n" \
"v_mfma_f32_32x32x16_bf16 v[112:127], a[76:79], v[60:63], v[112:127]\n" \
"global_load_lds_dwordx4 v103, s[46:47] offset:2048 sc1\n" \
"v_mfma_f32_32x32x16_bf16 v[128:143], a[220:223], v[60:63], v[128:143]\n" \
"s_waitcnt lgkmcnt(6)\n" \
"ds_read_b128 v[60:63], v111 offset:27648\n" \
"v_mfma_f32_32x32x16_bf16 v[112:127], a[80:83], v[32:35], v[112:127]\n" \
"v_mfma_f32_32x32x16_bf16 v[128:143], a[224:227], v[32:35], v[128:143]\n" \
"s_waitcnt lgkmcnt(6)\n" \
"ds_read_b128 v[32:35], v111 offset:28672\n" \
"v_mfma_f32_32x32x16_bf16 v[112:127], a[84:87], v[36:39], v[112:127]\n" \
"global_load_lds_dwordx4 v103, s[46:47] offset:3072 sc1\n" \
"v_mfma_f32_32x32x16_bf16 v[128:143], a[228:231], v[36:39], v[128:143]\n" \
"s_waitcnt lgkmcnt(6)\n" \
"ds_read_b128 v[36:39], v111 offset:29696\n" \
"v_mfma_f32_32x32x16_bf16 v[112:127], a[88:91], v[40:43], v[112:127]\n" \
"v_mfma_f32_32x32x16_bf16 v[128:143], a[232:235], v[40:43], v[128:143]\n" \
"s_waitcnt lgkmcnt(6)\n" \
"ds_read_b128 v[40:43], v111 offset:30720\n" \
"v_mfma_f32_32x32x16_bf16 v[112:127], a[92:95], v[44:47], v[112:127]\n" \
"s_add_u32 m0, s60, 8192\n" \
"s_nop 0\n" \
"global_load_lds_dwordx4 v104, s[46:47] offset:0 sc1\n" \
"s_add_u32 s46, s46, s45\n" \
"s_addc_u32 s47, s47, 0\n" \
"v_mfma_f32_32x32x16_bf16 v[128:143], a[236:239], v[44:47], v[128:143]\n" \
"s_waitcnt lgkmcnt(6)\n" \
"ds_read_b128 v[44:47], v111 offset:31744\n" \
"v_mfma_f32_32x32x16_bf16 v[112:127], a[96:99], v[48:51], v[112:127]\n" \
"s_add_u32 s60, s60, 36864\n" \
"v_mfma_f32_32x32x16_bf16 v[128:143], a[240:243], v[48:51], v[128:143]\n" \
"s_waitcnt lgkmcnt(6)\n" \
"ds_read_b128 v[48:51], v111 offset:32768\n" \
"v_mfma_f32_32x32x16_bf16 v[112:127], a[100:103], v[52:55], v[112:127]\n" \
"s_cmp_eq_u32 s60, s61\n" \
"s_cselect_b32 s60, s57, s60\n" \
"s_add_u32 s62, s62, 36864\n" \
"s_cmp_eq_u32 s62, 147456\n" \
"s_cselect_b32 s62, 0, s62\n" \
"v_mfma_f32_32x32x16_bf16 v[128:143], a[244:247], v[52:55], v[128:143]\n" \
"s_waitcnt lgkmcnt(6)\n" \
"ds_read_b128 v[52:55], v111 offset:33792\n" \
"v_mfma_f32_32x32x16_bf16 v[112:127], a[104:107], v[56:59], v[112:127]\n" \
"v_mfma_f32_32x32x16_bf16 v[128:143], a[248:251], v[56:59], v[128:143]\n" \
"s_waitcnt lgkmcnt(6)\n" \
"ds_read_b128 v[56:59], v111 offset:34816\n" \
"v_mfma_f32_32x32x16_bf16 v[112:127], a[108:111], v[60:63], v[112:127]\n" \
"v_mfma_f32_32x32x16_bf16 v[128:143], a[252:255], v[60:63], v[128:143]\n" \
"s_waitcnt lgkmcnt(6)\n" \
"ds_read_b128 v[60:63], v111 offset:35840\n" \
"v_add_u32 v111, s62, v101\n" \
"v_mfma_f32_32x32x16_bf16 v[112:127], a[112:115], v[32:35], v[112:127]\n" \
"v_mfma_f32_32x32x16_bf16 v[128:143], v[224:227], v[32:35], v[128:143]\n" \
"s_waitcnt lgkmcnt(6)\n" \
"ds_read_b128 v[32:35], v111 offset:0\n" \
"v_mfma_f32_32x32x16_bf16 v[112:127], a[116:119], v[36:39], v[112:127]\n" \
"v_mfma_f32_32x32x16_bf16 v[128:143], v[228:231], v[36:39], v[128:143]\n" \
"s_waitcnt lgkmcnt(6)\n" \
"ds_read_b128 v[36:39], v111 offset:1024\n" \
"v_mov_b32 v149, s58\n" \
"v_mfma_f32_32x32x16_bf16 v[112:127], a[120:123], v[40:43], v[112:127]\n" \
"s_waitcnt vmcnt(9)\n" \
"s_mov_b64 exec, 1\n" \
"global_store_dword v107, v149, s[52:53] sc1\n" \
"s_mov_b64 exec, -1\n" \
"v_mfma_f32_32x32x16_bf16 v[128:143], v[232:235], v[40:43], v[128:143]\n" \
"s_waitcnt lgkmcnt(6)\n" \
"ds_read_b128 v[40:43], v111 offset:2048\n" \
"v_mfma_f32_32x32x16_bf16 v[112:127], a[124:127], v[44:47], v[112:127]\n" \
"s_mov_b32 s63, s58\n" \
"s_add_u32 s58, s58, s59\n" \
"v_mfma_f32_32x32x16_bf16 v[128:143], v[236:239], v[44:47], v[128:143]\n" \
"s_waitcnt lgkmcnt(6)\n" \
"ds_read_b128 v[44:47], v111 offset:3072\n" \
"v_mfma_f32_32x32x16_bf16 v[112:127], a[128:131], v[48:51], v[112:127]\n" \
"v_mfma_f32_32x32x16_bf16 v[128:143], v[240:243], v[48:51], v[128:143]\n" \
"s_waitcnt lgkmcnt(6)\n" \
"ds_read_b128 v[48:51], v111 offset:4096\n" \
"v_mfma_f32_32x32x16_bf16 v[112:127], a[132:135], v[52:55], v[112:127]\n" \
"v_mfma_f32_32x32x16_bf16 v[128:143], v[244:247], v[52:55], v[128:143]\n" \
"s_waitcnt lgkmcnt(6)\n" \
"ds_read_b128 v[52:55], v111 offset:5120\n" \
"v_mfma_f32_32x32x16_bf16 v[112:127], a[136:139], v[56:59], v[112:127]\n" \
"v_mfma_f32_32x32x16_bf16 v[128:143], v[248:251], v[56:59], v[128:143]\n" \
"s_waitcnt lgkmcnt(6)\n" \
"ds_read_b128 v[56:59], v111 offset:6144\n" \
"v_mfma_f32_32x32x16_bf16 v[112:127], a[140:143], v[60:63], v[112:127]\n" \
"s_sub_u32 s51, s51, 1\n" \
"s_cmp_eq_u32 s51, 0\n" \
"v_mfma_f32_32x32x16_bf16 v[128:143], v[252:255], v[60:63], v[128:143]\n" \
"s_waitcnt lgkmcnt(6)\n" \
"ds_read_b128 v[60:63], v111 offset:7168\n" \
"s_cbranch_scc1 .Lfin%=\n" \
"v_mfma_f32_32x32x16_bf16 v[0:15], a[0:3], v[32:35], 0\n" \
"s_barrier\n" \
"v_mfma_f32_32x32x16_bf16 v[16:31], a[144:147], v[32:35], 0\n" \
"s_waitcnt lgkmcnt(6)\n" \
"ds_read_b128 v[32:35], v111 offset:8192\n" \
"v_mfma_f32_32x32x16_bf16 v[0:15], a[4:7], v[36:39], v[0:15]\n" \
"v_cvt_pk_bf16_f32 v64, v112, v113\n" \
"v_cvt_pk_bf16_f32 v65, v114, v115\n" \
"v_cvt_pk_bf16_f32 v66, v120, v121\n" \
"v_cvt_pk_bf16_f32 v67, v122, v123\n" \
"v_mfma_f32_32x32x16_bf16 v[16:31], a[148:151], v[36:39], v[16:31]\n" \
"s_waitcnt lgkmcnt(6)\n" \
"ds_read_b128 v[36:39], v111 offset:9216\n" \
"v_mfma_f32_32x32x16_bf16 v[0:15], a[8:11], v[40:43], v[0:15]\n" \
"global_store_dwordx4 v105, v[64:67], s[48:49] offset:0 sc1\n" \
"v_cvt_pk_bf16_f32 v68, v116, v117\n" \
"v_cvt_pk_bf16_f32 v69, v118, v119\n" \
"v_cvt_pk_bf16_f32 v70, v124, v125\n" \
"v_cvt_pk_bf16_f32 v71, v126, v127\n" \
"v_mfma_f32_32x32x16_bf16 v[16:31], a[152:155], v[40:43], v[16:31]\n" \
"s_waitcnt lgkmcnt(6)\n" \
"ds_read_b128 v[40:43], v111 offset:10240\n" \
"v_mfma_f32_32x32x16_bf16 v[0:15], a[12:15], v[44:47], v[0:15]\n" \
"global_store_dwordx4 v105, v[68:71], s[48:49] offset:512 sc1\n" \
"v_cvt_pk_bf16_f32 v72, v128, v129\n" \
"v_cvt_pk_bf16_f32 v73, v130, v131\n" \
"v_cvt_pk_bf16_f32 v74, v136, v137\n" \
"v_cvt_pk_bf16_f32 v75, v138, v139\n" \
"v_mfma_f32_32x32x16_bf16 v[16:31], a[156:159], v[44:47], v[16:31]\n" \
"s_waitcnt lgkmcnt(6)\n" \
"ds_read_b128 v[44:47], v111 offset:11264\n" \
"v_mfma_f32_32x32x16_bf16 v[0:15], a[16:19], v[48:51], v[0:15]\n" \
"global_store_dwordx4 v106, v[72:75], s[48:49] offset:0 sc1\n" \
"v_cvt_pk_bf16_f32 v76, v132, v133\n" \
"v_cvt_pk_bf16_f32 v77, v134, v135\n" \
"v_cvt_pk_bf16_f32 v78, v140, v141\n" \
"v_cvt_pk_bf16_f32 v79, v142, v143\n" \
"v_mfma_f32_32x32x16_bf16 v[16:31], a[160:163], v[48:51], v[16:31]\n" \
"s_waitcnt lgkmcnt(6)\n" \
"ds_read_b128 v[48:51], v111 offset:12288\n" \
"v_mfma_f32_32x32x16_bf16 v[0:15], a[20:23], v[52:55], v[0:15]\n" \
"global_store_dwordx4 v106, v[76:79], s[48:49] offset:512 sc1\n" \
"s_add_u32 s48, s48, s50\n" \
"s_addc_u32 s49, s49, 0\n" \
"v_mfma_f32_32x32x16_bf16 v[16:31], a[164:167], v[52:55], v[16:31]\n" \
"s_waitcnt lgkmcnt(6)\n" \
"ds_read_b128 v[52:55], v111 offset:13312\n" \
"v_mfma_f32_32x32x16_bf16 v[0:15], a[24:27], v[56:59], v[0:15]\n" \
"v_mfma_f32_32x32x16_bf16 v[16:31], a[168:171], v[56:59], v[16:31]\n" \
"s_waitcnt lgkmcnt(6)\n" \
"ds_read_b128 v[56:59], v111 offset:14336\n" \
"v_mfma_f32_32x32x16_bf16 v[0:15], a[28:31], v[60:63], v[0:15]\n" \
"s_add_u32 m0, s60, 0\n" \
"s_nop 0\n" \
"global_load_lds_dwordx4 v102, s[46:47] offset:0 sc1\n" \
"v_mfma_f32_32x32x16_bf16 v[16:31], a[172:175], v[60:63], v[16:31]\n" \
"s_waitcnt lgkmcnt(6)\n" \
"ds_read_b128 v[60:63], v111 offset:15360\n" \
"v_mfma_f32_32x32x16_bf16 v[0:15], a[32:35], v[32:35], v[0:15]\n" \
"v_mfma_f32_32x32x16_bf16 v[16:31], a[176:179], v[32:35], v[16:31]\n" \
"s_waitcnt lgkmcnt(6)\n" \
"ds_read_b128 v[32:35], v111 offset:16384\n" \
"v_mfma_f32_32x32x16_bf16 v[0:15], a[36:39], v[36:39], v[0:15]\n" \
"global_load_lds_dwordx4 v102, s[46:47] offset:1024 sc1\n" \
"v_mfma_f32_32x32x16_bf16 v[16:31], a[180:183], v[36:39], v[16:31]\n" \
"s_waitcnt lgkmcnt(6)\n" \
"ds_read_b128 v[36:39], v111 offset:17408\n" \
"v_mfma_f32_32x32x16_bf16 v[0:15], a[40:43], v[40:43], v[0:15]\n" \
"v_mfma_f32_32x32x16_bf16 v[16:31], a[184:187], v[40:43], v[16:31]\n" \
"s_waitcnt lgkmcnt(6)\n" \
"ds_read_b128 v[40:43], v111 offset:18432\n" \
"v_mfma_f32_32x32x16_bf16 v[0:15], a[44:47], v[44:47], v[0:15]\n" \
"global_load_lds_dwordx4 v102, s[46:47] offset:2048 sc1\n" \
"v_mfma_f32_32x32x16_bf16 v[16:31], a[188:191], v[44:47], v[16:31]\n" \
"s_waitcnt lgkmcnt(6)\n" \
"ds_read_b128 v[44:47], v111 offset:19456\n" \
"v_mfma_f32_32x32x16_bf16 v[0:15], a[48:51], v[48:51], v[0:15]\n" \
"v_mfma_f32_32x32x16_bf16 v[16:31], a[192:195], v[48:51], v[16:31]\n" \
"s_waitcnt lgkmcnt(6)\n" \
"ds_read_b128 v[48:51], v111 offset:20480\n" \
"v_mfma_f32_32x32x16_bf16 v[0:15], a[52:55], v[52:55], v[0:15]\n" \
"global_load_lds_dwordx4 v102, s[46:47] offset:3072 sc1\n" \
"v_mfma_f32_32x32x16_bf16 v[16:31], a[196:199], v[52:55], v[16:31]\n" \
"s_waitcnt lgkmcnt(6)\n" \
"ds_read_b128 v[52:55], v111 offset:21504\n" \
"v_mfma_f32_32x32x16_bf16 v[0:15], a[56:59], v[56:59], v[0:15]\n" \
"v_mfma_f32_32x32x16_bf16 v[16:31], a[200:203], v[56:59], v[16:31]\n" \
"s_waitcnt lgkmcnt(6)\n" \
"ds_read_b128 v[56:59], v111 offset:22528\n" \
"v_mfma_f32_32x32x16_bf16 v[0:15], a[60:63], v[60:63], v[0:15]\n" \
"s_add_u32 m0, s60, 4096\n" \
"s_nop 0\n" \
"global_load_lds_dwordx4 v103, s[46:47] offset:0 sc1\n" \
"v_mfma_f32_32x32x16_bf16 v[16:31], a[204:207], v[60:63], v[16:31]\n" \
"s_waitcnt lgkmcnt(6)\n" \
"ds_read_b128 v[60:63], v111 offset:23552\n" \
"v_mfma_f32_32x32x16_bf16 v[0:15], a[64:67], v[32:35], v[0:15]\n" \
"v_mfma_f32_32x32x16_bf16 v[16:31], a[208:211], v[32:35], v[16:31]\n" \
"s_waitcnt lgkmcnt(6)\n" \
"ds_read_b128 v[32:35], v111 offset:24576\n" \
"v_mfma_f32_32x32x16_bf16 v[0:15], a[68:71], v[36:39], v[0:15]\n" \
"global_load_lds_dwordx4 v103, s[46:47] offset:1024 sc1\n" \
"v_mfma_f32_32x32x16_bf16 v[16:31], a[212:215], v[36:39], v[16:31]\n" \
"s_waitcnt lgkmcnt(6)\n" \
"ds_read_b128 v[36:39], v111 offset:25600\n" \
"v_mfma_f32_32x32x16_bf16 v[0:15], a[72:75], v[40:43], v[0:15]\n" \
"v_mfma_f32_32x32x16_bf16 v[16:31], a[216:219], v[40:43], v[16:31]\n" \
"s_waitcnt lgkmcnt(6)\n" \
"ds_read_b128 v[40:43], v111 offset:26624\n" \
"v_mfma_f32_32x32x16_bf16 v[0:15], a[76:79], v[44:47], v[0:15]\n" \
"global_load_lds_dwordx4 v103, s[46:47] offset:2048 sc1\n" \
"v_mfma_f32_32x32x16_bf16 v[16:31], a[220:223], v[44:47], v[16:31]\n" \
"s_waitcnt lgkmcnt(6)\n" \
"ds_read_b128 v[44:47], v111 offset:27648\n" \
"v_mfma_f32_32x32x16_bf16 v[0:15], a[80:83], v[48:51], v[0:15]\n" \
"v_mfma_f32_32x32x16_bf16 v[16:31], a[224:227], v[48:51], v[16:31]\n" \
"s_waitcnt lgkmcnt(6)\n" \
"ds_read_b128 v[48:51], v111 offset:28672\n" \
"v_mfma_f32_32x32x16_bf16 v[0:15], a[84:87], v[52:55], v[0:15]\n" \
"global_load_lds_dwordx4 v103, s[46:47] offset:3072 sc1\n" \
"v_mfma_f32_32x32x16_bf16 v[16:31], a[228:231], v[52:55], v[16:31]\n" \
"s_waitcnt lgkmcnt(6)\n" \
"ds_read_b128 v[52:55], v111 offset:29696\n" \
"v_mfma_f32_32x32x16_bf16 v[0:15], a[88:91], v[56:59], v[0:15]\n" \
"v_mfma_f32_32x32x16_bf16 v[16:31], a[232:235], v[56:59], v[16:31]\n" \
"s_waitcnt lgkmcnt(6)\n" \
"ds_read_b128 v[56:59], v111 offset:30720\n" \
"v_mfma_f32_32x32x16_bf16 v[0:15], a[92:95], v[60:63], v[0:15]\n" \
"s_add_u32 m0, s60, 8192\n" \
"s_nop 0\n" \
"global_load_lds_dwordx4 v104, s[46:47] offset:0 sc1\n" \
"s_add_u32 s46, s46, s45\n" \
"s_addc_u32 s47, s47, 0\n" \
"v_mfma_f32_32x32x16_bf16 v[16:31], a[236:239], v[60:63], v[16:31]\n" \
"s_waitcnt lgkmcnt(6)\n" \
"ds_read_b128 v[60:63], v111 offset:31744\n" \
"v_mfma_f32_32x32x16_bf16 v[0:15], a[96:99], v[32:35], v[0:15]\n" \
"s_add_u32 s60, s60, 36864\n" \
"v_mfma_f32_32x32x16_bf16 v[16:31], a[240:243], v[32:35], v[16:31]\n" \
"s_waitcnt lgkmcnt(6)\n" \
"ds_read_b128 v[32:35], v111 offset:32768\n" \
"v_mfma_f32_32x32x16_bf16 v[0:15], a[100:103], v[36:39], v[0:15]\n" \
"s_cmp_eq_u32 s60, s61\n" \
"s_cselect_b32 s60, s57, s60\n" \
"s_add_u32 s62, s62, 36864\n" \
"s_cmp_eq_u32 s62, 147456\n" \
"s_cselect_b32 s62, 0, s62\n" \
"v_mfma_f32_32x32x16_bf16 v[16:31], a[244:247], v[36:39], v[16:31]\n" \
"s_waitcnt lgkmcnt(6)\n" \
"ds_read_b128 v[36:39], v111 offset:33792\n" \
"v_mfma_f32_32x32x16_bf16 v[0:15], a[104:107], v[40:43], v[0:15]\n" \
"v_mfma_f32_32x32x16_bf16 v[16:31], a[248:251], v[40:43], v[16:31]\n" \
"s_waitcnt lgkmcnt(6)\n" \
"ds_read_b128 v[40:43], v111 offset:34816\n" \
"v_mfma_f32_32x32x16_bf16 v[0:15], a[108:111], v[44:47], v[0:15]\n" \
"v_mfma_f32_32x32x16_bf16 v[16:31], a[252:255], v[44:47], v[16:31]\n" \
"s_waitcnt lgkmcnt(6)\n" \
"ds_read_b128 v[44:47], v111 offset:35840\n" \
"v_add_u32 v111, s62, v101\n" \
"v_mfma_f32_32x32x16_bf16 v[0:15], a[112:115], v[48:51], v[0:15]\n" \
"v_mfma_f32_32x32x16_bf16 v[16:31], v[224:227], v[48:51], v[16:31]\n" \
"s_waitcnt lgkmcnt(6)\n" \
"ds_read_b128 v[48:51], v111 offset:0\n" \
"v_mfma_f32_32x32x16_bf16 v[0:15], a[116:119], v[52:55], v[0:15]\n" \
"v_mfma_f32_32x32x16_bf16 v[16:31], v[228:231], v[52:55], v[16:31]\n" \
"s_waitcnt lgkmcnt(6)\n" \
"ds_read_b128 v[52:55], v111 offset:1024\n" \
"v_mov_b32 v149, s58\n" \
"v_mfma_f32_32x32x16_bf16 v[0:15], a[120:123], v[56:59], v[0:15]\n" \
"s_waitcnt vmcnt(9)\n" \
"s_mov_b64 exec, 1\n" \
"global_store_dword v107, v149, s[52:53] sc1\n" \
"s_mov_b64 exec, -1\n" \
"v_mfma_f32_32x32x16_bf16 v[16:31], v[232:235], v[56:59], v[16:31]\n" \
"s_waitcnt lgkmcnt(6)\n" \
"ds_read_b128 v[56:59], v111 offset:2048\n" \
"v_mfma_f32_32x32x16_bf16 v[0:15], a[124:127], v[60:63], v[0:15]\n" \
"s_mov_b32 s63, s58\n" \
"s_add_u32 s58, s58, s59\n" \
"v_mfma_f32_32x32x16_bf16 v[16:31], v[236:239], v[60:63], v[16:31]\n" \
"s_waitcnt lgkmcnt(6)\n" \
"ds_read_b128 v[60:63], v111 offset:3072\n" \
"v_mfma_f32_32x32x16_bf16 v[0:15], a[128:131], v[32:35], v[0:15]\n" \
"v_mfma_f32_32x32x16_bf16 v[16:31], v[240:243], v[32:35], v[16:31]\n" \
"s_waitcnt lgkmcnt(6)\n" \
"ds_read_b128 v[32:35], v111 offset:4096\n" \
"v_mfma_f32_32x32x16_bf16 v[0:15], a[132:135], v[36:39], v[0:15]\n" \
"v_mfma_f32_32x32x16_bf16 v[16:31], v[244:247], v[36:39], v[16:31]\n" \
"s_waitcnt lgkmcnt(6)\n" \
"ds_read_b128 v[36:39], v111 offset:5120\n" \
"v_mfma_f32_32x32x16_bf16 v[0:15], a[136:139], v[40:43], v[0:15]\n" \
"v_mfma_f32_32x32x16_bf16 v[16:31], v[248:251], v[40:43], v[16:31]\n" \
"s_waitcnt lgkmcnt(6)\n" \
"ds_read_b128 v[40:43], v111 offset:6144\n" \
"v_mfma_f32_32x32x16_bf16 v[0:15], a[140:143], v[44:47], v[0:15]\n" \
"v_mfma_f32_32x32x16_bf16 v[16:31], v[252:255], v[44:47], v[16:31]\n" \
"s_waitcnt lgkmcnt(6)\n" \
"ds_read_b128 v[44:47], v111 offset:7168\n" \
"s_branch .Ltop%=\n" \
".Lfin%=:\n" \
"s_nop 15\n" \
"v_cvt_pk_bf16_f32 v64, v112, v113\n" \
"v_cvt_pk_bf16_f32 v65, v114, v115\n" \
"v_cvt_pk_bf16_f32 v66, v120, v121\n" \
"v_cvt_pk_bf16_f32 v67, v122, v123\n" \
"global_store_dwordx4 v105, v[64:67], s[48:49] offset:0 sc1\n" \
"v_cvt_pk_bf16_f32 v68, v116, v117\n" \
"v_cvt_pk_bf16_f32 v69, v118, v119\n" \
"v_cvt_pk_bf16_f32 v70, v124, v125\n" \
"v_cvt_pk_bf16_f32 v71, v126, v127\n" \
"global_store_dwordx4 v105, v[68:71], s[48:49] offset:512 sc1\n" \
"v_cvt_pk_bf16_f32 v72, v128, v129\n" \
"v_cvt_pk_bf16_f32 v73, v130, v131\n" \
"v_cvt_pk_bf16_f32 v74, v136, v137\n" \
"v_cvt_pk_bf16_f32 v75, v138, v139\n" \
"global_store_dwordx4 v106, v[72:75], s[48:49] offset:0 sc1\n" \
"v_cvt_pk_bf16_f32 v76, v132, v133\n" \
"v_cvt_pk_bf16_f32 v77, v134, v135\n" \
"v_cvt_pk_bf16_f32 v78, v140, v141\n" \
"v_cvt_pk_bf16_f32 v79, v142, v143\n" \
"global_store_dwordx4 v106, v[76:79], s[48:49] offset:512 sc1\n" \
"s_waitcnt vmcnt(0)\n" \
"v_mov_b32 v80, 64\n" \
"s_mov_b64 exec, 1\n" \
"global_store_dword v107, v80, s[52:53] sc1\n" \
"s_mov_b64 exec, -1\n" \
"s_mov_b64 exec, -1\n" \
"s_waitcnt lgkmcnt(0)\n" \
"s_barrier\n" \
"s_branch .Lout%=\n" \
".Lend%=:\n" \
"s_endpgm\n" \
".Lhb_s%=:\n" \
"s_sub_u32 s73, s73, 1\n" \
"s_cbranch_scc1 .Lend%=\n" \
"global_load_dword v152, v150, s[54:55] sc1\n" \
"s_waitcnt vmcnt(0)\n" \
"s_branch .Lhb_c%=\n" \
".Lln_s%=:\n" \
"s_sub_u32 s73, s73, 1\n" \
"s_cbranch_scc1 .Lend%=\n" \
"s_sleep 4\n" \
"global_load_dword v64, v107, s[54:55] sc1\n" \
"s_waitcnt vmcnt(0)\n" \
"s_branch .Lln_c%=\n" \
".Lrdy_s%=:\n" \
"s_sub_u32 s73, s73, 1\n" \
"s_cbranch_scc1 .Lend%=\n" \
"s_sleep 4\n" \
"global_load_dword v65, v66, s[54:55] sc1\n" \
"s_waitcnt vmcnt(0)\n" \
"s_branch .Lrdy_c%=\n" \
".Lout%=:\n"

#define H_ASM \
"s_mov_b32 s73, 2000000\n" \
"s_mov_b32 s75, 0xbfb8aa3b\n" \
"v_mov_b32 v108, v100\n" \
"v_add_u32 v109, 0x10000, v100\n" \
"v_add_u32 v110, 0x20000, v100\n" \
"v_mov_b32 v150, s65\n" \
"global_load_dword v151, v150, s[54:55] sc1\n" \
"s_memrealtime s[76:77]\n" \
"s_waitcnt lgkmcnt(0)\n" \
".Ldly%=:\n" \
"s_memrealtime s[78:79]\n" \
"s_waitcnt lgkmcnt(0)\n" \
"s_sub_u32 s78, s78, s76\n" \
"s_cmp_lt_u32 s78, s64\n" \
"s_cbranch_scc1 .Ldly%=\n" \
"s_add_u32 m0, s56, 0\n" \
"s_nop 0\n" \
"global_load_lds_dwordx4 v96, s[40:41] offset:0 nt\n" \
"global_load_lds_dwordx4 v97, s[40:41] offset:1024 nt\n" \
"global_load_lds_dwordx4 v98, s[40:41] offset:2048 nt\n" \
"global_load_lds_dwordx4 v99, s[40:41] offset:3072 nt\n" \
"s_add_u32 s40, s40, s44\n" \
"s_addc_u32 s41, s41, 0\n" \
"s_add_u32 m0, s56, 16384\n" \
"s_nop 0\n" \
"global_load_lds_dwordx4 v96, s[40:41] offset:0 nt\n" \
"global_load_lds_dwordx4 v97, s[40:41] offset:1024 nt\n" \
"global_load_lds_dwordx4 v98, s[40:41] offset:2048 nt\n" \
"global_load_lds_dwordx4 v99, s[40:41] offset:3072 nt\n" \
"s_add_u32 s40, s40, s44\n" \
"s_addc_u32 s41, s41, 0\n" \
"s_add_u32 m0, s56, 32768\n" \
"s_nop 0\n" \
"global_load_lds_dwordx4 v96, s[40:41] offset:0 nt\n" \
"global_load_lds_dwordx4 v97, s[40:41] offset:1024 nt\n" \
"global_load_lds_dwordx4 v98, s[40:41] offset:2048 nt\n" \
"global_load_lds_dwordx4 v99, s[40:41] offset:3072 nt\n" \
"s_add_u32 s40, s40, s44\n" \
"s_addc_u32 s41, s41, 0\n" \
"s_add_u32 m0, s56, 49152\n" \
"s_nop 0\n" \
"global_load_lds_dwordx4 v96, s[40:41] offset:0 nt\n" \
"global_load_lds_dwordx4 v97, s[40:41] offset:1024 nt\n" \
"global_load_lds_dwordx4 v98, s[40:41] offset:2048 nt\n" \
"global_load_lds_dwordx4 v99, s[40:41] offset:3072 nt\n" \
"s_add_u32 s40, s40, s44\n" \
"s_addc_u32 s41, s41, 0\n" \
"s_add_u32 m0, s56, 65536\n" \
"s_nop 0\n" \
"global_load_lds_dwordx4 v96, s[40:41] offset:0 nt\n" \
"global_load_lds_dwordx4 v97, s[40:41] offset:1024 nt\n" \
"global_load_lds_dwordx4 v98, s[40:41] offset:2048 nt\n" \
"global_load_lds_dwordx4 v99, s[40:41] offset:3072 nt\n" \
"s_add_u32 s40, s40, s44\n" \
"s_addc_u32 s41, s41, 0\n" \
"s_add_u32 m0, s56, 81920\n" \
"s_nop 0\n" \
"global_load_lds_dwordx4 v96, s[40:41] offset:0 nt\n" \
"global_load_lds_dwordx4 v97, s[40:41] offset:1024 nt\n" \
"global_load_lds_dwordx4 v98, s[40:41] offset:2048 nt\n" \
"global_load_lds_dwordx4 v99, s[40:41] offset:3072 nt\n" \
"s_add_u32 s40, s40, s44\n" \
"s_addc_u32 s41, s41, 0\n" \
"s_add_u32 m0, s56, 98304\n" \
"s_nop 0\n" \
"global_load_lds_dwordx4 v96, s[40:41] offset:0 nt\n" \
"global_load_lds_dwordx4 v97, s[40:41] offset:1024 nt\n" \
"global_load_lds_dwordx4 v98, s[40:41] offset:2048 nt\n" \
"global_load_lds_dwordx4 v99, s[40:41] offset:3072 nt\n" \
"s_add_u32 s40, s40, s44\n" \
"s_addc_u32 s41, s41, 0\n" \
"s_add_u32 m0, s56, 114688\n" \
"s_nop 0\n" \
"global_load_lds_dwordx4 v96, s[40:41] offset:0 nt\n" \
"global_load_lds_dwordx4 v97, s[40:41] offset:1024 nt\n" \
"global_load_lds_dwordx4 v98, s[40:41] offset:2048 nt\n" \
"global_load_lds_dwordx4 v99, s[40:41] offset:3072 nt\n" \
"s_add_u32 s40, s40, s44\n" \
"s_addc_u32 s41, s41, 0\n" \
"s_add_u32 m0, s56, 131072\n" \
"s_nop 0\n" \
"global_load_lds_dwordx4 v96, s[40:41] offset:0 nt\n" \
"global_load_lds_dwordx4 v97, s[40:41] offset:1024 nt\n" \
"global_load_lds_dwordx4 v98, s[40:41] offset:2048 nt\n" \
"global_load_lds_dwordx4 v99, s[40:41] offset:3072 nt\n" \
"s_add_u32 s40, s40, s44\n" \
"s_addc_u32 s41, s41, 0\n" \
"s_waitcnt vmcnt(32)\n" \
"s_barrier\n" \
"ds_read_b64_tr_b16 a[0:1], v108 offset:0\n" \
"ds_read_b64_tr_b16 a[2:3], v108 offset:1024\n" \
"ds_read_b64_tr_b16 a[4:5], v108 offset:4096\n" \
"ds_read_b64_tr_b16 a[6:7], v108 offset:5120\n" \
"ds_read_b64_tr_b16 a[8:9], v108 offset:8192\n" \
"ds_read_b64_tr_b16 a[10:11], v108 offset:9216\n" \
"ds_read_b64_tr_b16 a[12:13], v108 offset:12288\n" \
"ds_read_b64_tr_b16 a[14:15], v108 offset:13312\n" \
"s_waitcnt lgkmcnt(0)\n" \
"s_waitcnt vmcnt(28)\n" \
"s_barrier\n" \
"s_add_u32 m0, s56, 0\n" \
"s_nop 0\n" \
"global_load_lds_dwordx4 v96, s[42:43] offset:0 nt\n" \
"global_load_lds_dwordx4 v97, s[42:43] offset:1024 nt\n" \
"global_load_lds_dwordx4 v98, s[42:43] offset:2048 nt\n" \
"global_load_lds_dwordx4 v99, s[42:43] offset:3072 nt\n" \
"s_add_u32 s42, s42, s44\n" \
"s_addc_u32 s43, s43, 0\n" \
"ds_read_b64_tr_b16 a[16:17], v108 offset:16384\n" \
"ds_read_b64_tr_b16 a[18:19], v108 offset:17408\n" \
"ds_read_b64_tr_b16 a[20:21], v108 offset:20480\n" \
"ds_read_b64_tr_b16 a[22:23], v108 offset:21504\n" \
"ds_read_b64_tr_b16 a[24:25], v108 offset:24576\n" \
"ds_read_b64_tr_b16 a[26:27], v108 offset:25600\n" \
"ds_read_b64_tr_b16 a[28:29], v108 offset:28672\n" \
"ds_read_b64_tr_b16 a[30:31], v108 offset:29696\n" \
"s_waitcnt lgkmcnt(0)\n" \
"s_waitcnt vmcnt(28)\n" \
"s_barrier\n" \
"s_add_u32 m0, s56, 16384\n" \
"s_nop 0\n" \
"global_load_lds_dwordx4 v96, s[42:43] offset:0 nt\n" \
"global_load_lds_dwordx4 v97, s[42:43] offset:1024 nt\n" \
"global_load_lds_dwordx4 v98, s[42:43] offset:2048 nt\n" \
"global_load_lds_dwordx4 v99, s[42:43] offset:3072 nt\n" \
"s_add_u32 s42, s42, s44\n" \
"s_addc_u32 s43, s43, 0\n" \
"ds_read_b64_tr_b16 a[32:33], v108 offset:32768\n" \
"ds_read_b64_tr_b16 a[34:35], v108 offset:33792\n" \
"ds_read_b64_tr_b16 a[36:37], v108 offset:36864\n" \
"ds_read_b64_tr_b16 a[38:39], v108 offset:37888\n" \
"ds_read_b64_tr_b16 a[40:41], v108 offset:40960\n" \
"ds_read_b64_tr_b16 a[42:43], v108 offset:41984\n" \
"ds_read_b64_tr_b16 a[44:45], v108 offset:45056\n" \
"ds_read_b64_tr_b16 a[46:47], v108 offset:46080\n" \
"s_waitcnt lgkmcnt(0)\n" \
"s_waitcnt vmcnt(28)\n" \
"s_barrier\n" \
"s_add_u32 m0, s56, 32768\n" \
"s_nop 0\n" \
"global_load_lds_dwordx4 v96, s[42:43] offset:0 nt\n" \
"global_load_lds_dwordx4 v97, s[42:43] offset:1024 nt\n" \
"global_load_lds_dwordx4 v98, s[42:43] offset:2048 nt\n" \
"global_load_lds_dwordx4 v99, s[42:43] offset:3072 nt\n" \
"s_add_u32 s42, s42, s44\n" \
"s_addc_u32 s43, s43, 0\n" \
"ds_read_b64_tr_b16 a[48:49], v108 offset:49152\n" \
"ds_read_b64_tr_b16 a[50:51], v108 offset:50176\n" \
"ds_read_b64_tr_b16 a[52:53], v108 offset:53248\n" \
"ds_read_b64_tr_b16 a[54:55], v108 offset:54272\n" \
"ds_read_b64_tr_b16 a[56:57], v108 offset:57344\n" \
"ds_read_b64_tr_b16 a[58:59], v108 offset:58368\n" \
"ds_read_b64_tr_b16 a[60:61], v108 offset:61440\n" \
"ds_read_b64_tr_b16 a[62:63], v108 offset:62464\n" \
"s_waitcnt lgkmcnt(0)\n" \
"s_waitcnt vmcnt(28)\n" \
"s_barrier\n" \
"s_add_u32 m0, s56, 49152\n" \
"s_nop 0\n" \
"global_load_lds_dwordx4 v96, s[42:43] offset:0 nt\n" \
"global_load_lds_dwordx4 v97, s[42:43] offset:1024 nt\n" \
"global_load_lds_dwordx4 v98, s[42:43] offset:2048 nt\n" \
"global_load_lds_dwordx4 v99, s[42:43] offset:3072 nt\n" \
"s_add_u32 s42, s42, s44\n" \
"s_addc_u32 s43, s43, 0\n" \
"ds_read_b64_tr_b16 a[64:65], v109 offset:0\n" \
"ds_read_b64_tr_b16 a[66:67], v109 offset:1024\n" \
"ds_read_b64_tr_b16 a[68:69], v109 offset:4096\n" \
"ds_read_b64_tr_b16 a[70:71], v109 offset:5120\n" \
"ds_read_b64_tr_b16 a[72:73], v109 offset:8192\n" \
"ds_read_b64_tr_b16 a[74:75], v109 offset:9216\n" \
"ds_read_b64_tr_b16 a[76:77], v109 offset:12288\n" \
"ds_read_b64_tr_b16 a[78:79], v109 offset:13312\n" \
"s_waitcnt lgkmcnt(0)\n" \
"s_waitcnt vmcnt(28)\n" \
"s_barrier\n" \
"s_add_u32 m0, s56, 65536\n" \
"s_nop 0\n" \
"global_load_lds_dwordx4 v96, s[42:43] offset:0 nt\n" \
"global_load_lds_dwordx4 v97, s[42:43] offset:1024 nt\n" \
"global_load_lds_dwordx4 v98, s[42:43] offset:2048 nt\n" \
"global_load_lds_dwordx4 v99, s[42:43] offset:3072 nt\n" \
"s_add_u32 s42, s42, s44\n" \
"s_addc_u32 s43, s43, 0\n" \
"ds_read_b64_tr_b16 a[80:81], v109 offset:16384\n" \
"ds_read_b64_tr_b16 a[82:83], v109 offset:17408\n" \
"ds_read_b64_tr_b16 a[84:85], v109 offset:20480\n" \
"ds_read_b64_tr_b16 a[86:87], v109 offset:21504\n" \
"ds_read_b64_tr_b16 a[88:89], v109 offset:24576\n" \
"ds_read_b64_tr_b16 a[90:91], v109 offset:25600\n" \
"ds_read_b64_tr_b16 a[92:93], v109 offset:28672\n" \
"ds_read_b64_tr_b16 a[94:95], v109 offset:29696\n" \
"s_waitcnt lgkmcnt(0)\n" \
"s_waitcnt vmcnt(28)\n" \
"s_barrier\n" \
"s_add_u32 m0, s56, 81920\n" \
"s_nop 0\n" \
"global_load_lds_dwordx4 v96, s[42:43] offset:0 nt\n" \
"global_load_lds_dwordx4 v97, s[42:43] offset:1024 nt\n" \
"global_load_lds_dwordx4 v98, s[42:43] offset:2048 nt\n" \
"global_load_lds_dwordx4 v99, s[42:43] offset:3072 nt\n" \
"s_add_u32 s42, s42, s44\n" \
"s_addc_u32 s43, s43, 0\n" \
"ds_read_b64_tr_b16 a[96:97], v109 offset:32768\n" \
"ds_read_b64_tr_b16 a[98:99], v109 offset:33792\n" \
"ds_read_b64_tr_b16 a[100:101], v109 offset:36864\n" \
"ds_read_b64_tr_b16 a[102:103], v109 offset:37888\n" \
"ds_read_b64_tr_b16 a[104:105], v109 offset:40960\n" \
"ds_read_b64_tr_b16 a[106:107], v109 offset:41984\n" \
"ds_read_b64_tr_b16 a[108:109], v109 offset:45056\n" \
"ds_read_b64_tr_b16 a[110:111], v109 offset:46080\n" \
"s_waitcnt lgkmcnt(0)\n" \
"s_waitcnt vmcnt(28)\n" \
"s_barrier\n" \
"s_add_u32 m0, s56, 98304\n" \
"s_nop 0\n" \
"global_load_lds_dwordx4 v96, s[42:43] offset:0 nt\n" \
"global_load_lds_dwordx4 v97, s[42:43] offset:1024 nt\n" \
"global_load_lds_dwordx4 v98, s[42:43] offset:2048 nt\n" \
"global_load_lds_dwordx4 v99, s[42:43] offset:3072 nt\n" \
"s_add_u32 s42, s42, s44\n" \
"s_addc_u32 s43, s43, 0\n" \
"ds_read_b64_tr_b16 a[112:113], v109 offset:49152\n" \
"ds_read_b64_tr_b16 a[114:115], v109 offset:50176\n" \
"ds_read_b64_tr_b16 a[116:117], v109 offset:53248\n" \
"ds_read_b64_tr_b16 a[118:119], v109 offset:54272\n" \
"ds_read_b64_tr_b16 a[120:121], v109 offset:57344\n" \
"ds_read_b64_tr_b16 a[122:123], v109 offset:58368\n" \
"ds_read_b64_tr_b16 a[124:125], v109 offset:61440\n" \
"ds_read_b64_tr_b16 a[126:127], v109 offset:62464\n" \
"s_waitcnt lgkmcnt(0)\n" \
"s_waitcnt vmcnt(28)\n" \
"s_barrier\n" \
"global_load_dword v152, v150, s[54:55] sc1\n" \
"s_add_u32 m0, s56, 114688\n" \
"s_nop 0\n" \
"global_load_lds_dwordx4 v96, s[42:43] offset:0 nt\n" \
"global_load_lds_dwordx4 v97, s[42:43] offset:1024 nt\n" \
"global_load_lds_dwordx4 v98, s[42:43] offset:2048 nt\n" \
"global_load_lds_dwordx4 v99, s[42:43] offset:3072 nt\n" \
"s_add_u32 s42, s42, s44\n" \
"s_addc_u32 s43, s43, 0\n" \
"ds_read_b64_tr_b16 a[128:129], v110 offset:0\n" \
"ds_read_b64_tr_b16 a[130:131], v110 offset:1024\n" \
"ds_read_b64_tr_b16 a[132:133], v110 offset:4096\n" \
"ds_read_b64_tr_b16 a[134:135], v110 offset:5120\n" \
"ds_read_b64_tr_b16 a[136:137], v110 offset:8192\n" \
"ds_read_b64_tr_b16 a[138:139], v110 offset:9216\n" \
"ds_read_b64_tr_b16 a[140:141], v110 offset:12288\n" \
"ds_read_b64_tr_b16 a[142:143], v110 offset:13312\n" \
"s_waitcnt lgkmcnt(0)\n" \
"s_waitcnt vmcnt(29)\n" \
"s_barrier\n" \
"s_add_u32 m0, s56, 131072\n" \
"s_nop 0\n" \
"global_load_lds_dwordx4 v96, s[42:43] offset:0 nt\n" \
"global_load_lds_dwordx4 v97, s[42:43] offset:1024 nt\n" \
"global_load_lds_dwordx4 v98, s[42:43] offset:2048 nt\n" \
"global_load_lds_dwordx4 v99, s[42:43] offset:3072 nt\n" \
"s_add_u32 s42, s42, s44\n" \
"s_addc_u32 s43, s43, 0\n" \
"ds_read_b64_tr_b16 a[144:145], v108 offset:0\n" \
"ds_read_b64_tr_b16 a[146:147], v108 offset:1024\n" \
"ds_read_b64_tr_b16 a[148:149], v108 offset:4096\n" \
"ds_read_b64_tr_b16 a[150:151], v108 offset:5120\n" \
"ds_read_b64_tr_b16 a[152:153], v108 offset:8192\n" \
"ds_read_b64_tr_b16 a[154:155], v108 offset:9216\n" \
"ds_read_b64_tr_b16 a[156:157], v108 offset:12288\n" \
"ds_read_b64_tr_b16 a[158:159], v108 offset:13312\n" \
"s_waitcnt lgkmcnt(0)\n" \
"s_waitcnt vmcnt(29)\n" \
"s_barrier\n" \
"ds_read_b64_tr_b16 a[160:161], v108 offset:16384\n" \
"ds_read_b64_tr_b16 a[162:163], v108 offset:17408\n" \
"ds_read_b64_tr_b16 a[164:165], v108 offset:20480\n" \
"ds_read_b64_tr_b16 a[166:167], v108 offset:21504\n" \
"ds_read_b64_tr_b16 a[168:169], v108 offset:24576\n" \
"ds_read_b64_tr_b16 a[170:171], v108 offset:25600\n" \
"ds_read_b64_tr_b16 a[172:173], v108 offset:28672\n" \
"ds_read_b64_tr_b16 a[174:175], v108 offset:29696\n" \
"s_waitcnt lgkmcnt(0)\n" \
"s_waitcnt vmcnt(25)\n" \
"s_barrier\n" \
"ds_read_b64_tr_b16 a[176:177], v108 offset:32768\n" \
"ds_read_b64_tr_b16 a[178:179], v108 offset:33792\n" \
"ds_read_b64_tr_b16 a[180:181], v108 offset:36864\n" \
"ds_read_b64_tr_b16 a[182:183], v108 offset:37888\n" \
"ds_read_b64_tr_b16 a[184:185], v108 offset:40960\n" \
"ds_read_b64_tr_b16 a[186:187], v108 offset:41984\n" \
"ds_read_b64_tr_b16 a[188:189], v108 offset:45056\n" \
"ds_read_b64_tr_b16 a[190:191], v108 offset:46080\n" \
"s_waitcnt lgkmcnt(0)\n" \
"s_waitcnt vmcnt(21)\n" \
"s_barrier\n" \
"ds_read_b64_tr_b16 a[192:193], v108 offset:49152\n" \
"ds_read_b64_tr_b16 a[194:195], v108 offset:50176\n" \
"ds_read_b64_tr_b16 a[196:197], v108 offset:53248\n" \
"ds_read_b64_tr_b16 a[198:199], v108 offset:54272\n" \
"ds_read_b64_tr_b16 a[200:201], v108 offset:57344\n" \
"ds_read_b64_tr_b16 a[202:203], v108 offset:58368\n" \
"ds_read_b64_tr_b16 a[204:205], v108 offset:61440\n" \
"ds_read_b64_tr_b16 a[206:207], v108 offset:62464\n" \
"s_waitcnt lgkmcnt(0)\n" \
"s_waitcnt vmcnt(17)\n" \
"s_barrier\n" \
"ds_read_b64_tr_b16 a[208:209], v109 offset:0\n" \
"ds_read_b64_tr_b16 a[210:211], v109 offset:1024\n" \
"ds_read_b64_tr_b16 a[212:213], v109 offset:4096\n" \
"ds_read_b64_tr_b16 a[214:215], v109 offset:5120\n" \
"ds_read_b64_tr_b16 a[216:217], v109 offset:8192\n" \
"ds_read_b64_tr_b16 a[218:219], v109 offset:9216\n" \
"ds_read_b64_tr_b16 a[220:221], v109 offset:12288\n" \
"ds_read_b64_tr_b16 a[222:223], v109 offset:13312\n" \
"s_waitcnt lgkmcnt(0)\n" \
"s_waitcnt vmcnt(13)\n" \
"s_barrier\n" \
"ds_read_b64_tr_b16 a[224:225], v109 offset:16384\n" \
"ds_read_b64_tr_b16 a[226:227], v109 offset:17408\n" \
"ds_read_b64_tr_b16 a[228:229], v109 offset:20480\n" \
"ds_read_b64_tr_b16 a[230:231], v109 offset:21504\n" \
"ds_read_b64_tr_b16 a[232:233], v109 offset:24576\n" \
"ds_read_b64_tr_b16 a[234:235], v109 offset:25600\n" \
"ds_read_b64_tr_b16 a[236:237], v109 offset:28672\n" \
"ds_read_b64_tr_b16 a[238:239], v109 offset:29696\n" \
"s_waitcnt lgkmcnt(0)\n" \
"s_waitcnt vmcnt(9)\n" \
"s_barrier\n" \
"ds_read_b64_tr_b16 a[240:241], v109 offset:32768\n" \
"ds_read_b64_tr_b16 a[242:243], v109 offset:33792\n" \
"ds_read_b64_tr_b16 a[244:245], v109 offset:36864\n" \
"ds_read_b64_tr_b16 a[246:247], v109 offset:37888\n" \
"ds_read_b64_tr_b16 a[248:249], v109 offset:40960\n" \
"ds_read_b64_tr_b16 a[250:251], v109 offset:41984\n" \
"ds_read_b64_tr_b16 a[252:253], v109 offset:45056\n" \
"ds_read_b64_tr_b16 a[254:255], v109 offset:46080\n" \
"s_waitcnt lgkmcnt(0)\n" \
"s_waitcnt vmcnt(4)\n" \
"s_barrier\n" \
".Lhb_c%=:\n" \
"v_cmp_eq_u32 vcc, v151, v152\n" \
"s_nop 4\n" \
"s_cbranch_vccnz .Lhb_s%=\n" \
"global_load_dword v64, v107, s[54:55] sc1\n" \
"ds_read_b64_tr_b16 v[224:225], v109 offset:49152\n" \
"ds_read_b64_tr_b16 v[226:227], v109 offset:50176\n" \
"ds_read_b64_tr_b16 v[228:229], v109 offset:53248\n" \
"ds_read_b64_tr_b16 v[230:231], v109 offset:54272\n" \
"ds_read_b64_tr_b16 v[232:233], v109 offset:57344\n" \
"ds_read_b64_tr_b16 v[234:235], v109 offset:58368\n" \
"ds_read_b64_tr_b16 v[236:237], v109 offset:61440\n" \
"ds_read_b64_tr_b16 v[238:239], v109 offset:62464\n" \
"s_waitcnt lgkmcnt(0)\n" \
"s_waitcnt vmcnt(1)\n" \
"s_barrier\n" \
"ds_read_b64_tr_b16 v[240:241], v110 offset:0\n" \
"ds_read_b64_tr_b16 v[242:243], v110 offset:1024\n" \
"ds_read_b64_tr_b16 v[244:245], v110 offset:4096\n" \
"ds_read_b64_tr_b16 v[246:247], v110 offset:5120\n" \
"ds_read_b64_tr_b16 v[248:249], v110 offset:8192\n" \
"ds_read_b64_tr_b16 v[250:251], v110 offset:9216\n" \
"ds_read_b64_tr_b16 v[252:253], v110 offset:12288\n" \
"ds_read_b64_tr_b16 v[254:255], v110 offset:13312\n" \
"s_waitcnt lgkmcnt(0)\n" \
"s_barrier\n" \
"s_waitcnt vmcnt(0)\n" \
".Lln_c%=:\n" \
"v_cmp_gt_u32 vcc, 1, v64\n" \
"s_cbranch_vccnz .Lln_s%=\n" \
"s_mov_b32 s60, s57\n" \
"s_add_u32 s61, s57, 147456\n" \
"s_mov_b32 s62, 0\n" \
"s_mov_b32 s63, 0\n" \
"s_mov_b32 s64, 0\n" \
"s_add_u32 m0, s60, 0\n" \
"s_nop 0\n" \
"global_load_lds_dwordx4 v102, s[46:47] offset:0 sc1\n" \
"global_load_lds_dwordx4 v102, s[46:47] offset:1024 sc1\n" \
"global_load_lds_dwordx4 v102, s[46:47] offset:2048 sc1\n" \
"global_load_lds_dwordx4 v102, s[46:47] offset:3072 sc1\n" \
"s_add_u32 m0, s60, 4096\n" \
"s_nop 0\n" \
"global_load_lds_dwordx4 v103, s[46:47] offset:0 sc1\n" \
"global_load_lds_dwordx4 v103, s[46:47] offset:1024 sc1\n" \
"global_load_lds_dwordx4 v103, s[46:47] offset:2048 sc1\n" \
"global_load_lds_dwordx4 v103, s[46:47] offset:3072 sc1\n" \
"s_add_u32 m0, s60, 8192\n" \
"s_nop 0\n" \
"global_load_lds_dwordx4 v104, s[46:47] offset:0 sc1\n" \
"s_add_u32 s46, s46, s45\n" \
"s_addc_u32 s47, s47, 0\n" \
"s_add_u32 s60, s60, 36864\n" \
"s_cmp_eq_u32 s60, s61\n" \
"s_cselect_b32 s60, s57, s60\n" \
"s_add_u32 m0, s60, 0\n" \
"s_nop 0\n" \
"global_load_lds_dwordx4 v102, s[46:47] offset:0 sc1\n" \
"global_load_lds_dwordx4 v102, s[46:47] offset:1024 sc1\n" \
"global_load_lds_dwordx4 v102, s[46:47] offset:2048 sc1\n" \
"global_load_lds_dwordx4 v102, s[46:47] offset:3072 sc1\n" \
"s_add_u32 m0, s60, 4096\n" \
"s_nop 0\n" \
"global_load_lds_dwordx4 v103, s[46:47] offset:0 sc1\n" \
"global_load_lds_dwordx4 v103, s[46:47] offset:1024 sc1\n" \
"global_load_lds_dwordx4 v103, s[46:47] offset:2048 sc1\n" \
"global_load_lds_dwordx4 v103, s[46:47] offset:3072 sc1\n" \
"s_add_u32 m0, s60, 8192\n" \
"s_nop 0\n" \
"global_load_lds_dwordx4 v104, s[46:47] offset:0 sc1\n" \
"s_add_u32 s46, s46, s45\n" \
"s_addc_u32 s47, s47, 0\n" \
"s_add_u32 s60, s60, 36864\n" \
"s_cmp_eq_u32 s60, s61\n" \
"s_cselect_b32 s60, s57, s60\n" \
"s_add_u32 m0, s60, 0\n" \
"s_nop 0\n" \
"global_load_lds_dwordx4 v102, s[46:47] offset:0 sc1\n" \
"global_load_lds_dwordx4 v102, s[46:47] offset:1024 sc1\n" \
"global_load_lds_dwordx4 v102, s[46:47] offset:2048 sc1\n" \
"global_load_lds_dwordx4 v102, s[46:47] offset:3072 sc1\n" \
"s_add_u32 m0, s60, 4096\n" \
"s_nop 0\n" \
"global_load_lds_dwordx4 v103, s[46:47] offset:0 sc1\n" \
"global_load_lds_dwordx4 v103, s[46:47] offset:1024 sc1\n" \
"global_load_lds_dwordx4 v103, s[46:47] offset:2048 sc1\n" \
"global_load_lds_dwordx4 v103, s[46:47] offset:3072 sc1\n" \
"s_add_u32 m0, s60, 8192\n" \
"s_nop 0\n" \
"global_load_lds_dwordx4 v104, s[46:47] offset:0 sc1\n" \
"s_add_u32 s46, s46, s45\n" \
"s_addc_u32 s47, s47, 0\n" \
"s_add_u32 s60, s60, 36864\n" \
"s_cmp_eq_u32 s60, s61\n" \
"s_cselect_b32 s60, s57, s60\n" \
"v_mov_b32 v111, v101\n" \
"v_lshlrev_b32 v148, 1, v107\n" \
"v_mov_b32 v144, -1\n" \
"v_mov_b32 v145, -1\n" \
"v_mov_b32 v146, -1\n" \
"v_mov_b32 v147, -1\n" \
"s_waitcnt vmcnt(18)\n" \
"s_barrier\n" \
"ds_read_b128 v[32:35], v111 offset:0\n" \
"ds_read_b128 v[36:39], v111 offset:1024\n" \
"ds_read_b128 v[40:43], v111 offset:2048\n" \
"ds_read_b128 v[44:47], v111 offset:3072\n" \
"ds_read_b128 v[48:51], v111 offset:4096\n" \
"ds_read_b128 v[52:55], v111 offset:5120\n" \
"ds_read_b128 v[56:59], v111 offset:6144\n" \
"ds_read_b128 v[60:63], v111 offset:7168\n" \
"s_waitcnt lgkmcnt(7)\n" \
"v_mfma_f32_32x32x16_bf16 v[0:15], a[0:3], v[32:35], 0\n" \
"v_mfma_f32_32x32x16_bf16 v[16:31], a[144:147], v[32:35], 0\n" \
"s_waitcnt lgkmcnt(6)\n" \
"ds_read_b128 v[32:35], v111 offset:8192\n" \
"v_mfma_f32_32x32x16_bf16 v[0:15], a[4:7], v[36:39], v[0:15]\n" \
"v_mfma_f32_32x32x16_bf16 v[16:31], a[148:151], v[36:39], v[16:31]\n" \
"s_waitcnt lgkmcnt(6)\n" \
"ds_read_b128 v[36:39], v111 offset:9216\n" \
"v_mfma_f32_32x32x16_bf16 v[0:15], a[8:11], v[40:43], v[0:15]\n" \
"v_mfma_f32_32x32x16_bf16 v[16:31], a[152:155], v[40:43], v[16:31]\n" \
"s_waitcnt lgkmcnt(6)\n" \
"ds_read_b128 v[40:43], v111 offset:10240\n" \
"v_mfma_f32_32x32x16_bf16 v[0:15], a[12:15], v[44:47], v[0:15]\n" \
"v_mfma_f32_32x32x16_bf16 v[16:31], a[156:159], v[44:47], v[16:31]\n" \
"s_waitcnt lgkmcnt(6)\n" \
"ds_read_b128 v[44:47], v111 offset:11264\n" \
"v_mfma_f32_32x32x16_bf16 v[0:15], a[16:19], v[48:51], v[0:15]\n" \
"v_mfma_f32_32x32x16_bf16 v[16:31], a[160:163], v[48:51], v[16:31]\n" \
"s_waitcnt lgkmcnt(6)\n" \
"ds_read_b128 v[48:51], v111 offset:12288\n" \
"v_mfma_f32_32x32x16_bf16 v[0:15], a[20:23], v[52:55], v[0:15]\n" \
"v_mfma_f32_32x32x16_bf16 v[16:31], a[164:167], v[52:55], v[16:31]\n" \
"s_waitcnt lgkmcnt(6)\n" \
"ds_read_b128 v[52:55], v111 offset:13312\n" \
"v_mfma_f32_32x32x16_bf16 v[0:15], a[24:27], v[56:59], v[0:15]\n" \
"v_mfma_f32_32x32x16_bf16 v[16:31], a[168:171], v[56:59], v[16:31]\n" \
"s_waitcnt lgkmcnt(6)\n" \
"ds_read_b128 v[56:59], v111 offset:14336\n" \
"v_mfma_f32_32x32x16_bf16 v[0:15], a[28:31], v[60:63], v[0:15]\n" \
"s_add_u32 m0, s60, 0\n" \
"s_nop 0\n" \
"global_load_lds_dwordx4 v102, s[46:47] offset:0 sc1\n" \
"v_mfma_f32_32x32x16_bf16 v[16:31], a[172:175], v[60:63], v[16:31]\n" \
"s_waitcnt lgkmcnt(6)\n" \
"ds_read_b128 v[60:63], v111 offset:15360\n" \
"v_mfma_f32_32x32x16_bf16 v[0:15], a[32:35], v[32:35], v[0:15]\n" \
"v_mfma_f32_32x32x16_bf16 v[16:31], a[176:179], v[32:35], v[16:31]\n" \
"s_waitcnt lgkmcnt(6)\n" \
"ds_read_b128 v[32:35], v111 offset:16384\n" \
"v_mfma_f32_32x32x16_bf16 v[0:15], a[36:39], v[36:39], v[0:15]\n" \
"global_load_lds_dwordx4 v102, s[46:47] offset:1024 sc1\n" \
"v_mfma_f32_32x32x16_bf16 v[16:31], a[180:183], v[36:39], v[16:31]\n" \
"s_waitcnt lgkmcnt(6)\n" \
"ds_read_b128 v[36:39], v111 offset:17408\n" \
"v_mfma_f32_32x32x16_bf16 v[0:15], a[40:43], v[40:43], v[0:15]\n" \
"v_mfma_f32_32x32x16_bf16 v[16:31], a[184:187], v[40:43], v[16:31]\n" \
"s_waitcnt lgkmcnt(6)\n" \
"ds_read_b128 v[40:43], v111 offset:18432\n" \
"v_mfma_f32_32x32x16_bf16 v[0:15], a[44:47], v[44:47], v[0:15]\n" \
"global_load_lds_dwordx4 v102, s[46:47] offset:2048 sc1\n" \
"v_mfma_f32_32x32x16_bf16 v[16:31], a[188:191], v[44:47], v[16:31]\n" \
"s_waitcnt lgkmcnt(6)\n" \
"ds_read_b128 v[44:47], v111 offset:19456\n" \
"v_mfma_f32_32x32x16_bf16 v[0:15], a[48:51], v[48:51], v[0:15]\n" \
"v_mfma_f32_32x32x16_bf16 v[16:31], a[192:195], v[48:51], v[16:31]\n" \
"s_waitcnt lgkmcnt(6)\n" \
"ds_read_b128 v[48:51], v111 offset:20480\n" \
"v_mfma_f32_32x32x16_bf16 v[0:15], a[52:55], v[52:55], v[0:15]\n" \
"global_load_lds_dwordx4 v102, s[46:47] offset:3072 sc1\n" \
"v_mfma_f32_32x32x16_bf16 v[16:31], a[196:199], v[52:55], v[16:31]\n" \
"s_waitcnt lgkmcnt(6)\n" \
"ds_read_b128 v[52:55], v111 offset:21504\n" \
"v_mfma_f32_32x32x16_bf16 v[0:15], a[56:59], v[56:59], v[0:15]\n" \
"v_mfma_f32_32x32x16_bf16 v[16:31], a[200:203], v[56:59], v[16:31]\n" \
"s_waitcnt lgkmcnt(6)\n" \
"ds_read_b128 v[56:59], v111 offset:22528\n" \
"v_mfma_f32_32x32x16_bf16 v[0:15], a[60:63], v[60:63], v[0:15]\n" \
"s_add_u32 m0, s60, 4096\n" \
"s_nop 0\n" \
"global_load_lds_dwordx4 v103, s[46:47] offset:0 sc1\n" \
"v_mfma_f32_32x32x16_bf16 v[16:31], a[204:207], v[60:63], v[16:31]\n" \
"s_waitcnt lgkmcnt(6)\n" \
"ds_read_b128 v[60:63], v111 offset:23552\n" \
"v_mfma_f32_32x32x16_bf16 v[0:15], a[64:67], v[32:35], v[0:15]\n" \
"v_mfma_f32_32x32x16_bf16 v[16:31], a[208:211], v[32:35], v[16:31]\n" \
"s_waitcnt lgkmcnt(6)\n" \
"ds_read_b128 v[32:35], v111 offset:24576\n" \
"v_mfma_f32_32x32x16_bf16 v[0:15], a[68:71], v[36:39], v[0:15]\n" \
"global_load_lds_dwordx4 v103, s[46:47] offset:1024 sc1\n" \
"v_mfma_f32_32x32x16_bf16 v[16:31], a[212:215], v[36:39], v[16:31]\n" \
"s_waitcnt lgkmcnt(6)\n" \
"ds_read_b128 v[36:39], v111 offset:25600\n" \
"v_mfma_f32_32x32x16_bf16 v[0:15], a[72:75], v[40:43], v[0:15]\n" \
"v_mfma_f32_32x32x16_bf16 v[16:31], a[216:219], v[40:43], v[16:31]\n" \
"s_waitcnt lgkmcnt(6)\n" \
"ds_read_b128 v[40:43], v111 offset:26624\n" \
"v_mfma_f32_32x32x16_bf16 v[0:15], a[76:79], v[44:47], v[0:15]\n" \
"global_load_lds_dwordx4 v103, s[46:47] offset:2048 sc1\n" \
"v_mfma_f32_32x32x16_bf16 v[16:31], a[220:223], v[44:47], v[16:31]\n" \
"s_waitcnt lgkmcnt(6)\n" \
"ds_read_b128 v[44:47], v111 offset:27648\n" \
"v_mfma_f32_32x32x16_bf16 v[0:15], a[80:83], v[48:51], v[0:15]\n" \
"s_waitcnt vmcnt(7)\n" \
"s_barrier\n" \
"v_mfma_f32_32x32x16_bf16 v[16:31], a[224:227], v[48:51], v[16:31]\n" \
"s_waitcnt lgkmcnt(6)\n" \
"ds_read_b128 v[48:51], v111 offset:28672\n" \
"v_mfma_f32_32x32x16_bf16 v[0:15], a[84:87], v[52:55], v[0:15]\n" \
"global_load_lds_dwordx4 v103, s[46:47] offset:3072 sc1\n" \
"v_mfma_f32_32x32x16_bf16 v[16:31], a[228:231], v[52:55], v[16:31]\n" \
"s_waitcnt lgkmcnt(6)\n" \
"ds_read_b128 v[52:55], v111 offset:29696\n" \
"v_mfma_f32_32x32x16_bf16 v[0:15], a[88:91], v[56:59], v[0:15]\n" \
"v_mfma_f32_32x32x16_bf16 v[16:31], a[232:235], v[56:59], v[16:31]\n" \
"s_waitcnt lgkmcnt(6)\n" \
"ds_read_b128 v[56:59], v111 offset:30720\n" \
"v_mfma_f32_32x32x16_bf16 v[0:15], a[92:95], v[60:63], v[0:15]\n" \
"s_add_u32 m0, s60, 8192\n" \
"s_nop 0\n" \
"global_load_lds_dwordx4 v104, s[46:47] offset:0 sc1\n" \
"s_add_u32 s46, s46, s45\n" \
"s_addc_u32 s47, s47, 0\n" \
"v_mfma_f32_32x32x16_bf16 v[16:31], a[236:239], v[60:63], v[16:31]\n" \
"s_waitcnt lgkmcnt(6)\n" \
"ds_read_b128 v[60:63], v111 offset:31744\n" \
"v_mfma_f32_32x32x16_bf16 v[0:15], a[96:99], v[32:35], v[0:15]\n" \
"s_add_u32 s60, s60, 36864\n" \
"v_mfma_f32_32x32x16_bf16 v[16:31], a[240:243], v[32:35], v[16:31]\n" \
"s_waitcnt lgkmcnt(6)\n" \
"ds_read_b128 v[32:35], v111 offset:32768\n" \
"v_mfma_f32_32x32x16_bf16 v[0:15], a[100:103], v[36:39], v[0:15]\n" \
"s_cmp_eq_u32 s60, s61\n" \
"s_cselect_b32 s60, s57, s60\n" \
"s_add_u32 s62, s62, 36864\n" \
"s_cmp_eq_u32 s62, 147456\n" \
"s_cselect_b32 s62, 0, s62\n" \
"v_mfma_f32_32x32x16_bf16 v[16:31], a[244:247], v[36:39], v[16:31]\n" \
"s_waitcnt lgkmcnt(6)\n" \
"ds_read_b128 v[36:39], v111 offset:33792\n" \
"v_mfma_f32_32x32x16_bf16 v[0:15], a[104:107], v[40:43], v[0:15]\n" \
"v_mfma_f32_32x32x16_bf16 v[16:31], a[248:251], v[40:43], v[16:31]\n" \
"s_waitcnt lgkmcnt(6)\n" \
"ds_read_b128 v[40:43], v111 offset:34816\n" \
"v_mfma_f32_32x32x16_bf16 v[0:15], a[108:111], v[44:47], v[0:15]\n" \
"v_mfma_f32_32x32x16_bf16 v[16:31], a[252:255], v[44:47], v[16:31]\n" \
"s_waitcnt lgkmcnt(6)\n" \
"ds_read_b128 v[44:47], v111 offset:35840\n" \
"v_add_u32 v111, s62, v101\n" \
"v_mfma_f32_32x32x16_bf16 v[0:15], a[112:115], v[48:51], v[0:15]\n" \
"v_mfma_f32_32x32x16_bf16 v[16:31], v[224:227], v[48:51], v[16:31]\n" \
"s_waitcnt lgkmcnt(6)\n" \
"ds_read_b128 v[48:51], v111 offset:0\n" \
"v_mfma_f32_32x32x16_bf16 v[0:15], a[116:119], v[52:55], v[0:15]\n" \
"v_mfma_f32_32x32x16_bf16 v[16:31], v[228:231], v[52:55], v[16:31]\n" \
"s_waitcnt lgkmcnt(6)\n" \
"ds_read_b128 v[52:55], v111 offset:1024\n" \
"v_mfma_f32_32x32x16_bf16 v[0:15], a[120:123], v[56:59], v[0:15]\n" \
"v_mfma_f32_32x32x16_bf16 v[16:31], v[232:235], v[56:59], v[16:31]\n" \
"s_waitcnt lgkmcnt(6)\n" \
"ds_read_b128 v[56:59], v111 offset:2048\n" \
"v_mfma_f32_32x32x16_bf16 v[0:15], a[124:127], v[60:63], v[0:15]\n" \
"v_mfma_f32_32x32x16_bf16 v[16:31], v[236:239], v[60:63], v[16:31]\n" \
"s_waitcnt lgkmcnt(6)\n" \
"ds_read_b128 v[60:63], v111 offset:3072\n" \
"v_mfma_f32_32x32x16_bf16 v[0:15], a[128:131], v[32:35], v[0:15]\n" \
"v_mfma_f32_32x32x16_bf16 v[16:31], v[240:243], v[32:35], v[16:31]\n" \
"s_waitcnt lgkmcnt(6)\n" \
"ds_read_b128 v[32:35], v111 offset:4096\n" \
"v_mfma_f32_32x32x16_bf16 v[0:15], a[132:135], v[36:39], v[0:15]\n" \
"v_mfma_f32_32x32x16_bf16 v[16:31], v[244:247], v[36:39], v[16:31]\n" \
"s_waitcnt lgkmcnt(6)\n" \
"ds_read_b128 v[36:39], v111 offset:5120\n" \
"v_mfma_f32_32x32x16_bf16 v[0:15], a[136:139], v[40:43], v[0:15]\n" \
"v_mfma_f32_32x32x16_bf16 v[16:31], v[248:251], v[40:43], v[16:31]\n" \
"s_waitcnt lgkmcnt(6)\n" \
"ds_read_b128 v[40:43], v111 offset:6144\n" \
"v_mfma_f32_32x32x16_bf16 v[0:15], a[140:143], v[44:47], v[0:15]\n" \
"v_mfma_f32_32x32x16_bf16 v[16:31], v[252:255], v[44:47], v[16:31]\n" \
"s_waitcnt lgkmcnt(6)\n" \
"ds_read_b128 v[44:47], v111 offset:7168\n" \
".Ltop%=:\n" \
"v_mfma_f32_32x32x16_bf16 v[112:127], a[0:3], v[48:51], 0\n" \
"s_barrier\n" \
"v_mfma_f32_32x32x16_bf16 v[128:143], a[144:147], v[48:51], 0\n" \
"s_waitcnt lgkmcnt(6)\n" \
"ds_read_b128 v[48:51], v111 offset:8192\n" \
"v_mfma_f32_32x32x16_bf16 v[112:127], a[4:7], v[52:55], v[112:127]\n" \
"v_mul_f32 v80, s75, v0\n" \
"v_mul_f32 v81, s75, v1\n" \
"v_mul_f32 v82, s75, v2\n" \
"v_mul_f32 v83, s75, v3\n" \
"v_mul_f32 v84, s75, v8\n" \
"v_mul_f32 v85, s75, v9\n" \
"v_mfma_f32_32x32x16_bf16 v[128:143], a[148:151], v[52:55], v[128:143]\n" \
"s_waitcnt lgkmcnt(6)\n" \
"ds_read_b128 v[52:55], v111 offset:9216\n" \
"v_mul_f32 v86, s75, v10\n" \
"v_mul_f32 v87, s75, v11\n" \
"v_exp_f32 v80, v80\n" \
"v_mfma_f32_32x32x16_bf16 v[112:127], a[8:11], v[56:59], v[112:127]\n" \
"v_exp_f32 v81, v81\n" \
"v_exp_f32 v82, v82\n" \
"v_exp_f32 v83, v83\n" \
"v_mfma_f32_32x32x16_bf16 v[128:143], a[152:155], v[56:59], v[128:143]\n" \
"s_waitcnt lgkmcnt(6)\n" \
"ds_read_b128 v[56:59], v111 offset:10240\n" \
"v_exp_f32 v84, v84\n" \
"v_exp_f32 v85, v85\n" \
"v_mfma_f32_32x32x16_bf16 v[112:127], a[12:15], v[60:63], v[112:127]\n" \
"v_exp_f32 v86, v86\n" \
"v_exp_f32 v87, v87\n" \
"v_add_f32 v80, 1.0, v80\n" \
"v_add_f32 v81, 1.0, v81\n" \
"v_mfma_f32_32x32x16_bf16 v[128:143], a[156:159], v[60:63], v[128:143]\n" \
"s_waitcnt lgkmcnt(6)\n" \
"ds_read_b128 v[60:63], v111 offset:11264\n" \
"v_add_f32 v82, 1.0, v82\n" \
"v_add_f32 v83, 1.0, v83\n" \
"v_add_f32 v84, 1.0, v84\n" \
"v_add_f32 v85, 1.0, v85\n" \
"v_add_f32 v86, 1.0, v86\n" \
"v_mfma_f32_32x32x16_bf16 v[112:127], a[16:19], v[32:35], v[112:127]\n" \
"v_add_f32 v87, 1.0, v87\n" \
"v_rcp_f32 v80, v80\n" \
"v_rcp_f32 v81, v81\n" \
"v_mfma_f32_32x32x16_bf16 v[128:143], a[160:163], v[32:35], v[128:143]\n" \
"s_waitcnt lgkmcnt(6)\n" \
"ds_read_b128 v[32:35], v111 offset:12288\n" \
"v_rcp_f32 v82, v82\n" \
"v_rcp_f32 v83, v83\n" \
"v_mfma_f32_32x32x16_bf16 v[112:127], a[20:23], v[36:39], v[112:127]\n" \
"v_rcp_f32 v84, v84\n" \
"v_rcp_f32 v85, v85\n" \
"v_rcp_f32 v86, v86\n" \
"v_mfma_f32_32x32x16_bf16 v[128:143], a[164:167], v[36:39], v[128:143]\n" \
"s_waitcnt lgkmcnt(6)\n" \
"ds_read_b128 v[36:39], v111 offset:13312\n" \
"v_rcp_f32 v87, v87\n" \
"v_mul_f32 v80, v80, v0\n" \
"v_mul_f32 v81, v81, v1\n" \
"v_mul_f32 v82, v82, v2\n" \
"v_mfma_f32_32x32x16_bf16 v[112:127], a[24:27], v[40:43], v[112:127]\n" \
"v_mul_f32 v83, v83, v3\n" \
"v_mul_f32 v84, v84, v8\n" \
"v_mul_f32 v85, v85, v9\n" \
"v_mul_f32 v86, v86, v10\n" \
"v_mul_f32 v87, v87, v11\n" \
"v_cvt_pk_bf16_f32 v64, v80, v81\n" \
"v_mfma_f32_32x32x16_bf16 v[128:143], a[168:171], v[40:43], v[128:143]\n" \
"s_waitcnt lgkmcnt(6)\n" \
"ds_read_b128 v[40:43], v111 offset:14336\n" \
"v_cvt_pk_bf16_f32 v65, v82, v83\n" \
"v_cvt_pk_bf16_f32 v66, v84, v85\n" \
"v_cvt_pk_bf16_f32 v67, v86, v87\n" \
"v_mfma_f32_32x32x16_bf16 v[112:127], a[28:31], v[44:47], v[112:127]\n" \
"global_store_dwordx4 v105, v[64:67], s[48:49] offset:0 sc1\n" \
"v_mul_f32 v80, s75, v4\n" \
"v_mul_f32 v81, s75, v5\n" \
"v_mul_f32 v82, s75, v6\n" \
"v_mul_f32 v83, s75, v7\n" \
"v_mfma_f32_32x32x16_bf16 v[128:143], a[172:175], v[44:47], v[128:143]\n" \
"s_waitcnt lgkmcnt(6)\n" \
"ds_read_b128 v[44:47], v111 offset:15360\n" \
"v_mul_f32 v84, s75, v12\n" \
"v_mul_f32 v85, s75, v13\n" \
"v_mul_f32 v86, s75, v14\n" \
"v_mul_f32 v87, s75, v15\n" \
"v_mfma_f32_32x32x16_bf16 v[112:127], a[32:35], v[48:51], v[112:127]\n" \
"s_add_u32 m0, s60, 0\n" \
"s_nop 0\n" \
"global_load_lds_dwordx4 v102, s[46:47] offset:0 sc1\n" \
"v_exp_f32 v80, v80\n" \
"v_exp_f32 v81, v81\n" \
"v_mfma_f32_32x32x16_bf16 v[128:143], a[176:179], v[48:51], v[128:143]\n" \
"s_waitcnt lgkmcnt(6)\n" \
"ds_read_b128 v[48:51], v111 offset:16384\n" \
"v_exp_f32 v82, v82\n" \
"v_exp_f32 v83, v83\n" \
"v_mfma_f32_32x32x16_bf16 v[112:127], a[36:39], v[52:55], v[112:127]\n" \
"global_load_lds_dwordx4 v102, s[46:47] offset:1024 sc1\n" \
"v_exp_f32 v84, v84\n" \
"v_exp_f32 v85, v85\n" \
"v_mfma_f32_32x32x16_bf16 v[128:143], a[180:183], v[52:55], v[128:143]\n" \
"s_waitcnt lgkmcnt(6)\n" \
"ds_read_b128 v[52:55], v111 offset:17408\n" \
"v_exp_f32 v86, v86\n" \
"v_exp_f32 v87, v87\n" \
"v_add_f32 v80, 1.0, v80\n" \
"v_mfma_f32_32x32x16_bf16 v[112:127], a[40:43], v[56:59], v[112:127]\n" \
"v_add_f32 v81, 1.0, v81\n" \
"v_add_f32 v82, 1.0, v82\n" \
"v_add_f32 v83, 1.0, v83\n" \
"v_add_f32 v84, 1.0, v84\n" \
"v_add_f32 v85, 1.0, v85\n" \
"v_add_f32 v86, 1.0, v86\n" \
"v_mfma_f32_32x32x16_bf16 v[128:143], a[184:187], v[56:59], v[128:143]\n" \
"s_waitcnt lgkmcnt(6)\n" \
"ds_read_b128 v[56:59], v111 offset:18432\n" \
"v_add_f32 v87, 1.0, v87\n" \
"v_rcp_f32 v80, v80\n" \
"v_rcp_f32 v81, v81\n" \
"v_mfma_f32_32x32x16_bf16 v[112:127], a[44:47], v[60:63], v[112:127]\n" \
"global_load_lds_dwordx4 v102, s[46:47] offset:2048 sc1\n" \
"v_rcp_f32 v82, v82\n" \
"v_rcp_f32 v83, v83\n" \
"v_mfma_f32_32x32x16_bf16 v[128:143], a[188:191], v[60:63], v[128:143]\n" \
"s_waitcnt lgkmcnt(6)\n" \
"ds_read_b128 v[60:63], v111 offset:19456\n" \
"v_rcp_f32 v84, v84\n" \
"v_rcp_f32 v85, v85\n" \
"v_mfma_f32_32x32x16_bf16 v[112:127], a[48:51], v[32:35], v[112:127]\n" \
"v_rcp_f32 v86, v86\n" \
"v_rcp_f32 v87, v87\n" \
"v_mul_f32 v80, v80, v4\n" \
"v_mul_f32 v81, v81, v5\n" \
"v_mfma_f32_32x32x16_bf16 v[128:143], a[192:195], v[32:35], v[128:143]\n" \
"s_waitcnt lgkmcnt(6)\n" \
"ds_read_b128 v[32:35], v111 offset:20480\n" \
"v_mul_f32 v82, v82, v6\n" \
"v_mul_f32 v83, v83, v7\n" \
"v_mul_f32 v84, v84, v12\n" \
"v_mul_f32 v85, v85, v13\n" \
"v_mul_f32 v86, v86, v14\n" \
"v_mfma_f32_32x32x16_bf16 v[112:127], a[52:55], v[36:39], v[112:127]\n" \
"global_load_lds_dwordx4 v102, s[46:47] offset:3072 sc1\n" \
"v_mul_f32 v87, v87, v15\n" \
"v_cvt_pk_bf16_f32 v68, v80, v81\n" \
"v_cvt_pk_bf16_f32 v69, v82, v83\n" \
"v_cvt_pk_bf16_f32 v70, v84, v85\n" \
"v_mfma_f32_32x32x16_bf16 v[128:143], a[196:199], v[36:39], v[128:143]\n" \
"s_waitcnt lgkmcnt(6)\n" \
"ds_read_b128 v[36:39], v111 offset:21504\n" \
"v_cvt_pk_bf16_f32 v71, v86, v87\n" \
"v_mfma_f32_32x32x16_bf16 v[112:127], a[56:59], v[40:43], v[112:127]\n" \
"global_store_dwordx4 v105, v[68:71], s[48:49] offset:512 sc1\n" \
"v_mul_f32 v80, s75, v16\n" \
"v_mul_f32 v81, s75, v17\n" \
"v_mul_f32 v82, s75, v18\n" \
"v_mul_f32 v83, s75, v19\n" \
"v_mfma_f32_32x32x16_bf16 v[128:143], a[200:203], v[40:43], v[128:143]\n" \
"s_waitcnt lgkmcnt(6)\n" \
"ds_read_b128 v[40:43], v111 offset:22528\n" \
"v_mul_f32 v84, s75, v24\n" \
"v_mul_f32 v85, s75, v25\n" \
"v_mul_f32 v86, s75, v26\n" \
"v_mul_f32 v87, s75, v27\n" \
"v_mfma_f32_32x32x16_bf16 v[112:127], a[60:63], v[44:47], v[112:127]\n" \
"s_add_u32 m0, s60, 4096\n" \
"s_nop 0\n" \
"global_load_lds_dwordx4 v103, s[46:47] offset:0 sc1\n" \
"v_exp_f32 v80, v80\n" \
"v_exp_f32 v81, v81\n" \
"v_mfma_f32_32x32x16_bf16 v[128:143], a[204:207], v[44:47], v[128:143]\n" \
"s_waitcnt lgkmcnt(6)\n" \
"ds_read_b128 v[44:47], v111 offset:23552\n" \
"v_exp_f32 v82, v82\n" \
"v_exp_f32 v83, v83\n" \
"v_mfma_f32_32x32x16_bf16 v[112:127], a[64:67], v[48:51], v[112:127]\n" \
"v_exp_f32 v84, v84\n" \
"v_exp_f32 v85, v85\n" \
"v_exp_f32 v86, v86\n" \
"v_mfma_f32_32x32x16_bf16 v[128:143], a[208:211], v[48:51], v[128:143]\n" \
"s_waitcnt lgkmcnt(6)\n" \
"ds_read_b128 v[48:51], v111 offset:24576\n" \
"v_exp_f32 v87, v87\n" \
"v_add_f32 v80, 1.0, v80\n" \
"v_add_f32 v81, 1.0, v81\n" \
"v_add_f32 v82, 1.0, v82\n" \
"v_mfma_f32_32x32x16_bf16 v[112:127], a[68:71], v[52:55], v[112:127]\n" \
"global_load_lds_dwordx4 v103, s[46:47] offset:1024 sc1\n" \
"v_add_f32 v83, 1.0, v83\n" \
"v_add_f32 v84, 1.0, v84\n" \
"v_add_f32 v85, 1.0, v85\n" \
"v_add_f32 v86, 1.0, v86\n" \
"v_mfma_f32_32x32x16_bf16 v[128:143], a[212:215], v[52:55], v[128:143]\n" \
"s_waitcnt lgkmcnt(6)\n" \
"ds_read_b128 v[52:55], v111 offset:25600\n" \
"v_add_f32 v87, 1.0, v87\n" \
"v_rcp_f32 v80, v80\n" \
"v_rcp_f32 v81, v81\n" \
"v_mfma_f32_32x32x16_bf16 v[112:127], a[72:75], v[56:59], v[112:127]\n" \
"v_rcp_f32 v82, v82\n" \
"v_rcp_f32 v83, v83\n" \
"v_rcp_f32 v84, v84\n" \
"v_mfma_f32_32x32x16_bf16 v[128:143], a[216:219], v[56:59], v[128:143]\n" \
"s_waitcnt lgkmcnt(6)\n" \
"ds_read_b128 v[56:59], v111 offset:26624\n" \
"v_rcp_f32 v85, v85\n" \
"v_rcp_f32 v86, v86\n" \
"v_mfma_f32_32x32x16_bf16 v[112:127], a[76:79], v[60:63], v[112:127]\n" \
"global_load_lds_dwordx4 v103, s[46:47] offset:2048 sc1\n" \
"v_rcp_f32 v87, v87\n" \
"v_mul_f32 v80, v80, v16\n" \
"v_mul_f32 v81, v81, v17\n" \
"v_mfma_f32_32x32x16_bf16 v[128:143], a[220:223], v[60:63], v[128:143]\n" \
"s_waitcnt lgkmcnt(6)\n" \
"ds_read_b128 v[60:63], v111 offset:27648\n" \
"v_mov_b32 v149, s63\n" \
"v_mul_f32 v82, v82, v18\n" \
"v_mul_f32 v83, v83, v19\n" \
"v_mul_f32 v84, v84, v24\n" \
"v_mul_f32 v85, v85, v25\n" \
"v_mfma_f32_32x32x16_bf16 v[112:127], a[80:83], v[32:35], v[112:127]\n" \
"s_waitcnt vmcnt(9)\n" \
"s_mov_b64 exec, 1\n" \
"global_store_dword v107, v149, s[52:53] sc1\n" \
"s_mov_b64 exec, -1\n" \
"v_mul_f32 v86, v86, v26\n" \
"v_mul_f32 v87, v87, v27\n" \
"v_mfma_f32_32x32x16_bf16 v[128:143], a[224:227], v[32:35], v[128:143]\n" \
"s_waitcnt lgkmcnt(6)\n" \
"ds_read_b128 v[32:35], v111 offset:28672\n" \
"v_cvt_pk_bf16_f32 v72, v80, v81\n" \
"v_cvt_pk_bf16_f32 v73, v82, v83\n" \
"v_cvt_pk_bf16_f32 v74, v84, v85\n" \
"v_cvt_pk_bf16_f32 v75, v86, v87\n" \
"v_mfma_f32_32x32x16_bf16 v[112:127], a[84:87], v[36:39], v[112:127]\n" \
"global_store_dwordx4 v106, v[72:75], s[48:49] offset:0 sc1\n" \
"s_mov_b32 s63, s58\n" \
"s_add_u32 s58, s58, s59\n" \
"v_mul_f32 v80, s75, v20\n" \
"v_mul_f32 v81, s75, v21\n" \
"v_mfma_f32_32x32x16_bf16 v[128:143], a[228:231], v[36:39], v[128:143]\n" \
"s_waitcnt lgkmcnt(6)\n" \
"ds_read_b128 v[36:39], v111 offset:29696\n" \
"v_mul_f32 v82, s75, v22\n" \
"v_mul_f32 v83, s75, v23\n" \
"v_mul_f32 v84, s75, v28\n" \
"v_mul_f32 v85, s75, v29\n" \
"v_mul_f32 v86, s75, v30\n" \
"v_mfma_f32_32x32x16_bf16 v[112:127], a[88:91], v[40:43], v[112:127]\n" \
"global_load_lds_dwordx4 v103, s[46:47] offset:3072 sc1\n" \
"v_mul_f32 v87, s75, v31\n" \
"v_exp_f32 v80, v80\n" \
"v_mfma_f32_32x32x16_bf16 v[128:143], a[232:235], v[40:43], v[128:143]\n" \
"s_waitcnt lgkmcnt(6)\n" \
"ds_read_b128 v[40:43], v111 offset:30720\n" \
"v_exp_f32 v81, v81\n" \
"v_exp_f32 v82, v82\n" \
"v_mfma_f32_32x32x16_bf16 v[112:127], a[92:95], v[44:47], v[112:127]\n" \
"s_add_u32 m0, s60, 8192\n" \
"s_nop 0\n" \
"global_load_lds_dwordx4 v104, s[46:47] offset:0 sc1\n" \
"s_add_u32 s46, s46, s45\n" \
"s_addc_u32 s47, s47, 0\n" \
"v_exp_f32 v83, v83\n" \
"v_mfma_f32_32x32x16_bf16 v[128:143], a[236:239], v[44:47], v[128:143]\n" \
"s_waitcnt lgkmcnt(6)\n" \
"ds_read_b128 v[44:47], v111 offset:31744\n" \
"v_exp_f32 v84, v84\n" \
"v_exp_f32 v85, v85\n" \
"v_mfma_f32_32x32x16_bf16 v[112:127], a[96:99], v[48:51], v[112:127]\n" \
"s_add_u32 s60, s60, 36864\n" \
"v_exp_f32 v86, v86\n" \
"v_exp_f32 v87, v87\n" \
"v_add_f32 v80, 1.0, v80\n" \
"v_mfma_f32_32x32x16_bf16 v[128:143], a[240:243], v[48:51], v[128:143]\n" \
"s_waitcnt lgkmcnt(6)\n" \
"ds_read_b128 v[48:51], v111 offset:32768\n" \
"v_add_f32 v81, 1.0, v81\n" \
"v_add_f32 v82, 1.0, v82\n" \
"v_add_f32 v83, 1.0, v83\n" \
"v_add_f32 v84, 1.0, v84\n" \
"v_add_f32 v85, 1.0, v85\n" \
"v_mfma_f32_32x32x16_bf16 v[112:127], a[100:103], v[52:55], v[112:127]\n" \
"s_cmp_eq_u32 s60, s61\n" \
"s_cselect_b32 s60, s57, s60\n" \
"s_add_u32 s62, s62, 36864\n" \
"s_cmp_eq_u32 s62, 147456\n" \
"s_cselect_b32 s62, 0, s62\n" \
"v_add_f32 v86, 1.0, v86\n" \
"v_add_f32 v87, 1.0, v87\n" \
"v_mfma_f32_32x32x16_bf16 v[128:143], a[244:247], v[52:55], v[128:143]\n" \
"s_waitcnt lgkmcnt(6)\n" \
"ds_read_b128 v[52:55], v111 offset:33792\n" \
"v_rcp_f32 v80, v80\n" \
"v_rcp_f32 v81, v81\n" \
"v_mfma_f32_32x32x16_bf16 v[112:127], a[104:107], v[56:59], v[112:127]\n" \
"v_rcp_f32 v82, v82\n" \
"v_rcp_f32 v83, v83\n" \
"v_rcp_f32 v84, v84\n" \
"v_mfma_f32_32x32x16_bf16 v[128:143], a[248:251], v[56:59], v[128:143]\n" \
"s_waitcnt lgkmcnt(6)\n" \
"ds_read_b128 v[56:59], v111 offset:34816\n" \
"v_rcp_f32 v85, v85\n" \
"v_rcp_f32 v86, v86\n" \
"v_mfma_f32_32x32x16_bf16 v[112:127], a[108:111], v[60:63], v[112:127]\n" \
"v_rcp_f32 v87, v87\n" \
"v_mul_f32 v80, v80, v20\n" \
"v_mul_f32 v81, v81, v21\n" \
"v_mul_f32 v82, v82, v22\n" \
"v_mul_f32 v83, v83, v23\n" \
"v_mfma_f32_32x32x16_bf16 v[128:143], a[252:255], v[60:63], v[128:143]\n" \
"s_waitcnt lgkmcnt(6)\n" \
"ds_read_b128 v[60:63], v111 offset:35840\n" \
"v_add_u32 v111, s62, v101\n" \
"v_mul_f32 v84, v84, v28\n" \
"v_mul_f32 v85, v85, v29\n" \
"v_mul_f32 v86, v86, v30\n" \
"v_mul_f32 v87, v87, v31\n" \
"v_mfma_f32_32x32x16_bf16 v[112:127], a[112:115], v[32:35], v[112:127]\n" \
"v_cvt_pk_bf16_f32 v76, v80, v81\n" \
"v_cvt_pk_bf16_f32 v77, v82, v83\n" \
"v_cvt_pk_bf16_f32 v78, v84, v85\n" \
"v_cvt_pk_bf16_f32 v79, v86, v87\n" \
"v_mfma_f32_32x32x16_bf16 v[128:143], v[224:227], v[32:35], v[128:143]\n" \
"s_waitcnt lgkmcnt(6)\n" \
"ds_read_b128 v[32:35], v111 offset:0\n" \
"v_mfma_f32_32x32x16_bf16 v[112:127], a[116:119], v[36:39], v[112:127]\n" \
"global_store_dwordx4 v106, v[76:79], s[48:49] offset:512 sc1\n" \
"s_add_u32 s48, s48, s50\n" \
"s_addc_u32 s49, s49, 0\n" \
"v_mfma_f32_32x32x16_bf16 v[128:143], v[228:231], v[36:39], v[128:143]\n" \
"s_waitcnt lgkmcnt(6)\n" \
"ds_read_b128 v[36:39], v111 offset:1024\n" \
"v_mfma_f32_32x32x16_bf16 v[112:127], a[120:123], v[40:43], v[112:127]\n" \
"v_mfma_f32_32x32x16_bf16 v[128:143], v[232:235], v[40:43], v[128:143]\n" \
"s_waitcnt lgkmcnt(6)\n" \
"ds_read_b128 v[40:43], v111 offset:2048\n" \
"v_mfma_f32_32x32x16_bf16 v[112:127], a[124:127], v[44:47], v[112:127]\n" \
"v_mfma_f32_32x32x16_bf16 v[128:143], v[236:239], v[44:47], v[128:143]\n" \
"s_waitcnt lgkmcnt(6)\n" \
"ds_read_b128 v[44:47], v111 offset:3072\n" \
"v_mfma_f32_32x32x16_bf16 v[112:127], a[128:131], v[48:51], v[112:127]\n" \
"v_mfma_f32_32x32x16_bf16 v[128:143], v[240:243], v[48:51], v[128:143]\n" \
"s_waitcnt lgkmcnt(6)\n" \
"ds_read_b128 v[48:51], v111 offset:4096\n" \
"v_mfma_f32_32x32x16_bf16 v[112:127], a[132:135], v[52:55], v[112:127]\n" \
"v_mfma_f32_32x32x16_bf16 v[128:143], v[244:247], v[52:55], v[128:143]\n" \
"s_waitcnt lgkmcnt(6)\n" \
"ds_read_b128 v[52:55], v111 offset:5120\n" \
"v_mfma_f32_32x32x16_bf16 v[112:127], a[136:139], v[56:59], v[112:127]\n" \
"v_mfma_f32_32x32x16_bf16 v[128:143], v[248:251], v[56:59], v[128:143]\n" \
"s_waitcnt lgkmcnt(6)\n" \
"ds_read_b128 v[56:59], v111 offset:6144\n" \
"v_mfma_f32_32x32x16_bf16 v[112:127], a[140:143], v[60:63], v[112:127]\n" \
"s_sub_u32 s51, s51, 1\n" \
"s_cmp_eq_u32 s51, 0\n" \
"v_mfma_f32_32x32x16_bf16 v[128:143], v[252:255], v[60:63], v[128:143]\n" \
"s_waitcnt lgkmcnt(6)\n" \
"ds_read_b128 v[60:63], v111 offset:7168\n" \
"s_cbranch_scc1 .Lfin%=\n" \
"v_mfma_f32_32x32x16_bf16 v[0:15], a[0:3], v[32:35], 0\n" \
"s_barrier\n" \
"v_mfma_f32_32x32x16_bf16 v[16:31], a[144:147], v[32:35], 0\n" \
"s_waitcnt lgkmcnt(6)\n" \
"ds_read_b128 v[32:35], v111 offset:8192\n" \
"v_mfma_f32_32x32x16_bf16 v[0:15], a[4:7], v[36:39], v[0:15]\n" \
"v_mul_f32 v80, s75, v112\n" \
"v_mul_f32 v81, s75, v113\n" \
"v_mul_f32 v82, s75, v114\n" \
"v_mul_f32 v83, s75, v115\n" \
"v_mul_f32 v84, s75, v120\n" \
"v_mul_f32 v85, s75, v121\n" \
"v_mfma_f32_32x32x16_bf16 v[16:31], a[148:151], v[36:39], v[16:31]\n" \
"s_waitcnt lgkmcnt(6)\n" \
"ds_read_b128 v[36:39], v111 offset:9216\n" \
"v_mul_f32 v86, s75, v122\n" \
"v_mul_f32 v87, s75, v123\n" \
"v_exp_f32 v80, v80\n" \
"v_mfma_f32_32x32x16_bf16 v[0:15], a[8:11], v[40:43], v[0:15]\n" \
"v_exp_f32 v81, v81\n" \
"v_exp_f32 v82, v82\n" \
"v_exp_f32 v83, v83\n" \
"v_mfma_f32_32x32x16_bf16 v[16:31], a[152:155], v[40:43], v[16:31]\n" \
"s_waitcnt lgkmcnt(6)\n" \
"ds_read_b128 v[40:43], v111 offset:10240\n" \
"v_exp_f32 v84, v84\n" \
"v_exp_f32 v85, v85\n" \
"v_mfma_f32_32x32x16_bf16 v[0:15], a[12:15], v[44:47], v[0:15]\n" \
"v_exp_f32 v86, v86\n" \
"v_exp_f32 v87, v87\n" \
"v_add_f32 v80, 1.0, v80\n" \
"v_add_f32 v81, 1.0, v81\n" \
"v_mfma_f32_32x32x16_bf16 v[16:31], a[156:159], v[44:47], v[16:31]\n" \
"s_waitcnt lgkmcnt(6)\n" \
"ds_read_b128 v[44:47], v111 offset:11264\n" \
"v_add_f32 v82, 1.0, v82\n" \
"v_add_f32 v83, 1.0, v83\n" \
"v_add_f32 v84, 1.0, v84\n" \
"v_add_f32 v85, 1.0, v85\n" \
"v_add_f32 v86, 1.0, v86\n" \
"v_mfma_f32_32x32x16_bf16 v[0:15], a[16:19], v[48:51], v[0:15]\n" \
"v_add_f32 v87, 1.0, v87\n" \
"v_rcp_f32 v80, v80\n" \
"v_rcp_f32 v81, v81\n" \
"v_mfma_f32_32x32x16_bf16 v[16:31], a[160:163], v[48:51], v[16:31]\n" \
"s_waitcnt lgkmcnt(6)\n" \
"ds_read_b128 v[48:51], v111 offset:12288\n" \
"v_rcp_f32 v82, v82\n" \
"v_rcp_f32 v83, v83\n" \
"v_mfma_f32_32x32x16_bf16 v[0:15], a[20:23], v[52:55], v[0:15]\n" \
"v_rcp_f32 v84, v84\n" \
"v_rcp_f32 v85, v85\n" \
"v_rcp_f32 v86, v86\n" \
"v_mfma_f32_32x32x16_bf16 v[16:31], a[164:167], v[52:55], v[16:31]\n" \
"s_waitcnt lgkmcnt(6)\n" \
"ds_read_b128 v[52:55], v111 offset:13312\n" \
"v_rcp_f32 v87, v87\n" \
"v_mul_f32 v80, v80, v112\n" \
"v_mul_f32 v81, v81, v113\n" \
"v_mul_f32 v82, v82, v114\n" \
"v_mfma_f32_32x32x16_bf16 v[0:15], a[24:27], v[56:59], v[0:15]\n" \
"v_mul_f32 v83, v83, v115\n" \
"v_mul_f32 v84, v84, v120\n" \
"v_mul_f32 v85, v85, v121\n" \
"v_mul_f32 v86, v86, v122\n" \
"v_mul_f32 v87, v87, v123\n" \
"v_cvt_pk_bf16_f32 v64, v80, v81\n" \
"v_mfma_f32_32x32x16_bf16 v[16:31], a[168:171], v[56:59], v[16:31]\n" \
"s_waitcnt lgkmcnt(6)\n" \
"ds_read_b128 v[56:59], v111 offset:14336\n" \
"v_cvt_pk_bf16_f32 v65, v82, v83\n" \
"v_cvt_pk_bf16_f32 v66, v84, v85\n" \
"v_cvt_pk_bf16_f32 v67, v86, v87\n" \
"v_mfma_f32_32x32x16_bf16 v[0:15], a[28:31], v[60:63], v[0:15]\n" \
"global_store_dwordx4 v105, v[64:67], s[48:49] offset:0 sc1\n" \
"v_mul_f32 v80, s75, v116\n" \
"v_mul_f32 v81, s75, v117\n" \
"v_mul_f32 v82, s75, v118\n" \
"v_mul_f32 v83, s75, v119\n" \
"v_mfma_f32_32x32x16_bf16 v[16:31], a[172:175], v[60:63], v[16:31]\n" \
"s_waitcnt lgkmcnt(6)\n" \
"ds_read_b128 v[60:63], v111 offset:15360\n" \
"v_mul_f32 v84, s75, v124\n" \
"v_mul_f32 v85, s75, v125\n" \
"v_mul_f32 v86, s75, v126\n" \
"v_mul_f32 v87, s75, v127\n" \
"v_mfma_f32_32x32x16_bf16 v[0:15], a[32:35], v[32:35], v[0:15]\n" \
"s_add_u32 m0, s60, 0\n" \
"s_nop 0\n" \
"global_load_lds_dwordx4 v102, s[46:47] offset:0 sc1\n" \
"v_exp_f32 v80, v80\n" \
"v_exp_f32 v81, v81\n" \
"v_mfma_f32_32x32x16_bf16 v[16:31], a[176:179], v[32:35], v[16:31]\n" \
"s_waitcnt lgkmcnt(6)\n" \
"ds_read_b128 v[32:35], v111 offset:16384\n" \
"v_exp_f32 v82, v82\n" \
"v_exp_f32 v83, v83\n" \
"v_mfma_f32_32x32x16_bf16 v[0:15], a[36:39], v[36:39], v[0:15]\n" \
"global_load_lds_dwordx4 v102, s[46:47] offset:1024 sc1\n" \
"v_exp_f32 v84, v84\n" \
"v_exp_f32 v85, v85\n" \
"v_mfma_f32_32x32x16_bf16 v[16:31], a[180:183], v[36:39], v[16:31]\n" \
"s_waitcnt lgkmcnt(6)\n" \
"ds_read_b128 v[36:39], v111 offset:17408\n" \
"v_exp_f32 v86, v86\n" \
"v_exp_f32 v87, v87\n" \
"v_add_f32 v80, 1.0, v80\n" \
"v_mfma_f32_32x32x16_bf16 v[0:15], a[40:43], v[40:43], v[0:15]\n" \
"v_add_f32 v81, 1.0, v81\n" \
"v_add_f32 v82, 1.0, v82\n" \
"v_add_f32 v83, 1.0, v83\n" \
"v_add_f32 v84, 1.0, v84\n" \
"v_add_f32 v85, 1.0, v85\n" \
"v_add_f32 v86, 1.0, v86\n" \
"v_mfma_f32_32x32x16_bf16 v[16:31], a[184:187], v[40:43], v[16:31]\n" \
"s_waitcnt lgkmcnt(6)\n" \
"ds_read_b128 v[40:43], v111 offset:18432\n" \
"v_add_f32 v87, 1.0, v87\n" \
"v_rcp_f32 v80, v80\n" \
"v_rcp_f32 v81, v81\n" \
"v_mfma_f32_32x32x16_bf16 v[0:15], a[44:47], v[44:47], v[0:15]\n" \
"global_load_lds_dwordx4 v102, s[46:47] offset:2048 sc1\n" \
"v_rcp_f32 v82, v82\n" \
"v_rcp_f32 v83, v83\n" \
"v_mfma_f32_32x32x16_bf16 v[16:31], a[188:191], v[44:47], v[16:31]\n" \
"s_waitcnt lgkmcnt(6)\n" \
"ds_read_b128 v[44:47], v111 offset:19456\n" \
"v_rcp_f32 v84, v84\n" \
"v_rcp_f32 v85, v85\n" \
"v_mfma_f32_32x32x16_bf16 v[0:15], a[48:51], v[48:51], v[0:15]\n" \
"v_rcp_f32 v86, v86\n" \
"v_rcp_f32 v87, v87\n" \
"v_mul_f32 v80, v80, v116\n" \
"v_mul_f32 v81, v81, v117\n" \
"v_mfma_f32_32x32x16_bf16 v[16:31], a[192:195], v[48:51], v[16:31]\n" \
"s_waitcnt lgkmcnt(6)\n" \
"ds_read_b128 v[48:51], v111 offset:20480\n" \
"v_mul_f32 v82, v82, v118\n" \
"v_mul_f32 v83, v83, v119\n" \
"v_mul_f32 v84, v84, v124\n" \
"v_mul_f32 v85, v85, v125\n" \
"v_mul_f32 v86, v86, v126\n" \
"v_mfma_f32_32x32x16_bf16 v[0:15], a[52:55], v[52:55], v[0:15]\n" \
"global_load_lds_dwordx4 v102, s[46:47] offset:3072 sc1\n" \
"v_mul_f32 v87, v87, v127\n" \
"v_cvt_pk_bf16_f32 v68, v80, v81\n" \
"v_cvt_pk_bf16_f32 v69, v82, v83\n" \
"v_cvt_pk_bf16_f32 v70, v84, v85\n" \
"v_mfma_f32_32x32x16_bf16 v[16:31], a[196:199], v[52:55], v[16:31]\n" \
"s_waitcnt lgkmcnt(6)\n" \
"ds_read_b128 v[52:55], v111 offset:21504\n" \
"v_cvt_pk_bf16_f32 v71, v86, v87\n" \
"v_mfma_f32_32x32x16_bf16 v[0:15], a[56:59], v[56:59], v[0:15]\n" \
"global_store_dwordx4 v105, v[68:71], s[48:49] offset:512 sc1\n" \
"v_mul_f32 v80, s75, v128\n" \
"v_mul_f32 v81, s75, v129\n" \
"v_mul_f32 v82, s75, v130\n" \
"v_mul_f32 v83, s75, v131\n" \
"v_mfma_f32_32x32x16_bf16 v[16:31], a[200:203], v[56:59], v[16:31]\n" \
"s_waitcnt lgkmcnt(6)\n" \
"ds_read_b128 v[56:59], v111 offset:22528\n" \
"v_mul_f32 v84, s75, v136\n" \
"v_mul_f32 v85, s75, v137\n" \
"v_mul_f32 v86, s75, v138\n" \
"v_mul_f32 v87, s75, v139\n" \
"v_mfma_f32_32x32x16_bf16 v[0:15], a[60:63], v[60:63], v[0:15]\n" \
"s_add_u32 m0, s60, 4096\n" \
"s_nop 0\n" \
"global_load_lds_dwordx4 v103, s[46:47] offset:0 sc1\n" \
"v_exp_f32 v80, v80\n" \
"v_exp_f32 v81, v81\n" \
"v_mfma_f32_32x32x16_bf16 v[16:31], a[204:207], v[60:63], v[16:31]\n" \
"s_waitcnt lgkmcnt(6)\n" \
"ds_read_b128 v[60:63], v111 offset:23552\n" \
"v_exp_f32 v82, v82\n" \
"v_exp_f32 v83, v83\n" \
"v_mfma_f32_32x32x16_bf16 v[0:15], a[64:67], v[32:35], v[0:15]\n" \
"v_exp_f32 v84, v84\n" \
"v_exp_f32 v85, v85\n" \
"v_exp_f32 v86, v86\n" \
"v_mfma_f32_32x32x16_bf16 v[16:31], a[208:211], v[32:35], v[16:31]\n" \
"s_waitcnt lgkmcnt(6)\n" \
"ds_read_b128 v[32:35], v111 offset:24576\n" \
"v_exp_f32 v87, v87\n" \
"v_add_f32 v80, 1.0, v80\n" \
"v_add_f32 v81, 1.0, v81\n" \
"v_add_f32 v82, 1.0, v82\n" \
"v_mfma_f32_32x32x16_bf16 v[0:15], a[68:71], v[36:39], v[0:15]\n" \
"global_load_lds_dwordx4 v103, s[46:47] offset:1024 sc1\n" \
"v_add_f32 v83, 1.0, v83\n" \
"v_add_f32 v84, 1.0, v84\n" \
"v_add_f32 v85, 1.0, v85\n" \
"v_add_f32 v86, 1.0, v86\n" \
"v_mfma_f32_32x32x16_bf16 v[16:31], a[212:215], v[36:39], v[16:31]\n" \
"s_waitcnt lgkmcnt(6)\n" \
"ds_read_b128 v[36:39], v111 offset:25600\n" \
"v_add_f32 v87, 1.0, v87\n" \
"v_rcp_f32 v80, v80\n" \
"v_rcp_f32 v81, v81\n" \
"v_mfma_f32_32x32x16_bf16 v[0:15], a[72:75], v[40:43], v[0:15]\n" \
"v_rcp_f32 v82, v82\n" \
"v_rcp_f32 v83, v83\n" \
"v_rcp_f32 v84, v84\n" \
"v_mfma_f32_32x32x16_bf16 v[16:31], a[216:219], v[40:43], v[16:31]\n" \
"s_waitcnt lgkmcnt(6)\n" \
"ds_read_b128 v[40:43], v111 offset:26624\n" \
"v_rcp_f32 v85, v85\n" \
"v_rcp_f32 v86, v86\n" \
"v_mfma_f32_32x32x16_bf16 v[0:15], a[76:79], v[44:47], v[0:15]\n" \
"global_load_lds_dwordx4 v103, s[46:47] offset:2048 sc1\n" \
"v_rcp_f32 v87, v87\n" \
"v_mul_f32 v80, v80, v128\n" \
"v_mul_f32 v81, v81, v129\n" \
"v_mfma_f32_32x32x16_bf16 v[16:31], a[220:223], v[44:47], v[16:31]\n" \
"s_waitcnt lgkmcnt(6)\n" \
"ds_read_b128 v[44:47], v111 offset:27648\n" \
"v_mov_b32 v149, s63\n" \
"v_mul_f32 v82, v82, v130\n" \
"v_mul_f32 v83, v83, v131\n" \
"v_mul_f32 v84, v84, v136\n" \
"v_mul_f32 v85, v85, v137\n" \
"v_mfma_f32_32x32x16_bf16 v[0:15], a[80:83], v[48:51], v[0:15]\n" \
"s_waitcnt vmcnt(9)\n" \
"s_mov_b64 exec, 1\n" \
"global_store_dword v107, v149, s[52:53] sc1\n" \
"s_mov_b64 exec, -1\n" \
"v_mul_f32 v86, v86, v138\n" \
"v_mul_f32 v87, v87, v139\n" \
"v_mfma_f32_32x32x16_bf16 v[16:31], a[224:227], v[48:51], v[16:31]\n" \
"s_waitcnt lgkmcnt(6)\n" \
"ds_read_b128 v[48:51], v111 offset:28672\n" \
"v_cvt_pk_bf16_f32 v72, v80, v81\n" \
"v_cvt_pk_bf16_f32 v73, v82, v83\n" \
"v_cvt_pk_bf16_f32 v74, v84, v85\n" \
"v_cvt_pk_bf16_f32 v75, v86, v87\n" \
"v_mfma_f32_32x32x16_bf16 v[0:15], a[84:87], v[52:55], v[0:15]\n" \
"global_store_dwordx4 v106, v[72:75], s[48:49] offset:0 sc1\n" \
"s_mov_b32 s63, s58\n" \
"s_add_u32 s58, s58, s59\n" \
"v_mul_f32 v80, s75, v132\n" \
"v_mul_f32 v81, s75, v133\n" \
"v_mfma_f32_32x32x16_bf16 v[16:31], a[228:231], v[52:55], v[16:31]\n" \
"s_waitcnt lgkmcnt(6)\n" \
"ds_read_b128 v[52:55], v111 offset:29696\n" \
"v_mul_f32 v82, s75, v134\n" \
"v_mul_f32 v83, s75, v135\n" \
"v_mul_f32 v84, s75, v140\n" \
"v_mul_f32 v85, s75, v141\n" \
"v_mul_f32 v86, s75, v142\n" \
"v_mfma_f32_32x32x16_bf16 v[0:15], a[88:91], v[56:59], v[0:15]\n" \
"global_load_lds_dwordx4 v103, s[46:47] offset:3072 sc1\n" \
"v_mul_f32 v87, s75, v143\n" \
"v_exp_f32 v80, v80\n" \
"v_mfma_f32_32x32x16_bf16 v[16:31], a[232:235], v[56:59], v[16:31]\n" \
"s_waitcnt lgkmcnt(6)\n" \
"ds_read_b128 v[56:59], v111 offset:30720\n" \
"v_exp_f32 v81, v81\n" \
"v_exp_f32 v82, v82\n" \
"v_mfma_f32_32x32x16_bf16 v[0:15], a[92:95], v[60:63], v[0:15]\n" \
"s_add_u32 m0, s60, 8192\n" \
"s_nop 0\n" \
"global_load_lds_dwordx4 v104, s[46:47] offset:0 sc1\n" \
"s_add_u32 s46, s46, s45\n" \
"s_addc_u32 s47, s47, 0\n" \
"v_exp_f32 v83, v83\n" \
"v_mfma_f32_32x32x16_bf16 v[16:31], a[236:239], v[60:63], v[16:31]\n" \
"s_waitcnt lgkmcnt(6)\n" \
"ds_read_b128 v[60:63], v111 offset:31744\n" \
"v_exp_f32 v84, v84\n" \
"v_exp_f32 v85, v85\n" \
"v_mfma_f32_32x32x16_bf16 v[0:15], a[96:99], v[32:35], v[0:15]\n" \
"s_add_u32 s60, s60, 36864\n" \
"v_exp_f32 v86, v86\n" \
"v_exp_f32 v87, v87\n" \
"v_add_f32 v80, 1.0, v80\n" \
"v_mfma_f32_32x32x16_bf16 v[16:31], a[240:243], v[32:35], v[16:31]\n" \
"s_waitcnt lgkmcnt(6)\n" \
"ds_read_b128 v[32:35], v111 offset:32768\n" \
"v_add_f32 v81, 1.0, v81\n" \
"v_add_f32 v82, 1.0, v82\n" \
"v_add_f32 v83, 1.0, v83\n" \
"v_add_f32 v84, 1.0, v84\n" \
"v_add_f32 v85, 1.0, v85\n" \
"v_mfma_f32_32x32x16_bf16 v[0:15], a[100:103], v[36:39], v[0:15]\n" \
"s_cmp_eq_u32 s60, s61\n" \
"s_cselect_b32 s60, s57, s60\n" \
"s_add_u32 s62, s62, 36864\n" \
"s_cmp_eq_u32 s62, 147456\n" \
"s_cselect_b32 s62, 0, s62\n" \
"v_add_f32 v86, 1.0, v86\n" \
"v_add_f32 v87, 1.0, v87\n" \
"v_mfma_f32_32x32x16_bf16 v[16:31], a[244:247], v[36:39], v[16:31]\n" \
"s_waitcnt lgkmcnt(6)\n" \
"ds_read_b128 v[36:39], v111 offset:33792\n" \
"v_rcp_f32 v80, v80\n" \
"v_rcp_f32 v81, v81\n" \
"v_mfma_f32_32x32x16_bf16 v[0:15], a[104:107], v[40:43], v[0:15]\n" \
"v_rcp_f32 v82, v82\n" \
"v_rcp_f32 v83, v83\n" \
"v_rcp_f32 v84, v84\n" \
"v_mfma_f32_32x32x16_bf16 v[16:31], a[248:251], v[40:43], v[16:31]\n" \
"s_waitcnt lgkmcnt(6)\n" \
"ds_read_b128 v[40:43], v111 offset:34816\n" \
"v_rcp_f32 v85, v85\n" \
"v_rcp_f32 v86, v86\n" \
"v_mfma_f32_32x32x16_bf16 v[0:15], a[108:111], v[44:47], v[0:15]\n" \
"v_rcp_f32 v87, v87\n" \
"v_mul_f32 v80, v80, v132\n" \
"v_mul_f32 v81, v81, v133\n" \
"v_mul_f32 v82, v82, v134\n" \
"v_mul_f32 v83, v83, v135\n" \
"v_mfma_f32_32x32x16_bf16 v[16:31], a[252:255], v[44:47], v[16:31]\n" \
"s_waitcnt lgkmcnt(6)\n" \
"ds_read_b128 v[44:47], v111 offset:35840\n" \
"v_add_u32 v111, s62, v101\n" \
"v_mul_f32 v84, v84, v140\n" \
"v_mul_f32 v85, v85, v141\n" \
"v_mul_f32 v86, v86, v142\n" \
"v_mul_f32 v87, v87, v143\n" \
"v_mfma_f32_32x32x16_bf16 v[0:15], a[112:115], v[48:51], v[0:15]\n" \
"v_cvt_pk_bf16_f32 v76, v80, v81\n" \
"v_cvt_pk_bf16_f32 v77, v82, v83\n" \
"v_cvt_pk_bf16_f32 v78, v84, v85\n" \
"v_cvt_pk_bf16_f32 v79, v86, v87\n" \
"v_mfma_f32_32x32x16_bf16 v[16:31], v[224:227], v[48:51], v[16:31]\n" \
"s_waitcnt lgkmcnt(6)\n" \
"ds_read_b128 v[48:51], v111 offset:0\n" \
"v_mfma_f32_32x32x16_bf16 v[0:15], a[116:119], v[52:55], v[0:15]\n" \
"global_store_dwordx4 v106, v[76:79], s[48:49] offset:512 sc1\n" \
"s_add_u32 s48, s48, s50\n" \
"s_addc_u32 s49, s49, 0\n" \
"v_mfma_f32_32x32x16_bf16 v[16:31], v[228:231], v[52:55], v[16:31]\n" \
"s_waitcnt lgkmcnt(6)\n" \
"ds_read_b128 v[52:55], v111 offset:1024\n" \
"v_mfma_f32_32x32x16_bf16 v[0:15], a[120:123], v[56:59], v[0:15]\n" \
"v_mfma_f32_32x32x16_bf16 v[16:31], v[232:235], v[56:59], v[16:31]\n" \
"s_waitcnt lgkmcnt(6)\n" \
"ds_read_b128 v[56:59], v111 offset:2048\n" \
"v_mfma_f32_32x32x16_bf16 v[0:15], a[124:127], v[60:63], v[0:15]\n" \
"v_mfma_f32_32x32x16_bf16 v[16:31], v[236:239], v[60:63], v[16:31]\n" \
"s_waitcnt lgkmcnt(6)\n" \
"ds_read_b128 v[60:63], v111 offset:3072\n" \
"v_mfma_f32_32x32x16_bf16 v[0:15], a[128:131], v[32:35], v[0:15]\n" \
"v_mfma_f32_32x32x16_bf16 v[16:31], v[240:243], v[32:35], v[16:31]\n" \
"s_waitcnt lgkmcnt(6)\n" \
"ds_read_b128 v[32:35], v111 offset:4096\n" \
"v_mfma_f32_32x32x16_bf16 v[0:15], a[132:135], v[36:39], v[0:15]\n" \
"v_mfma_f32_32x32x16_bf16 v[16:31], v[244:247], v[36:39], v[16:31]\n" \
"s_waitcnt lgkmcnt(6)\n" \
"ds_read_b128 v[36:39], v111 offset:5120\n" \
"v_mfma_f32_32x32x16_bf16 v[0:15], a[136:139], v[40:43], v[0:15]\n" \
"v_mfma_f32_32x32x16_bf16 v[16:31], v[248:251], v[40:43], v[16:31]\n" \
"s_waitcnt lgkmcnt(6)\n" \
"ds_read_b128 v[40:43], v111 offset:6144\n" \
"v_mfma_f32_32x32x16_bf16 v[0:15], a[140:143], v[44:47], v[0:15]\n" \
"v_mfma_f32_32x32x16_bf16 v[16:31], v[252:255], v[44:47], v[16:31]\n" \
"s_waitcnt lgkmcnt(6)\n" \
"ds_read_b128 v[44:47], v111 offset:7168\n" \
"s_branch .Ltop%=\n" \
".Lfin%=:\n" \
"s_nop 15\n" \
"v_mul_f32 v80, s75, v112\n" \
"v_mul_f32 v81, s75, v113\n" \
"v_mul_f32 v82, s75, v114\n" \
"v_mul_f32 v83, s75, v115\n" \
"v_mul_f32 v84, s75, v120\n" \
"v_mul_f32 v85, s75, v121\n" \
"v_mul_f32 v86, s75, v122\n" \
"v_mul_f32 v87, s75, v123\n" \
"v_exp_f32 v80, v80\n" \
"v_exp_f32 v81, v81\n" \
"v_exp_f32 v82, v82\n" \
"v_exp_f32 v83, v83\n" \
"v_exp_f32 v84, v84\n" \
"v_exp_f32 v85, v85\n" \
"v_exp_f32 v86, v86\n" \
"v_exp_f32 v87, v87\n" \
"v_add_f32 v80, 1.0, v80\n" \
"v_add_f32 v81, 1.0, v81\n" \
"v_add_f32 v82, 1.0, v82\n" \
"v_add_f32 v83, 1.0, v83\n" \
"v_add_f32 v84, 1.0, v84\n" \
"v_add_f32 v85, 1.0, v85\n" \
"v_add_f32 v86, 1.0, v86\n" \
"v_add_f32 v87, 1.0, v87\n" \
"v_rcp_f32 v80, v80\n" \
"v_rcp_f32 v81, v81\n" \
"v_rcp_f32 v82, v82\n" \
"v_rcp_f32 v83, v83\n" \
"v_rcp_f32 v84, v84\n" \
"v_rcp_f32 v85, v85\n" \
"v_rcp_f32 v86, v86\n" \
"v_rcp_f32 v87, v87\n" \
"v_mul_f32 v80, v80, v112\n" \
"v_mul_f32 v81, v81, v113\n" \
"v_mul_f32 v82, v82, v114\n" \
"v_mul_f32 v83, v83, v115\n" \
"v_mul_f32 v84, v84, v120\n" \
"v_mul_f32 v85, v85, v121\n" \
"v_mul_f32 v86, v86, v122\n" \
"v_mul_f32 v87, v87, v123\n" \
"v_cvt_pk_bf16_f32 v64, v80, v81\n" \
"v_cvt_pk_bf16_f32 v65, v82, v83\n" \
"v_cvt_pk_bf16_f32 v66, v84, v85\n" \
"v_cvt_pk_bf16_f32 v67, v86, v87\n" \
"global_store_dwordx4 v105, v[64:67], s[48:49] offset:0 sc1\n" \
"v_mul_f32 v80, s75, v116\n" \
"v_mul_f32 v81, s75, v117\n" \
"v_mul_f32 v82, s75, v118\n" \
"v_mul_f32 v83, s75, v119\n" \
"v_mul_f32 v84, s75, v124\n" \
"v_mul_f32 v85, s75, v125\n" \
"v_mul_f32 v86, s75, v126\n" \
"v_mul_f32 v87, s75, v127\n" \
"v_exp_f32 v80, v80\n" \
"v_exp_f32 v81, v81\n" \
"v_exp_f32 v82, v82\n" \
"v_exp_f32 v83, v83\n" \
"v_exp_f32 v84, v84\n" \
"v_exp_f32 v85, v85\n" \
"v_exp_f32 v86, v86\n" \
"v_exp_f32 v87, v87\n" \
"v_add_f32 v80, 1.0, v80\n" \
"v_add_f32 v81, 1.0, v81\n" \
"v_add_f32 v82, 1.0, v82\n" \
"v_add_f32 v83, 1.0, v83\n" \
"v_add_f32 v84, 1.0, v84\n" \
"v_add_f32 v85, 1.0, v85\n" \
"v_add_f32 v86, 1.0, v86\n" \
"v_add_f32 v87, 1.0, v87\n" \
"v_rcp_f32 v80, v80\n" \
"v_rcp_f32 v81, v81\n" \
"v_rcp_f32 v82, v82\n" \
"v_rcp_f32 v83, v83\n" \
"v_rcp_f32 v84, v84\n" \
"v_rcp_f32 v85, v85\n" \
"v_rcp_f32 v86, v86\n" \
"v_rcp_f32 v87, v87\n" \
"v_mul_f32 v80, v80, v116\n" \
"v_mul_f32 v81, v81, v117\n" \
"v_mul_f32 v82, v82, v118\n" \
"v_mul_f32 v83, v83, v119\n" \
"v_mul_f32 v84, v84, v124\n" \
"v_mul_f32 v85, v85, v125\n" \
"v_mul_f32 v86, v86, v126\n" \
"v_mul_f32 v87, v87, v127\n" \
"v_cvt_pk_bf16_f32 v68, v80, v81\n" \
"v_cvt_pk_bf16_f32 v69, v82, v83\n" \
"v_cvt_pk_bf16_f32 v70, v84, v85\n" \
"v_cvt_pk_bf16_f32 v71, v86, v87\n" \
"global_store_dwordx4 v105, v[68:71], s[48:49] offset:512 sc1\n" \
"v_mul_f32 v80, s75, v128\n" \
"v_mul_f32 v81, s75, v129\n" \
"v_mul_f32 v82, s75, v130\n" \
"v_mul_f32 v83, s75, v131\n" \
"v_mul_f32 v84, s75, v136\n" \
"v_mul_f32 v85, s75, v137\n" \
"v_mul_f32 v86, s75, v138\n" \
"v_mul_f32 v87, s75, v139\n" \
"v_exp_f32 v80, v80\n" \
"v_exp_f32 v81, v81\n" \
"v_exp_f32 v82, v82\n" \
"v_exp_f32 v83, v83\n" \
"v_exp_f32 v84, v84\n" \
"v_exp_f32 v85, v85\n" \
"v_exp_f32 v86, v86\n" \
"v_exp_f32 v87, v87\n" \
"v_add_f32 v80, 1.0, v80\n" \
"v_add_f32 v81, 1.0, v81\n" \
"v_add_f32 v82, 1.0, v82\n" \
"v_add_f32 v83, 1.0, v83\n" \
"v_add_f32 v84, 1.0, v84\n" \
"v_add_f32 v85, 1.0, v85\n" \
"v_add_f32 v86, 1.0, v86\n" \
"v_add_f32 v87, 1.0, v87\n" \
"v_rcp_f32 v80, v80\n" \
"v_rcp_f32 v81, v81\n" \
"v_rcp_f32 v82, v82\n" \
"v_rcp_f32 v83, v83\n" \
"v_rcp_f32 v84, v84\n" \
"v_rcp_f32 v85, v85\n" \
"v_rcp_f32 v86, v86\n" \
"v_rcp_f32 v87, v87\n" \
"v_mul_f32 v80, v80, v128\n" \
"v_mul_f32 v81, v81, v129\n" \
"v_mul_f32 v82, v82, v130\n" \
"v_mul_f32 v83, v83, v131\n" \
"v_mul_f32 v84, v84, v136\n" \
"v_mul_f32 v85, v85, v137\n" \
"v_mul_f32 v86, v86, v138\n" \
"v_mul_f32 v87, v87, v139\n" \
"v_cvt_pk_bf16_f32 v72, v80, v81\n" \
"v_cvt_pk_bf16_f32 v73, v82, v83\n" \
"v_cvt_pk_bf16_f32 v74, v84, v85\n" \
"v_cvt_pk_bf16_f32 v75, v86, v87\n" \
"global_store_dwordx4 v106, v[72:75], s[48:49] offset:0 sc1\n" \
"v_mul_f32 v80, s75, v132\n" \
"v_mul_f32 v81, s75, v133\n" \
"v_mul_f32 v82, s75, v134\n" \
"v_mul_f32 v83, s75, v135\n" \
"v_mul_f32 v84, s75, v140\n" \
"v_mul_f32 v85, s75, v141\n" \
"v_mul_f32 v86, s75, v142\n" \
"v_mul_f32 v87, s75, v143\n" \
"v_exp_f32 v80, v80\n" \
"v_exp_f32 v81, v81\n" \
"v_exp_f32 v82, v82\n" \
"v_exp_f32 v83, v83\n" \
"v_exp_f32 v84, v84\n" \
"v_exp_f32 v85, v85\n" \
"v_exp_f32 v86, v86\n" \
"v_exp_f32 v87, v87\n" \
"v_add_f32 v80, 1.0, v80\n" \
"v_add_f32 v81, 1.0, v81\n" \
"v_add_f32 v82, 1.0, v82\n" \
"v_add_f32 v83, 1.0, v83\n" \
"v_add_f32 v84, 1.0, v84\n" \
"v_add_f32 v85, 1.0, v85\n" \
"v_add_f32 v86, 1.0, v86\n" \
"v_add_f32 v87, 1.0, v87\n" \
"v_rcp_f32 v80, v80\n" \
"v_rcp_f32 v81, v81\n" \
"v_rcp_f32 v82, v82\n" \
"v_rcp_f32 v83, v83\n" \
"v_rcp_f32 v84, v84\n" \
"v_rcp_f32 v85, v85\n" \
"v_rcp_f32 v86, v86\n" \
"v_rcp_f32 v87, v87\n" \
"v_mul_f32 v80, v80, v132\n" \
"v_mul_f32 v81, v81, v133\n" \
"v_mul_f32 v82, v82, v134\n" \
"v_mul_f32 v83, v83, v135\n" \
"v_mul_f32 v84, v84, v140\n" \
"v_mul_f32 v85, v85, v141\n" \
"v_mul_f32 v86, v86, v142\n" \
"v_mul_f32 v87, v87, v143\n" \
"v_cvt_pk_bf16_f32 v76, v80, v81\n" \
"v_cvt_pk_bf16_f32 v77, v82, v83\n" \
"v_cvt_pk_bf16_f32 v78, v84, v85\n" \
"v_cvt_pk_bf16_f32 v79, v86, v87\n" \
"global_store_dwordx4 v106, v[76:79], s[48:49] offset:512 sc1\n" \
"s_waitcnt vmcnt(0)\n" \
"v_mov_b32 v80, 64\n" \
"s_mov_b64 exec, 1\n" \
"global_store_dword v107, v80, s[52:53] sc1\n" \
"s_mov_b64 exec, -1\n" \
"s_mov_b64 exec, -1\n" \
"s_waitcnt lgkmcnt(0)\n" \
"s_barrier\n" \
"s_branch .Lout%=\n" \
".Lend%=:\n" \
"s_endpgm\n" \
".Lhb_s%=:\n" \
"s_sub_u32 s73, s73, 1\n" \
"s_cbranch_scc1 .Lend%=\n" \
"global_load_dword v152, v150, s[54:55] sc1\n" \
"s_waitcnt vmcnt(0)\n" \
"s_branch .Lhb_c%=\n" \
".Lln_s%=:\n" \
"s_sub_u32 s73, s73, 1\n" \
"s_cbranch_scc1 .Lend%=\n" \
"s_sleep 4\n" \
"global_load_dword v64, v107, s[54:55] sc1\n" \
"s_waitcnt vmcnt(0)\n" \
"s_branch .Lln_c%=\n" \
".Lout%=:\n"

#define F2_ASM \
"s_mov_b32 s73, 2000000\n" \
"v_mov_b32 v108, v100\n" \
"v_add_u32 v109, 0x10000, v100\n" \
"v_add_u32 v110, 0x20000, v100\n" \
"v_mov_b32 v150, s65\n" \
"global_load_dword v151, v150, s[54:55] sc1\n" \
"s_memrealtime s[76:77]\n" \
"s_waitcnt lgkmcnt(0)\n" \
".Ldly%=:\n" \
"s_memrealtime s[78:79]\n" \
"s_waitcnt lgkmcnt(0)\n" \
"s_sub_u32 s78, s78, s76\n" \
"s_cmp_lt_u32 s78, s64\n" \
"s_cbranch_scc1 .Ldly%=\n" \
"s_add_u32 m0, s56, 0\n" \
"s_nop 0\n" \
"global_load_lds_dwordx4 v96, s[40:41] offset:0 nt\n" \
"global_load_lds_dwordx4 v97, s[40:41] offset:1024 nt\n" \
"global_load_lds_dwordx4 v98, s[40:41] offset:2048 nt\n" \
"s_add_u32 s40, s40, 36864\n" \
"s_addc_u32 s41, s41, 0\n" \
"s_add_u32 m0, s56, 12288\n" \
"s_nop 0\n" \
"global_load_lds_dwordx4 v96, s[40:41] offset:0 nt\n" \
"global_load_lds_dwordx4 v97, s[40:41] offset:1024 nt\n" \
"global_load_lds_dwordx4 v98, s[40:41] offset:2048 nt\n" \
"s_add_u32 s40, s40, 36864\n" \
"s_addc_u32 s41, s41, 0\n" \
"s_add_u32 m0, s56, 24576\n" \
"s_nop 0\n" \
"global_load_lds_dwordx4 v96, s[40:41] offset:0 nt\n" \
"global_load_lds_dwordx4 v97, s[40:41] offset:1024 nt\n" \
"global_load_lds_dwordx4 v98, s[40:41] offset:2048 nt\n" \
"s_add_u32 s40, s40, 36864\n" \
"s_addc_u32 s41, s41, 0\n" \
"s_add_u32 m0, s56, 36864\n" \
"s_nop 0\n" \
"global_load_lds_dwordx4 v96, s[40:41] offset:0 nt\n" \
"global_load_lds_dwordx4 v97, s[40:41] offset:1024 nt\n" \
"global_load_lds_dwordx4 v98, s[40:41] offset:2048 nt\n" \
"s_add_u32 s40, s40, 36864\n" \
"s_addc_u32 s41, s41, 0\n" \
"s_add_u32 m0, s56, 49152\n" \
"s_nop 0\n" \
"global_load_lds_dwordx4 v96, s[40:41] offset:0 nt\n" \
"global_load_lds_dwordx4 v97, s[40:41] offset:1024 nt\n" \
"global_load_lds_dwordx4 v98, s[40:41] offset:2048 nt\n" \
"s_add_u32 s40, s40, 36864\n" \
"s_addc_u32 s41, s41, 0\n" \
"s_add_u32 m0, s56, 61440\n" \
"s_nop 0\n" \
"global_load_lds_dwordx4 v96, s[40:41] offset:0 nt\n" \
"global_load_lds_dwordx4 v97, s[40:41] offset:1024 nt\n" \
"global_load_lds_dwordx4 v98, s[40:41] offset:2048 nt\n" \
"s_add_u32 s40, s40, 36864\n" \
"s_addc_u32 s41, s41, 0\n" \
"s_add_u32 m0, s56, 73728\n" \
"s_nop 0\n" \
"global_load_lds_dwordx4 v96, s[40:41] offset:0 nt\n" \
"global_load_lds_dwordx4 v97, s[40:41] offset:1024 nt\n" \
"global_load_lds_dwordx4 v98, s[40:41] offset:2048 nt\n" \
"s_add_u32 s40, s40, 36864\n" \
"s_addc_u32 s41, s41, 0\n" \
"s_add_u32 m0, s56, 86016\n" \
"s_nop 0\n" \
"global_load_lds_dwordx4 v96, s[40:41] offset:0 nt\n" \
"global_load_lds_dwordx4 v97, s[40:41] offset:1024 nt\n" \
"global_load_lds_dwordx4 v98, s[40:41] offset:2048 nt\n" \
"s_add_u32 s40, s40, 36864\n" \
"s_addc_u32 s41, s41, 0\n" \
"s_add_u32 m0, s56, 98304\n" \
"s_nop 0\n" \
"global_load_lds_dwordx4 v96, s[40:41] offset:0 nt\n" \
"global_load_lds_dwordx4 v97, s[40:41] offset:1024 nt\n" \
"global_load_lds_dwordx4 v98, s[40:41] offset:2048 nt\n" \
"s_add_u32 s40, s40, 36864\n" \
"s_addc_u32 s41, s41, 0\n" \
"s_add_u32 m0, s56, 110592\n" \
"s_nop 0\n" \
"global_load_lds_dwordx4 v96, s[40:41] offset:0 nt\n" \
"global_load_lds_dwordx4 v97, s[40:41] offset:1024 nt\n" \
"global_load_lds_dwordx4 v98, s[40:41] offset:2048 nt\n" \
"s_add_u32 s40, s40, 36864\n" \
"s_addc_u32 s41, s41, 0\n" \
"s_add_u32 m0, s56, 122880\n" \
"s_nop 0\n" \
"global_load_lds_dwordx4 v96, s[40:41] offset:0 nt\n" \
"global_load_lds_dwordx4 v97, s[40:41] offset:1024 nt\n" \
"global_load_lds_dwordx4 v98, s[40:41] offset:2048 nt\n" \
"s_add_u32 s40, s40, 36864\n" \
"s_addc_u32 s41, s41, 0\n" \
"s_add_u32 m0, s56, 135168\n" \
"s_nop 0\n" \
"global_load_lds_dwordx4 v96, s[40:41] offset:0 nt\n" \
"global_load_lds_dwordx4 v97, s[40:41] offset:1024 nt\n" \
"global_load_lds_dwordx4 v98, s[40:41] offset:2048 nt\n" \
"s_add_u32 s40, s40, 36864\n" \
"s_addc_u32 s41, s41, 0\n" \
"s_waitcnt vmcnt(33)\n" \
"s_barrier\n" \
"ds_read_b64_tr_b16 a[0:1], v108 offset:0\n" \
"ds_read_b64_tr_b16 a[2:3], v108 offset:6144\n" \
"ds_read_b64_tr_b16 a[4:5], v108 offset:32\n" \
"ds_read_b64_tr_b16 a[6:7], v108 offset:6176\n" \
"ds_read_b64_tr_b16 a[8:9], v108 offset:64\n" \
"ds_read_b64_tr_b16 a[10:11], v108 offset:6208\n" \
"s_waitcnt lgkmcnt(0)\n" \
"s_waitcnt vmcnt(30)\n" \
"s_barrier\n" \
"s_add_u32 m0, s56, 0\n" \
"s_nop 0\n" \
"global_load_lds_dwordx4 v96, s[40:41] offset:0 nt\n" \
"global_load_lds_dwordx4 v97, s[40:41] offset:1024 nt\n" \
"global_load_lds_dwordx4 v98, s[40:41] offset:2048 nt\n" \
"s_add_u32 s40, s40, 36864\n" \
"s_addc_u32 s41, s41, 0\n" \
"ds_read_b64_tr_b16 a[12:13], v108 offset:12288\n" \
"ds_read_b64_tr_b16 a[14:15], v108 offset:18432\n" \
"ds_read_b64_tr_b16 a[16:17], v108 offset:12320\n" \
"ds_read_b64_tr_b16 a[18:19], v108 offset:18464\n" \
"ds_read_b64_tr_b16 a[20:21], v108 offset:12352\n" \
"ds_read_b64_tr_b16 a[22:23], v108 offset:18496\n" \
"s_waitcnt lgkmcnt(0)\n" \
"s_waitcnt vmcnt(30)\n" \
"s_barrier\n" \
"s_add_u32 m0, s56, 12288\n" \
"s_nop 0\n" \
"global_load_lds_dwordx4 v96, s[40:41] offset:0 nt\n" \
"global_load_lds_dwordx4 v97, s[40:41] offset:1024 nt\n" \
"global_load_lds_dwordx4 v98, s[40:41] offset:2048 nt\n" \
"s_add_u32 s40, s40, 36864\n" \
"s_addc_u32 s41, s41, 0\n" \
"ds_read_b64_tr_b16 a[24:25], v108 offset:24576\n" \
"ds_read_b64_tr_b16 a[26:27], v108 offset:30720\n" \
"ds_read_b64_tr_b16 a[28:29], v108 offset:24608\n" \
"ds_read_b64_tr_b16 a[30:31], v108 offset:30752\n" \
"ds_read_b64_tr_b16 a[32:33], v108 offset:24640\n" \
"ds_read_b64_tr_b16 a[34:35], v108 offset:30784\n" \
"s_waitcnt lgkmcnt(0)\n" \
"s_waitcnt vmcnt(30)\n" \
"s_barrier\n" \
"s_add_u32 m0, s56, 24576\n" \
"s_nop 0\n" \
"global_load_lds_dwordx4 v96, s[40:41] offset:0 nt\n" \
"global_load_lds_dwordx4 v97, s[40:41] offset:1024 nt\n" \
"global_load_lds_dwordx4 v98, s[40:41] offset:2048 nt\n" \
"s_add_u32 s40, s40, 36864\n" \
"s_addc_u32 s41, s41, 0\n" \
"ds_read_b64_tr_b16 a[36:37], v108 offset:36864\n" \
"ds_read_b64_tr_b16 a[38:39], v108 offset:43008\n" \
"ds_read_b64_tr_b16 a[40:41], v108 offset:36896\n" \
"ds_read_b64_tr_b16 a[42:43], v108 offset:43040\n" \
"ds_read_b64_tr_b16 a[44:45], v108 offset:36928\n" \
"ds_read_b64_tr_b16 a[46:47], v108 offset:43072\n" \
"s_waitcnt lgkmcnt(0)\n" \
"s_waitcnt vmcnt(30)\n" \
"s_barrier\n" \
"s_add_u32 m0, s56, 36864\n" \
"s_nop 0\n" \
"global_load_lds_dwordx4 v96, s[40:41] offset:0 nt\n" \
"global_load_lds_dwordx4 v97, s[40:41] offset:1024 nt\n" \
"global_load_lds_dwordx4 v98, s[40:41] offset:2048 nt\n" \
"s_add_u32 s40, s40, 36864\n" \
"s_addc_u32 s41, s41, 0\n" \
"ds_read_b64_tr_b16 a[48:49], v108 offset:49152\n" \
"ds_read_b64_tr_b16 a[50:51], v108 offset:55296\n" \
"ds_read_b64_tr_b16 a[52:53], v108 offset:49184\n" \
"ds_read_b64_tr_b16 a[54:55], v108 offset:55328\n" \
"ds_read_b64_tr_b16 a[56:57], v108 offset:49216\n" \
"ds_read_b64_tr_b16 a[58:59], v108 offset:55360\n" \
"s_waitcnt lgkmcnt(0)\n" \
"s_waitcnt vmcnt(30)\n" \
"s_barrier\n" \
"s_add_u32 m0, s56, 49152\n" \
"s_nop 0\n" \
"global_load_lds_dwordx4 v96, s[40:41] offset:0 nt\n" \
"global_load_lds_dwordx4 v97, s[40:41] offset:1024 nt\n" \
"global_load_lds_dwordx4 v98, s[40:41] offset:2048 nt\n" \
"s_add_u32 s40, s40, 36864\n" \
"s_addc_u32 s41, s41, 0\n" \
"ds_read_b64_tr_b16 a[60:61], v108 offset:61440\n" \
"ds_read_b64_tr_b16 a[62:63], v109 offset:2048\n" \
"ds_read_b64_tr_b16 a[64:65], v108 offset:61472\n" \
"ds_read_b64_tr_b16 a[66:67], v109 offset:2080\n" \
"ds_read_b64_tr_b16 a[68:69], v108 offset:61504\n" \
"ds_read_b64_tr_b16 a[70:71], v109 offset:2112\n" \
"s_waitcnt lgkmcnt(0)\n" \
"s_waitcnt vmcnt(30)\n" \
"s_barrier\n" \
"s_add_u32 m0, s56, 61440\n" \
"s_nop 0\n" \
"global_load_lds_dwordx4 v96, s[40:41] offset:0 nt\n" \
"global_load_lds_dwordx4 v97, s[40:41] offset:1024 nt\n" \
"global_load_lds_dwordx4 v98, s[40:41] offset:2048 nt\n" \
"s_add_u32 s40, s40, 36864\n" \
"s_addc_u32 s41, s41, 0\n" \
"ds_read_b64_tr_b16 a[72:73], v109 offset:8192\n" \
"ds_read_b64_tr_b16 a[74:75], v109 offset:14336\n" \
"ds_read_b64_tr_b16 a[76:77], v109 offset:8224\n" \
"ds_read_b64_tr_b16 a[78:79], v109 offset:14368\n" \
"ds_read_b64_tr_b16 a[80:81], v109 offset:8256\n" \
"ds_read_b64_tr_b16 a[82:83], v109 offset:14400\n" \
"s_waitcnt lgkmcnt(0)\n" \
"s_waitcnt vmcnt(30)\n" \
"s_barrier\n" \
"s_add_u32 m0, s56, 73728\n" \
"s_nop 0\n" \
"global_load_lds_dwordx4 v96, s[40:41] offset:0 nt\n" \
"global_load_lds_dwordx4 v97, s[40:41] offset:1024 nt\n" \
"global_load_lds_dwordx4 v98, s[40:41] offset:2048 nt\n" \
"s_add_u32 s40, s40, 36864\n" \
"s_addc_u32 s41, s41, 0\n" \
"ds_read_b64_tr_b16 a[84:85], v109 offset:20480\n" \
"ds_read_b64_tr_b16 a[86:87], v109 offset:26624\n" \
"ds_read_b64_tr_b16 a[88:89], v109 offset:20512\n" \
"ds_read_b64_tr_b16 a[90:91], v109 offset:26656\n" \
"ds_read_b64_tr_b16 a[92:93], v109 offset:20544\n" \
"ds_read_b64_tr_b16 a[94:95], v109 offset:26688\n" \
"s_waitcnt lgkmcnt(0)\n" \
"s_waitcnt vmcnt(30)\n" \
"s_barrier\n" \
"s_add_u32 m0, s56, 86016\n" \
"s_nop 0\n" \
"global_load_lds_dwordx4 v96, s[40:41] offset:0 nt\n" \
"global_load_lds_dwordx4 v97, s[40:41] offset:1024 nt\n" \
"global_load_lds_dwordx4 v98, s[40:41] offset:2048 nt\n" \
"s_add_u32 s40, s40, 36864\n" \
"s_addc_u32 s41, s41, 0\n" \
"ds_read_b64_tr_b16 a[96:97], v109 offset:32768\n" \
"ds_read_b64_tr_b16 a[98:99], v109 offset:38912\n" \
"ds_read_b64_tr_b16 a[100:101], v109 offset:32800\n" \
"ds_read_b64_tr_b16 a[102:103], v109 offset:38944\n" \
"ds_read_b64_tr_b16 a[104:105], v109 offset:32832\n" \
"ds_read_b64_tr_b16 a[106:107], v109 offset:38976\n" \
"s_waitcnt lgkmcnt(0)\n" \
"s_waitcnt vmcnt(30)\n" \
"s_barrier\n" \
"s_add_u32 m0, s56, 98304\n" \
"s_nop 0\n" \
"global_load_lds_dwordx4 v96, s[40:41] offset:0 nt\n" \
"global_load_lds_dwordx4 v97, s[40:41] offset:1024 nt\n" \
"global_load_lds_dwordx4 v98, s[40:41] offset:2048 nt\n" \
"s_add_u32 s40, s40, 36864\n" \
"s_addc_u32 s41, s41, 0\n" \
"ds_read_b64_tr_b16 a[108:109], v109 offset:45056\n" \
"ds_read_b64_tr_b16 a[110:111], v109 offset:51200\n" \
"ds_read_b64_tr_b16 a[112:113], v109 offset:45088\n" \
"ds_read_b64_tr_b16 a[114:115], v109 offset:51232\n" \
"ds_read_b64_tr_b16 a[116:117], v109 offset:45120\n" \
"ds_read_b64_tr_b16 a[118:119], v109 offset:51264\n" \
"s_waitcnt lgkmcnt(0)\n" \
"s_waitcnt vmcnt(30)\n" \
"s_barrier\n" \
"s_add_u32 m0, s56, 110592\n" \
"s_nop 0\n" \
"global_load_lds_dwordx4 v96, s[40:41] offset:0 nt\n" \
"global_load_lds_dwordx4 v97, s[40:41] offset:1024 nt\n" \
"global_load_lds_dwordx4 v98, s[40:41] offset:2048 nt\n" \
"s_add_u32 s40, s40, 36864\n" \
"s_addc_u32 s41, s41, 0\n" \
"ds_read_b64_tr_b16 a[120:121], v109 offset:57344\n" \
"ds_read_b64_tr_b16 a[122:123], v109 offset:63488\n" \
"ds_read_b64_tr_b16 a[124:125], v109 offset:57376\n" \
"ds_read_b64_tr_b16 a[126:127], v109 offset:63520\n" \
"ds_read_b64_tr_b16 a[128:129], v109 offset:57408\n" \
"ds_read_b64_tr_b16 a[130:131], v109 offset:63552\n" \
"s_waitcnt lgkmcnt(0)\n" \
"s_waitcnt vmcnt(30)\n" \
"s_barrier\n" \
"global_load_dword v152, v150, s[54:55] sc1\n" \
"s_add_u32 m0, s56, 122880\n" \
"s_nop 0\n" \
"global_load_lds_dwordx4 v96, s[40:41] offset:0 nt\n" \
"global_load_lds_dwordx4 v97, s[40:41] offset:1024 nt\n" \
"global_load_lds_dwordx4 v98, s[40:41] offset:2048 nt\n" \
"s_add_u32 s40, s40, 36864\n" \
"s_addc_u32 s41, s41, 0\n" \
"ds_read_b64_tr_b16 a[132:133], v110 offset:4096\n" \
"ds_read_b64_tr_b16 a[134:135], v110 offset:10240\n" \
"ds_read_b64_tr_b16 a[136:137], v110 offset:4128\n" \
"ds_read_b64_tr_b16 a[138:139], v110 offset:10272\n" \
"ds_read_b64_tr_b16 a[140:141], v110 offset:4160\n" \
"ds_read_b64_tr_b16 a[142:143], v110 offset:10304\n" \
"s_waitcnt lgkmcnt(0)\n" \
"s_waitcnt vmcnt(31)\n" \
"s_barrier\n" \
"s_add_u32 m0, s56, 135168\n" \
"s_nop 0\n" \
"global_load_lds_dwordx4 v96, s[40:41] offset:0 nt\n" \
"global_load_lds_dwordx4 v97, s[40:41] offset:1024 nt\n" \
"global_load_lds_dwordx4 v98, s[40:41] offset:2048 nt\n" \
"s_add_u32 s40, s40, 36864\n" \
"s_addc_u32 s41, s41, 0\n" \
"ds_read_b64_tr_b16 a[144:145], v108 offset:0\n" \
"ds_read_b64_tr_b16 a[146:147], v108 offset:6144\n" \
"ds_read_b64_tr_b16 a[148:149], v108 offset:32\n" \
"ds_read_b64_tr_b16 a[150:151], v108 offset:6176\n" \
"ds_read_b64_tr_b16 a[152:153], v108 offset:64\n" \
"ds_read_b64_tr_b16 a[154:155], v108 offset:6208\n" \
"s_waitcnt lgkmcnt(0)\n" \
"s_waitcnt vmcnt(31)\n" \
"s_barrier\n" \
"ds_read_b64_tr_b16 a[156:157], v108 offset:12288\n" \
"ds_read_b64_tr_b16 a[158:159], v108 offset:18432\n" \
"ds_read_b64_tr_b16 a[160:161], v108 offset:12320\n" \
"ds_read_b64_tr_b16 a[162:163], v108 offset:18464\n" \
"ds_read_b64_tr_b16 a[164:165], v108 offset:12352\n" \
"ds_read_b64_tr_b16 a[166:167], v108 offset:18496\n" \
"s_waitcnt lgkmcnt(0)\n" \
"s_waitcnt vmcnt(28)\n" \
"s_barrier\n" \
"ds_read_b64_tr_b16 a[168:169], v108 offset:24576\n" \
"ds_read_b64_tr_b16 a[170:171], v108 offset:30720\n" \
"ds_read_b64_tr_b16 a[172:173], v108 offset:24608\n" \
"ds_read_b64_tr_b16 a[174:175], v108 offset:30752\n" \
"ds_read_b64_tr_b16 a[176:177], v108 offset:24640\n" \
"ds_read_b64_tr_b16 a[178:179], v108 offset:30784\n" \
"s_waitcnt lgkmcnt(0)\n" \
"s_waitcnt vmcnt(25)\n" \
"s_barrier\n" \
"ds_read_b64_tr_b16 a[180:181], v108 offset:36864\n" \
"ds_read_b64_tr_b16 a[182:183], v108 offset:43008\n" \
"ds_read_b64_tr_b16 a[184:185], v108 offset:36896\n" \
"ds_read_b64_tr_b16 a[186:187], v108 offset:43040\n" \
"ds_read_b64_tr_b16 a[188:189], v108 offset:36928\n" \
"ds_read_b64_tr_b16 a[190:191], v108 offset:43072\n" \
"s_waitcnt lgkmcnt(0)\n" \
"s_waitcnt vmcnt(22)\n" \
"s_barrier\n" \
"ds_read_b64_tr_b16 a[192:193], v108 offset:49152\n" \
"ds_read_b64_tr_b16 a[194:195], v108 offset:55296\n" \
"ds_read_b64_tr_b16 a[196:197], v108 offset:49184\n" \
"ds_read_b64_tr_b16 a[198:199], v108 offset:55328\n" \
"ds_read_b64_tr_b16 a[200:201], v108 offset:49216\n" \
"ds_read_b64_tr_b16 a[202:203], v108 offset:55360\n" \
"s_waitcnt lgkmcnt(0)\n" \
"s_waitcnt vmcnt(19)\n" \
"s_barrier\n" \
"ds_read_b64_tr_b16 a[204:205], v108 offset:61440\n" \
"ds_read_b64_tr_b16 a[206:207], v109 offset:2048\n" \
"ds_read_b64_tr_b16 a[208:209], v108 offset:61472\n" \
"ds_read_b64_tr_b16 a[210:211], v109 offset:2080\n" \
"ds_read_b64_tr_b16 a[212:213], v108 offset:61504\n" \
"ds_read_b64_tr_b16 a[214:215], v109 offset:2112\n" \
"s_waitcnt lgkmcnt(0)\n" \
"s_waitcnt vmcnt(16)\n" \
"s_barrier\n" \
"ds_read_b64_tr_b16 a[216:217], v109 offset:8192\n" \
"ds_read_b64_tr_b16 a[218:219], v109 offset:14336\n" \
"ds_read_b64_tr_b16 a[220:221], v109 offset:8224\n" \
"ds_read_b64_tr_b16 a[222:223], v109 offset:14368\n" \
"ds_read_b64_tr_b16 a[224:225], v109 offset:8256\n" \
"ds_read_b64_tr_b16 a[226:227], v109 offset:14400\n" \
"s_waitcnt lgkmcnt(0)\n" \
"s_waitcnt vmcnt(13)\n" \
"s_barrier\n" \
"ds_read_b64_tr_b16 a[228:229], v109 offset:20480\n" \
"ds_read_b64_tr_b16 a[230:231], v109 offset:26624\n" \
"ds_read_b64_tr_b16 a[232:233], v109 offset:20512\n" \
"ds_read_b64_tr_b16 a[234:235], v109 offset:26656\n" \
"ds_read_b64_tr_b16 a[236:237], v109 offset:20544\n" \
"ds_read_b64_tr_b16 a[238:239], v109 offset:26688\n" \
"s_waitcnt lgkmcnt(0)\n" \
"s_waitcnt vmcnt(10)\n" \
"s_barrier\n" \
"ds_read_b64_tr_b16 a[240:241], v109 offset:32768\n" \
"ds_read_b64_tr_b16 a[242:243], v109 offset:38912\n" \
"ds_read_b64_tr_b16 a[244:245], v109 offset:32800\n" \
"ds_read_b64_tr_b16 a[246:247], v109 offset:38944\n" \
"ds_read_b64_tr_b16 a[248:249], v109 offset:32832\n" \
"ds_read_b64_tr_b16 a[250:251], v109 offset:38976\n" \
"s_waitcnt lgkmcnt(0)\n" \
"s_waitcnt vmcnt(7)\n" \
"s_barrier\n" \
"ds_read_b64_tr_b16 a[252:253], v109 offset:45056\n" \
"ds_read_b64_tr_b16 a[254:255], v109 offset:51200\n" \
"ds_read_b64_tr_b16 v[224:225], v109 offset:45088\n" \
"ds_read_b64_tr_b16 v[226:227], v109 offset:51232\n" \
"ds_read_b64_tr_b16 v[228:229], v109 offset:45120\n" \
"ds_read_b64_tr_b16 v[230:231], v109 offset:51264\n" \
"s_waitcnt lgkmcnt(0)\n" \
"s_waitcnt vmcnt(3)\n" \
"s_barrier\n" \
".Lhb_c%=:\n" \
"v_cmp_eq_u32 vcc, v151, v152\n" \
"s_nop 4\n" \
"s_cbranch_vccnz .Lhb_s%=\n" \
"ds_read_b64_tr_b16 v[232:233], v109 offset:57344\n" \
"ds_read_b64_tr_b16 v[234:235], v109 offset:63488\n" \
"ds_read_b64_tr_b16 v[236:237], v109 offset:57376\n" \
"ds_read_b64_tr_b16 v[238:239], v109 offset:63520\n" \
"ds_read_b64_tr_b16 v[240:241], v109 offset:57408\n" \
"ds_read_b64_tr_b16 v[242:243], v109 offset:63552\n" \
"s_waitcnt lgkmcnt(0)\n" \
"s_waitcnt vmcnt(0)\n" \
"s_barrier\n" \
"ds_read_b64_tr_b16 v[244:245], v110 offset:4096\n" \
"ds_read_b64_tr_b16 v[246:247], v110 offset:10240\n" \
"ds_read_b64_tr_b16 v[248:249], v110 offset:4128\n" \
"ds_read_b64_tr_b16 v[250:251], v110 offset:10272\n" \
"ds_read_b64_tr_b16 v[252:253], v110 offset:4160\n" \
"ds_read_b64_tr_b16 v[254:255], v110 offset:10304\n" \
"s_waitcnt lgkmcnt(0)\n" \
"s_barrier\n" \
"s_mov_b32 s60, s57\n" \
"s_add_u32 s61, s57, 147456\n" \
"s_mov_b32 s62, 0\n" \
"s_mov_b32 s58, 1\n" \
"s_mov_b32 s51, 32\n" \
"s_mov_b32 s64, 3\n" \
"global_load_dword v99, v105, s[54:55] sc1\n" \
"s_waitcnt vmcnt(0)\n" \
".Lh0_c%=:\n" \
"v_cmp_gt_u32 vcc, 1, v99\n" \
"s_cbranch_vccnz .Lh0_s%=\n" \
"s_add_u32 m0, s60, 0\n" \
"s_nop 0\n" \
"global_load_lds_dwordx4 v102, s[46:47] offset:0 sc1\n" \
"global_load_lds_dwordx4 v102, s[46:47] offset:1024 sc1\n" \
"global_load_lds_dwordx4 v102, s[46:47] offset:2048 sc1\n" \
"global_load_lds_dwordx4 v102, s[46:47] offset:3072 sc1\n" \
"s_add_u32 m0, s60, 4096\n" \
"s_nop 0\n" \
"global_load_lds_dwordx4 v103, s[46:47] offset:0 sc1\n" \
"global_load_lds_dwordx4 v103, s[46:47] offset:1024 sc1\n" \
"global_load_lds_dwordx4 v103, s[46:47] offset:2048 sc1\n" \
"global_load_lds_dwordx4 v103, s[46:47] offset:3072 sc1\n" \
"s_add_u32 m0, s60, 8192\n" \
"s_nop 0\n" \
"global_load_lds_dwordx4 v104, s[46:47] offset:0 sc1\n" \
"global_load_lds_dwordx4 v104, s[46:47] offset:1024 sc1\n" \
"global_load_lds_dwordx4 v104, s[46:47] offset:2048 sc1\n" \
"global_load_lds_dwordx4 v104, s[46:47] offset:3072 sc1\n" \
"s_add_u32 s46, s46, 294912\n" \
"s_addc_u32 s47, s47, 0\n" \
"s_add_u32 s60, s60, 49152\n" \
"s_cmp_eq_u32 s60, s61\n" \
"s_cselect_b32 s60, s57, s60\n" \
"global_load_dword v99, v105, s[54:55] sc1\n" \
"s_waitcnt vmcnt(0)\n" \
".Lh1_c%=:\n" \
"v_cmp_gt_u32 vcc, 2, v99\n" \
"s_cbranch_vccnz .Lh1_s%=\n" \
"s_add_u32 m0, s60, 0\n" \
"s_nop 0\n" \
"global_load_lds_dwordx4 v102, s[46:47] offset:0 sc1\n" \
"global_load_lds_dwordx4 v102, s[46:47] offset:1024 sc1\n" \
"global_load_lds_dwordx4 v102, s[46:47] offset:2048 sc1\n" \
"global_load_lds_dwordx4 v102, s[46:47] offset:3072 sc1\n" \
"s_add_u32 m0, s60, 4096\n" \
"s_nop 0\n" \
"global_load_lds_dwordx4 v103, s[46:47] offset:0 sc1\n" \
"global_load_lds_dwordx4 v103, s[46:47] offset:1024 sc1\n" \
"global_load_lds_dwordx4 v103, s[46:47] offset:2048 sc1\n" \
"global_load_lds_dwordx4 v103, s[46:47] offset:3072 sc1\n" \
"s_add_u32 m0, s60, 8192\n" \
"s_nop 0\n" \
"global_load_lds_dwordx4 v104, s[46:47] offset:0 sc1\n" \
"global_load_lds_dwordx4 v104, s[46:47] offset:1024 sc1\n" \
"global_load_lds_dwordx4 v104, s[46:47] offset:2048 sc1\n" \
"global_load_lds_dwordx4 v104, s[46:47] offset:3072 sc1\n" \
"s_add_u32 s46, s46, 294912\n" \
"s_addc_u32 s47, s47, 0\n" \
"s_add_u32 s60, s60, 49152\n" \
"s_cmp_eq_u32 s60, s61\n" \
"s_cselect_b32 s60, s57, s60\n" \
"global_load_dword v99, v105, s[54:55] sc1\n" \
"v_mov_b32 v111, v101\n" \
"s_waitcnt vmcnt(0)\n" \
"s_barrier\n" \
"ds_read_b128 v[32:35], v111 offset:0\n" \
"ds_read_b128 v[36:39], v111 offset:1024\n" \
"ds_read_b128 v[40:43], v111 offset:2048\n" \
"ds_read_b128 v[44:47], v111 offset:3072\n" \
"ds_read_b128 v[48:51], v111 offset:4096\n" \
"ds_read_b128 v[52:55], v111 offset:5120\n" \
"ds_read_b128 v[56:59], v111 offset:6144\n" \
"ds_read_b128 v[60:63], v111 offset:7168\n" \
"s_waitcnt lgkmcnt(7)\n" \
"v_mfma_f32_16x16x32_bf16 v[0:3], a[0:3], v[32:35], 0\n" \
"s_barrier\n" \
"v_mfma_f32_16x16x32_bf16 v[8:11], a[4:7], v[32:35], 0\n" \
"v_cmp_gt_u32 vcc, s64, v99\n" \
"v_mfma_f32_16x16x32_bf16 v[16:19], a[8:11], v[32:35], 0\n" \
"s_waitcnt lgkmcnt(6)\n" \
"ds_read_b128 v[32:35], v111 offset:8192\n" \
"v_mfma_f32_16x16x32_bf16 v[4:7], a[0:3], v[36:39], 0\n" \
"s_cbranch_vccnz .Lhc1_s%=\n" \
".Lhc1_c%=:\n" \
"v_mfma_f32_16x16x32_bf16 v[12:15], a[4:7], v[36:39], 0\n" \
"global_load_dword v99, v105, s[54:55] sc1\n" \
"v_mfma_f32_16x16x32_bf16 v[20:23], a[8:11], v[36:39], 0\n" \
"s_waitcnt lgkmcnt(6)\n" \
"ds_read_b128 v[36:39], v111 offset:9216\n" \
"v_mfma_f32_16x16x32_bf16 v[0:3], a[12:15], v[40:43], v[0:3]\n" \
"s_add_u32 s64, s64, 1\n" \
"s_min_u32 s64, s64, 64\n" \
"v_mfma_f32_16x16x32_bf16 v[8:11], a[16:19], v[40:43], v[8:11]\n" \
"v_mfma_f32_16x16x32_bf16 v[16:19], a[20:23], v[40:43], v[16:19]\n" \
"s_waitcnt lgkmcnt(6)\n" \
"ds_read_b128 v[40:43], v111 offset:10240\n" \
"v_mfma_f32_16x16x32_bf16 v[4:7], a[12:15], v[44:47], v[4:7]\n" \
"v_mfma_f32_16x16x32_bf16 v[12:15], a[16:19], v[44:47], v[12:15]\n" \
"v_mfma_f32_16x16x32_bf16 v[20:23], a[20:23], v[44:47], v[20:23]\n" \
"s_waitcnt lgkmcnt(6)\n" \
"ds_read_b128 v[44:47], v111 offset:11264\n" \
"v_mfma_f32_16x16x32_bf16 v[0:3], a[24:27], v[48:51], v[0:3]\n" \
"v_mfma_f32_16x16x32_bf16 v[8:11], a[28:31], v[48:51], v[8:11]\n" \
"v_mfma_f32_16x16x32_bf16 v[16:19], a[32:35], v[48:51], v[16:19]\n" \
"s_waitcnt lgkmcnt(6)\n" \
"ds_read_b128 v[48:51], v111 offset:12288\n" \
"v_mfma_f32_16x16x32_bf16 v[4:7], a[24:27], v[52:55], v[4:7]\n" \
"v_mfma_f32_16x16x32_bf16 v[12:15], a[28:31], v[52:55], v[12:15]\n" \
"v_mfma_f32_16x16x32_bf16 v[20:23], a[32:35], v[52:55], v[20:23]\n" \
"s_waitcnt lgkmcnt(6)\n" \
"ds_read_b128 v[52:55], v111 offset:13312\n" \
"v_mfma_f32_16x16x32_bf16 v[0:3], a[36:39], v[56:59], v[0:3]\n" \
"v_mfma_f32_16x16x32_bf16 v[8:11], a[40:43], v[56:59], v[8:11]\n" \
"v_mfma_f32_16x16x32_bf16 v[16:19], a[44:47], v[56:59], v[16:19]\n" \
"s_waitcnt lgkmcnt(6)\n" \
"ds_read_b128 v[56:59], v111 offset:14336\n" \
"v_mfma_f32_16x16x32_bf16 v[4:7], a[36:39], v[60:63], v[4:7]\n" \
"s_add_u32 m0, s60, 0\n" \
"v_mfma_f32_16x16x32_bf16 v[12:15], a[40:43], v[60:63], v[12:15]\n" \
"global_load_lds_dwordx4 v102, s[46:47] offset:0 sc1\n" \
"v_mfma_f32_16x16x32_bf16 v[20:23], a[44:47], v[60:63], v[20:23]\n" \
"s_waitcnt lgkmcnt(6)\n" \
"ds_read_b128 v[60:63], v111 offset:15360\n" \
"v_mfma_f32_16x16x32_bf16 v[0:3], a[48:51], v[32:35], v[0:3]\n" \
"v_mfma_f32_16x16x32_bf16 v[8:11], a[52:55], v[32:35], v[8:11]\n" \
"v_mfma_f32_16x16x32_bf16 v[16:19], a[56:59], v[32:35], v[16:19]\n" \
"s_waitcnt lgkmcnt(6)\n" \
"ds_read_b128 v[32:35], v111 offset:16384\n" \
"v_mfma_f32_16x16x32_bf16 v[4:7], a[48:51], v[36:39], v[4:7]\n" \
"v_mfma_f32_16x16x32_bf16 v[12:15], a[52:55], v[36:39], v[12:15]\n" \
"v_mfma_f32_16x16x32_bf16 v[20:23], a[56:59], v[36:39], v[20:23]\n" \
"s_waitcnt lgkmcnt(6)\n" \
"ds_read_b128 v[36:39], v111 offset:17408\n" \
"v_mfma_f32_16x16x32_bf16 v[0:3], a[60:63], v[40:43], v[0:3]\n" \
"v_mfma_f32_16x16x32_bf16 v[8:11], a[64:67], v[40:43], v[8:11]\n" \
"global_load_lds_dwordx4 v102, s[46:47] offset:1024 sc1\n" \
"v_mfma_f32_16x16x32_bf16 v[16:19], a[68:71], v[40:43], v[16:19]\n" \
"s_waitcnt lgkmcnt(6)\n" \
"ds_read_b128 v[40:43], v111 offset:18432\n" \
"v_mfma_f32_16x16x32_bf16 v[4:7], a[60:63], v[44:47], v[4:7]\n" \
"v_mfma_f32_16x16x32_bf16 v[12:15], a[64:67], v[44:47], v[12:15]\n" \
"v_mfma_f32_16x16x32_bf16 v[20:23], a[68:71], v[44:47], v[20:23]\n" \
"s_waitcnt lgkmcnt(6)\n" \
"ds_read_b128 v[44:47], v111 offset:19456\n" \
"v_mfma_f32_16x16x32_bf16 v[0:3], a[72:75], v[48:51], v[0:3]\n" \
"v_mfma_f32_16x16x32_bf16 v[8:11], a[76:79], v[48:51], v[8:11]\n" \
"v_mfma_f32_16x16x32_bf16 v[16:19], a[80:83], v[48:51], v[16:19]\n" \
"s_waitcnt lgkmcnt(6)\n" \
"ds_read_b128 v[48:51], v111 offset:20480\n" \
"v_mfma_f32_16x16x32_bf16 v[4:7], a[72:75], v[52:55], v[4:7]\n" \
"v_mfma_f32_16x16x32_bf16 v[12:15], a[76:79], v[52:55], v[12:15]\n" \
"global_load_lds_dwordx4 v102, s[46:47] offset:2048 sc1\n" \
"v_mfma_f32_16x16x32_bf16 v[20:23], a[80:83], v[52:55], v[20:23]\n" \
"s_waitcnt lgkmcnt(6)\n" \
"ds_read_b128 v[52:55], v111 offset:21504\n" \
"v_mfma_f32_16x16x32_bf16 v[0:3], a[84:87], v[56:59], v[0:3]\n" \
"v_mfma_f32_16x16x32_bf16 v[8:11], a[88:91], v[56:59], v[8:11]\n" \
"v_mfma_f32_16x16x32_bf16 v[16:19], a[92:95], v[56:59], v[16:19]\n" \
"s_waitcnt lgkmcnt(6)\n" \
"ds_read_b128 v[56:59], v111 offset:22528\n" \
"v_mfma_f32_16x16x32_bf16 v[4:7], a[84:87], v[60:63], v[4:7]\n" \
"v_mfma_f32_16x16x32_bf16 v[12:15], a[88:91], v[60:63], v[12:15]\n" \
"v_mfma_f32_16x16x32_bf16 v[20:23], a[92:95], v[60:63], v[20:23]\n" \
"s_waitcnt lgkmcnt(6)\n" \
"ds_read_b128 v[60:63], v111 offset:23552\n" \
"v_mfma_f32_16x16x32_bf16 v[0:3], a[96:99], v[32:35], v[0:3]\n" \
"v_mfma_f32_16x16x32_bf16 v[8:11], a[100:103], v[32:35], v[8:11]\n" \
"global_load_lds_dwordx4 v102, s[46:47] offset:3072 sc1\n" \
"v_mfma_f32_16x16x32_bf16 v[16:19], a[104:107], v[32:35], v[16:19]\n" \
"s_waitcnt lgkmcnt(6)\n" \
"ds_read_b128 v[32:35], v111 offset:24576\n" \
"v_mfma_f32_16x16x32_bf16 v[4:7], a[96:99], v[36:39], v[4:7]\n" \
"v_mfma_f32_16x16x32_bf16 v[12:15], a[100:103], v[36:39], v[12:15]\n" \
"v_mfma_f32_16x16x32_bf16 v[20:23], a[104:107], v[36:39], v[20:23]\n" \
"s_waitcnt lgkmcnt(6)\n" \
"ds_read_b128 v[36:39], v111 offset:25600\n" \
"v_mfma_f32_16x16x32_bf16 v[0:3], a[108:111], v[40:43], v[0:3]\n" \
"v_mfma_f32_16x16x32_bf16 v[8:11], a[112:115], v[40:43], v[8:11]\n" \
"v_mfma_f32_16x16x32_bf16 v[16:19], a[116:119], v[40:43], v[16:19]\n" \
"s_waitcnt lgkmcnt(6)\n" \
"ds_read_b128 v[40:43], v111 offset:26624\n" \
"v_mfma_f32_16x16x32_bf16 v[4:7], a[108:111], v[44:47], v[4:7]\n" \
"s_add_u32 m0, s60, 4096\n" \
"v_mfma_f32_16x16x32_bf16 v[12:15], a[112:115], v[44:47], v[12:15]\n" \
"global_load_lds_dwordx4 v103, s[46:47] offset:0 sc1\n" \
"v_mfma_f32_16x16x32_bf16 v[20:23], a[116:119], v[44:47], v[20:23]\n" \
"s_waitcnt lgkmcnt(6)\n" \
"ds_read_b128 v[44:47], v111 offset:27648\n" \
"v_mfma_f32_16x16x32_bf16 v[0:3], a[120:123], v[48:51], v[0:3]\n" \
"v_mfma_f32_16x16x32_bf16 v[8:11], a[124:127], v[48:51], v[8:11]\n" \
"v_mfma_f32_16x16x32_bf16 v[16:19], a[128:131], v[48:51], v[16:19]\n" \
"s_waitcnt lgkmcnt(6)\n" \
"ds_read_b128 v[48:51], v111 offset:28672\n" \
"v_mfma_f32_16x16x32_bf16 v[4:7], a[120:123], v[52:55], v[4:7]\n" \
"v_mfma_f32_16x16x32_bf16 v[12:15], a[124:127], v[52:55], v[12:15]\n" \
"v_mfma_f32_16x16x32_bf16 v[20:23], a[128:131], v[52:55], v[20:23]\n" \
"s_waitcnt lgkmcnt(6)\n" \
"ds_read_b128 v[52:55], v111 offset:29696\n" \
"v_mfma_f32_16x16x32_bf16 v[0:3], a[132:135], v[56:59], v[0:3]\n" \
"v_mfma_f32_16x16x32_bf16 v[8:11], a[136:139], v[56:59], v[8:11]\n" \
"global_load_lds_dwordx4 v103, s[46:47] offset:1024 sc1\n" \
"v_mfma_f32_16x16x32_bf16 v[16:19], a[140:143], v[56:59], v[16:19]\n" \
"s_waitcnt lgkmcnt(6)\n" \
"ds_read_b128 v[56:59], v111 offset:30720\n" \
"v_mfma_f32_16x16x32_bf16 v[4:7], a[132:135], v[60:63], v[4:7]\n" \
"s_add_u32 s62, s62, 49152\n" \
"v_mfma_f32_16x16x32_bf16 v[12:15], a[136:139], v[60:63], v[12:15]\n" \
"s_cmp_eq_u32 s62, 147456\n" \
"s_cselect_b32 s62, 0, s62\n" \
"v_mfma_f32_16x16x32_bf16 v[20:23], a[140:143], v[60:63], v[20:23]\n" \
"s_waitcnt lgkmcnt(6)\n" \
"ds_read_b128 v[60:63], v111 offset:31744\n" \
"v_mfma_f32_16x16x32_bf16 v[0:3], a[144:147], v[32:35], v[0:3]\n" \
"v_mfma_f32_16x16x32_bf16 v[8:11], a[148:151], v[32:35], v[8:11]\n" \
"v_mfma_f32_16x16x32_bf16 v[16:19], a[152:155], v[32:35], v[16:19]\n" \
"s_waitcnt lgkmcnt(6)\n" \
"ds_read_b128 v[32:35], v111 offset:32768\n" \
"v_mfma_f32_16x16x32_bf16 v[4:7], a[144:147], v[36:39], v[4:7]\n" \
"v_mfma_f32_16x16x32_bf16 v[12:15], a[148:151], v[36:39], v[12:15]\n" \
"global_load_lds_dwordx4 v103, s[46:47] offset:2048 sc1\n" \
"v_mfma_f32_16x16x32_bf16 v[20:23], a[152:155], v[36:39], v[20:23]\n" \
"s_waitcnt lgkmcnt(6)\n" \
"ds_read_b128 v[36:39], v111 offset:33792\n" \
"v_mfma_f32_16x16x32_bf16 v[0:3], a[156:159], v[40:43], v[0:3]\n" \
"v_mfma_f32_16x16x32_bf16 v[8:11], a[160:163], v[40:43], v[8:11]\n" \
"v_mfma_f32_16x16x32_bf16 v[16:19], a[164:167], v[40:43], v[16:19]\n" \
"s_waitcnt lgkmcnt(6)\n" \
"ds_read_b128 v[40:43], v111 offset:34816\n" \
"v_mfma_f32_16x16x32_bf16 v[4:7], a[156:159], v[44:47], v[4:7]\n" \
"v_mfma_f32_16x16x32_bf16 v[12:15], a[160:163], v[44:47], v[12:15]\n" \
"v_mfma_f32_16x16x32_bf16 v[20:23], a[164:167], v[44:47], v[20:23]\n" \
"s_waitcnt lgkmcnt(6)\n" \
"ds_read_b128 v[44:47], v111 offset:35840\n" \
"v_mfma_f32_16x16x32_bf16 v[0:3], a[168:171], v[48:51], v[0:3]\n" \
"v_mfma_f32_16x16x32_bf16 v[8:11], a[172:175], v[48:51], v[8:11]\n" \
"global_load_lds_dwordx4 v103, s[46:47] offset:3072 sc1\n" \
"v_mfma_f32_16x16x32_bf16 v[16:19], a[176:179], v[48:51], v[16:19]\n" \
"s_waitcnt lgkmcnt(6)\n" \
"ds_read_b128 v[48:51], v111 offset:36864\n" \
"v_mfma_f32_16x16x32_bf16 v[4:7], a[168:171], v[52:55], v[4:7]\n" \
"v_mfma_f32_16x16x32_bf16 v[12:15], a[172:175], v[52:55], v[12:15]\n" \
"v_mfma_f32_16x16x32_bf16 v[20:23], a[176:179], v[52:55], v[20:23]\n" \
"s_waitcnt lgkmcnt(6)\n" \
"ds_read_b128 v[52:55], v111 offset:37888\n" \
"v_mfma_f32_16x16x32_bf16 v[0:3], a[180:183], v[56:59], v[0:3]\n" \
"v_mfma_f32_16x16x32_bf16 v[8:11], a[184:187], v[56:59], v[8:11]\n" \
"v_mfma_f32_16x16x32_bf16 v[16:19], a[188:191], v[56:59], v[16:19]\n" \
"s_waitcnt lgkmcnt(6)\n" \
"ds_read_b128 v[56:59], v111 offset:38912\n" \
"v_mfma_f32_16x16x32_bf16 v[4:7], a[180:183], v[60:63], v[4:7]\n" \
"s_add_u32 m0, s60, 8192\n" \
"v_mfma_f32_16x16x32_bf16 v[12:15], a[184:187], v[60:63], v[12:15]\n" \
"global_load_lds_dwordx4 v104, s[46:47] offset:0 sc1\n" \
"v_mfma_f32_16x16x32_bf16 v[20:23], a[188:191], v[60:63], v[20:23]\n" \
"s_waitcnt lgkmcnt(6)\n" \
"ds_read_b128 v[60:63], v111 offset:39936\n" \
"v_mfma_f32_16x16x32_bf16 v[0:3], a[192:195], v[32:35], v[0:3]\n" \
"v_mfma_f32_16x16x32_bf16 v[8:11], a[196:199], v[32:35], v[8:11]\n" \
"v_mfma_f32_16x16x32_bf16 v[16:19], a[200:203], v[32:35], v[16:19]\n" \
"s_waitcnt lgkmcnt(6)\n" \
"ds_read_b128 v[32:35], v111 offset:40960\n" \
"v_mfma_f32_16x16x32_bf16 v[4:7], a[192:195], v[36:39], v[4:7]\n" \
"v_mfma_f32_16x16x32_bf16 v[12:15], a[196:199], v[36:39], v[12:15]\n" \
"v_mfma_f32_16x16x32_bf16 v[20:23], a[200:203], v[36:39], v[20:23]\n" \
"s_waitcnt lgkmcnt(6)\n" \
"ds_read_b128 v[36:39], v111 offset:41984\n" \
"v_mfma_f32_16x16x32_bf16 v[0:3], a[204:207], v[40:43], v[0:3]\n" \
"v_mfma_f32_16x16x32_bf16 v[8:11], a[208:211], v[40:43], v[8:11]\n" \
"global_load_lds_dwordx4 v104, s[46:47] offset:1024 sc1\n" \
"v_mfma_f32_16x16x32_bf16 v[16:19], a[212:215], v[40:43], v[16:19]\n" \
"s_waitcnt lgkmcnt(6)\n" \
"ds_read_b128 v[40:43], v111 offset:43008\n" \
"v_mfma_f32_16x16x32_bf16 v[4:7], a[204:207], v[44:47], v[4:7]\n" \
"v_mfma_f32_16x16x32_bf16 v[12:15], a[208:211], v[44:47], v[12:15]\n" \
"v_mfma_f32_16x16x32_bf16 v[20:23], a[212:215], v[44:47], v[20:23]\n" \
"s_waitcnt lgkmcnt(6)\n" \
"ds_read_b128 v[44:47], v111 offset:44032\n" \
"v_mfma_f32_16x16x32_bf16 v[0:3], a[216:219], v[48:51], v[0:3]\n" \
"v_mfma_f32_16x16x32_bf16 v[8:11], a[220:223], v[48:51], v[8:11]\n" \
"v_mfma_f32_16x16x32_bf16 v[16:19], a[224:227], v[48:51], v[16:19]\n" \
"s_waitcnt lgkmcnt(6)\n" \
"ds_read_b128 v[48:51], v111 offset:45056\n" \
"v_mfma_f32_16x16x32_bf16 v[4:7], a[216:219], v[52:55], v[4:7]\n" \
"v_mov_b32 v149, s58\n" \
"v_mfma_f32_16x16x32_bf16 v[12:15], a[220:223], v[52:55], v[12:15]\n" \
"global_load_lds_dwordx4 v104, s[46:47] offset:2048 sc1\n" \
"v_mfma_f32_16x16x32_bf16 v[20:23], a[224:227], v[52:55], v[20:23]\n" \
"s_waitcnt lgkmcnt(6)\n" \
"ds_read_b128 v[52:55], v111 offset:46080\n" \
"v_mfma_f32_16x16x32_bf16 v[0:3], a[228:231], v[56:59], v[0:3]\n" \
"s_waitcnt vmcnt(11)\n" \
"s_barrier\n" \
"v_mfma_f32_16x16x32_bf16 v[8:11], a[232:235], v[56:59], v[8:11]\n" \
"v_mfma_f32_16x16x32_bf16 v[16:19], a[236:239], v[56:59], v[16:19]\n" \
"s_waitcnt lgkmcnt(6)\n" \
"ds_read_b128 v[56:59], v111 offset:47104\n" \
"v_mfma_f32_16x16x32_bf16 v[4:7], a[228:231], v[60:63], v[4:7]\n" \
"v_mfma_f32_16x16x32_bf16 v[12:15], a[232:235], v[60:63], v[12:15]\n" \
"v_mfma_f32_16x16x32_bf16 v[20:23], a[236:239], v[60:63], v[20:23]\n" \
"s_waitcnt lgkmcnt(6)\n" \
"ds_read_b128 v[60:63], v111 offset:48128\n" \
"v_mfma_f32_16x16x32_bf16 v[0:3], a[240:243], v[32:35], v[0:3]\n" \
"v_add_u32 v111, s62, v101\n" \
"v_mfma_f32_16x16x32_bf16 v[8:11], a[244:247], v[32:35], v[8:11]\n" \
"global_load_lds_dwordx4 v104, s[46:47] offset:3072 sc1\n" \
"v_mfma_f32_16x16x32_bf16 v[16:19], a[248:251], v[32:35], v[16:19]\n" \
"s_waitcnt lgkmcnt(6)\n" \
"ds_read_b128 v[32:35], v111 offset:0\n" \
"v_mfma_f32_16x16x32_bf16 v[4:7], a[240:243], v[36:39], v[4:7]\n" \
"s_add_u32 s46, s46, 294912\n" \
"s_addc_u32 s47, s47, 0\n" \
"v_mfma_f32_16x16x32_bf16 v[12:15], a[244:247], v[36:39], v[12:15]\n" \
"s_add_u32 s60, s60, 49152\n" \
"v_mfma_f32_16x16x32_bf16 v[20:23], a[248:251], v[36:39], v[20:23]\n" \
"s_waitcnt lgkmcnt(6)\n" \
"ds_read_b128 v[36:39], v111 offset:1024\n" \
"v_mfma_f32_16x16x32_bf16 v[0:3], a[252:255], v[40:43], v[0:3]\n" \
"s_cmp_eq_u32 s60, s61\n" \
"s_cselect_b32 s60, s57, s60\n" \
"v_mfma_f32_16x16x32_bf16 v[8:11], v[224:227], v[40:43], v[8:11]\n" \
"v_mfma_f32_16x16x32_bf16 v[16:19], v[228:231], v[40:43], v[16:19]\n" \
"s_waitcnt lgkmcnt(6)\n" \
"ds_read_b128 v[40:43], v111 offset:2048\n" \
"v_mfma_f32_16x16x32_bf16 v[4:7], a[252:255], v[44:47], v[4:7]\n" \
"v_mfma_f32_16x16x32_bf16 v[12:15], v[224:227], v[44:47], v[12:15]\n" \
"v_mfma_f32_16x16x32_bf16 v[20:23], v[228:231], v[44:47], v[20:23]\n" \
"s_waitcnt lgkmcnt(6)\n" \
"ds_read_b128 v[44:47], v111 offset:3072\n" \
"v_mfma_f32_16x16x32_bf16 v[0:3], v[232:235], v[48:51], v[0:3]\n" \
"v_mfma_f32_16x16x32_bf16 v[8:11], v[236:239], v[48:51], v[8:11]\n" \
"v_mfma_f32_16x16x32_bf16 v[16:19], v[240:243], v[48:51], v[16:19]\n" \
"s_waitcnt lgkmcnt(6)\n" \
"ds_read_b128 v[48:51], v111 offset:4096\n" \
"v_mfma_f32_16x16x32_bf16 v[4:7], v[232:235], v[52:55], v[4:7]\n" \
"v_mfma_f32_16x16x32_bf16 v[12:15], v[236:239], v[52:55], v[12:15]\n" \
"v_mfma_f32_16x16x32_bf16 v[20:23], v[240:243], v[52:55], v[20:23]\n" \
"s_waitcnt lgkmcnt(6)\n" \
"ds_read_b128 v[52:55], v111 offset:5120\n" \
"v_mfma_f32_16x16x32_bf16 v[0:3], v[244:247], v[56:59], v[0:3]\n" \
"v_mfma_f32_16x16x32_bf16 v[8:11], v[248:251], v[56:59], v[8:11]\n" \
"v_mfma_f32_16x16x32_bf16 v[16:19], v[252:255], v[56:59], v[16:19]\n" \
"s_waitcnt lgkmcnt(6)\n" \
"ds_read_b128 v[56:59], v111 offset:6144\n" \
"v_mfma_f32_16x16x32_bf16 v[4:7], v[244:247], v[60:63], v[4:7]\n" \
"v_mfma_f32_16x16x32_bf16 v[12:15], v[248:251], v[60:63], v[12:15]\n" \
"v_mfma_f32_16x16x32_bf16 v[20:23], v[252:255], v[60:63], v[20:23]\n" \
"s_waitcnt lgkmcnt(6)\n" \
"ds_read_b128 v[60:63], v111 offset:7168\n" \
".Ltop%=:\n" \
"v_mfma_f32_16x16x32_bf16 v[112:115], a[0:3], v[32:35], 0\n" \
"s_barrier\n" \
"v_mfma_f32_16x16x32_bf16 v[120:123], a[4:7], v[32:35], 0\n" \
"v_cmp_gt_u32 vcc, s64, v99\n" \
"v_mfma_f32_16x16x32_bf16 v[128:131], a[8:11], v[32:35], 0\n" \
"s_waitcnt lgkmcnt(6)\n" \
"ds_read_b128 v[32:35], v111 offset:8192\n" \
"v_mfma_f32_16x16x32_bf16 v[116:119], a[0:3], v[36:39], 0\n" \
"s_cbranch_vccnz .Lhc2_s%=\n" \
".Lhc2_c%=:\n" \
"v_mfma_f32_16x16x32_bf16 v[124:127], a[4:7], v[36:39], 0\n" \
"global_load_dword v99, v105, s[54:55] sc1\n" \
"v_mfma_f32_16x16x32_bf16 v[132:135], a[8:11], v[36:39], 0\n" \
"s_waitcnt lgkmcnt(6)\n" \
"ds_read_b128 v[36:39], v111 offset:9216\n" \
"v_mfma_f32_16x16x32_bf16 v[112:115], a[12:15], v[40:43], v[112:115]\n" \
"v_cvt_pk_bf16_f32 v64, v0, v1\n" \
"v_cvt_pk_bf16_f32 v65, v2, v3\n" \
"v_mfma_f32_16x16x32_bf16 v[120:123], a[16:19], v[40:43], v[120:123]\n" \
"v_cvt_pk_bf16_f32 v66, v4, v5\n" \
"v_cvt_pk_bf16_f32 v67, v6, v7\n" \
"v_mfma_f32_16x16x32_bf16 v[128:131], a[20:23], v[40:43], v[128:131]\n" \
"s_waitcnt lgkmcnt(6)\n" \
"ds_read_b128 v[40:43], v111 offset:10240\n" \
"v_mfma_f32_16x16x32_bf16 v[116:119], a[12:15], v[44:47], v[116:119]\n" \
"global_store_dwordx4 v101, v[64:67], s[48:49] offset:0 sc1\n" \
"v_mfma_f32_16x16x32_bf16 v[124:127], a[16:19], v[44:47], v[124:127]\n" \
"v_cvt_pk_bf16_f32 v68, v8, v9\n" \
"v_cvt_pk_bf16_f32 v69, v10, v11\n" \
"v_mfma_f32_16x16x32_bf16 v[132:135], a[20:23], v[44:47], v[132:135]\n" \
"s_waitcnt lgkmcnt(6)\n" \
"ds_read_b128 v[44:47], v111 offset:11264\n" \
"v_mfma_f32_16x16x32_bf16 v[112:115], a[24:27], v[48:51], v[112:115]\n" \
"v_cvt_pk_bf16_f32 v70, v12, v13\n" \
"v_cvt_pk_bf16_f32 v71, v14, v15\n" \
"v_mfma_f32_16x16x32_bf16 v[120:123], a[28:31], v[48:51], v[120:123]\n" \
"global_store_dwordx4 v101, v[68:71], s[48:49] offset:1024 sc1\n" \
"v_mfma_f32_16x16x32_bf16 v[128:131], a[32:35], v[48:51], v[128:131]\n" \
"s_waitcnt lgkmcnt(6)\n" \
"ds_read_b128 v[48:51], v111 offset:12288\n" \
"v_mfma_f32_16x16x32_bf16 v[116:119], a[24:27], v[52:55], v[116:119]\n" \
"v_cvt_pk_bf16_f32 v72, v16, v17\n" \
"v_cvt_pk_bf16_f32 v73, v18, v19\n" \
"v_mfma_f32_16x16x32_bf16 v[124:127], a[28:31], v[52:55], v[124:127]\n" \
"v_cvt_pk_bf16_f32 v74, v20, v21\n" \
"v_cvt_pk_bf16_f32 v75, v22, v23\n" \
"v_mfma_f32_16x16x32_bf16 v[132:135], a[32:35], v[52:55], v[132:135]\n" \
"s_waitcnt lgkmcnt(6)\n" \
"ds_read_b128 v[52:55], v111 offset:13312\n" \
"v_mfma_f32_16x16x32_bf16 v[112:115], a[36:39], v[56:59], v[112:115]\n" \
"global_store_dwordx4 v101, v[72:75], s[48:49] offset:2048 sc1\n" \
"v_mfma_f32_16x16x32_bf16 v[120:123], a[40:43], v[56:59], v[120:123]\n" \
"s_add_u32 s64, s64, 1\n" \
"s_min_u32 s64, s64, 64\n" \
"v_mfma_f32_16x16x32_bf16 v[128:131], a[44:47], v[56:59], v[128:131]\n" \
"s_waitcnt lgkmcnt(6)\n" \
"ds_read_b128 v[56:59], v111 offset:14336\n" \
"v_mfma_f32_16x16x32_bf16 v[116:119], a[36:39], v[60:63], v[116:119]\n" \
"s_add_u32 m0, s60, 0\n" \
"v_mfma_f32_16x16x32_bf16 v[124:127], a[40:43], v[60:63], v[124:127]\n" \
"global_load_lds_dwordx4 v102, s[46:47] offset:0 sc1\n" \
"v_mfma_f32_16x16x32_bf16 v[132:135], a[44:47], v[60:63], v[132:135]\n" \
"s_waitcnt lgkmcnt(6)\n" \
"ds_read_b128 v[60:63], v111 offset:15360\n" \
"v_mfma_f32_16x16x32_bf16 v[112:115], a[48:51], v[32:35], v[112:115]\n" \
"s_add_u32 s48, s48, 221184\n" \
"s_addc_u32 s49, s49, 0\n" \
"v_mfma_f32_16x16x32_bf16 v[120:123], a[52:55], v[32:35], v[120:123]\n" \
"v_mfma_f32_16x16x32_bf16 v[128:131], a[56:59], v[32:35], v[128:131]\n" \
"s_waitcnt lgkmcnt(6)\n" \
"ds_read_b128 v[32:35], v111 offset:16384\n" \
"v_mfma_f32_16x16x32_bf16 v[116:119], a[48:51], v[36:39], v[116:119]\n" \
"v_mfma_f32_16x16x32_bf16 v[124:127], a[52:55], v[36:39], v[124:127]\n" \
"v_mfma_f32_16x16x32_bf16 v[132:135], a[56:59], v[36:39], v[132:135]\n" \
"s_waitcnt lgkmcnt(6)\n" \
"ds_read_b128 v[36:39], v111 offset:17408\n" \
"v_mfma_f32_16x16x32_bf16 v[112:115], a[60:63], v[40:43], v[112:115]\n" \
"v_mfma_f32_16x16x32_bf16 v[120:123], a[64:67], v[40:43], v[120:123]\n" \
"global_load_lds_dwordx4 v102, s[46:47] offset:1024 sc1\n" \
"v_mfma_f32_16x16x32_bf16 v[128:131], a[68:71], v[40:43], v[128:131]\n" \
"s_waitcnt lgkmcnt(6)\n" \
"ds_read_b128 v[40:43], v111 offset:18432\n" \
"v_mfma_f32_16x16x32_bf16 v[116:119], a[60:63], v[44:47], v[116:119]\n" \
"v_mfma_f32_16x16x32_bf16 v[124:127], a[64:67], v[44:47], v[124:127]\n" \
"v_mfma_f32_16x16x32_bf16 v[132:135], a[68:71], v[44:47], v[132:135]\n" \
"s_waitcnt lgkmcnt(6)\n" \
"ds_read_b128 v[44:47], v111 offset:19456\n" \
"v_mfma_f32_16x16x32_bf16 v[112:115], a[72:75], v[48:51], v[112:115]\n" \
"v_mfma_f32_16x16x32_bf16 v[120:123], a[76:79], v[48:51], v[120:123]\n" \
"v_mfma_f32_16x16x32_bf16 v[128:131], a[80:83], v[48:51], v[128:131]\n" \
"s_waitcnt lgkmcnt(6)\n" \
"ds_read_b128 v[48:51], v111 offset:20480\n" \
"v_mfma_f32_16x16x32_bf16 v[116:119], a[72:75], v[52:55], v[116:119]\n" \
"v_mfma_f32_16x16x32_bf16 v[124:127], a[76:79], v[52:55], v[124:127]\n" \
"global_load_lds_dwordx4 v102, s[46:47] offset:2048 sc1\n" \
"v_mfma_f32_16x16x32_bf16 v[132:135], a[80:83], v[52:55], v[132:135]\n" \
"s_waitcnt lgkmcnt(6)\n" \
"ds_read_b128 v[52:55], v111 offset:21504\n" \
"v_mfma_f32_16x16x32_bf16 v[112:115], a[84:87], v[56:59], v[112:115]\n" \
"v_mfma_f32_16x16x32_bf16 v[120:123], a[88:91], v[56:59], v[120:123]\n" \
"v_mfma_f32_16x16x32_bf16 v[128:131], a[92:95], v[56:59], v[128:131]\n" \
"s_waitcnt lgkmcnt(6)\n" \
"ds_read_b128 v[56:59], v111 offset:22528\n" \
"v_mfma_f32_16x16x32_bf16 v[116:119], a[84:87], v[60:63], v[116:119]\n" \
"v_mfma_f32_16x16x32_bf16 v[124:127], a[88:91], v[60:63], v[124:127]\n" \
"v_mfma_f32_16x16x32_bf16 v[132:135], a[92:95], v[60:63], v[132:135]\n" \
"s_waitcnt lgkmcnt(6)\n" \
"ds_read_b128 v[60:63], v111 offset:23552\n" \
"v_mfma_f32_16x16x32_bf16 v[112:115], a[96:99], v[32:35], v[112:115]\n" \
"v_mfma_f32_16x16x32_bf16 v[120:123], a[100:103], v[32:35], v[120:123]\n" \
"global_load_lds_dwordx4 v102, s[46:47] offset:3072 sc1\n" \
"v_mfma_f32_16x16x32_bf16 v[128:131], a[104:107], v[32:35], v[128:131]\n" \
"s_waitcnt lgkmcnt(6)\n" \
"ds_read_b128 v[32:35], v111 offset:24576\n" \
"v_mfma_f32_16x16x32_bf16 v[116:119], a[96:99], v[36:39], v[116:119]\n" \
"v_mfma_f32_16x16x32_bf16 v[124:127], a[100:103], v[36:39], v[124:127]\n" \
"v_mfma_f32_16x16x32_bf16 v[132:135], a[104:107], v[36:39], v[132:135]\n" \
"s_waitcnt lgkmcnt(6)\n" \
"ds_read_b128 v[36:39], v111 offset:25600\n" \
"v_mfma_f32_16x16x32_bf16 v[112:115], a[108:111], v[40:43], v[112:115]\n" \
"v_mfma_f32_16x16x32_bf16 v[120:123], a[112:115], v[40:43], v[120:123]\n" \
"v_mfma_f32_16x16x32_bf16 v[128:131], a[116:119], v[40:43], v[128:131]\n" \
"s_waitcnt lgkmcnt(6)\n" \
"ds_read_b128 v[40:43], v111 offset:26624\n" \
"v_mfma_f32_16x16x32_bf16 v[116:119], a[108:111], v[44:47], v[116:119]\n" \
"s_add_u32 m0, s60, 4096\n" \
"v_mfma_f32_16x16x32_bf16 v[124:127], a[112:115], v[44:47], v[124:127]\n" \
"global_load_lds_dwordx4 v103, s[46:47] offset:0 sc1\n" \
"v_mfma_f32_16x16x32_bf16 v[132:135], a[116:119], v[44:47], v[132:135]\n" \
"s_waitcnt lgkmcnt(6)\n" \
"ds_read_b128 v[44:47], v111 offset:27648\n" \
"v_mfma_f32_16x16x32_bf16 v[112:115], a[120:123], v[48:51], v[112:115]\n" \
"v_mfma_f32_16x16x32_bf16 v[120:123], a[124:127], v[48:51], v[120:123]\n" \
"v_mfma_f32_16x16x32_bf16 v[128:131], a[128:131], v[48:51], v[128:131]\n" \
"s_waitcnt lgkmcnt(6)\n" \
"ds_read_b128 v[48:51], v111 offset:28672\n" \
"v_mfma_f32_16x16x32_bf16 v[116:119], a[120:123], v[52:55], v[116:119]\n" \
"v_mfma_f32_16x16x32_bf16 v[124:127], a[124:127], v[52:55], v[124:127]\n" \
"v_mfma_f32_16x16x32_bf16 v[132:135], a[128:131], v[52:55], v[132:135]\n" \
"s_waitcnt lgkmcnt(6)\n" \
"ds_read_b128 v[52:55], v111 offset:29696\n" \
"v_mfma_f32_16x16x32_bf16 v[112:115], a[132:135], v[56:59], v[112:115]\n" \
"v_mfma_f32_16x16x32_bf16 v[120:123], a[136:139], v[56:59], v[120:123]\n" \
"global_load_lds_dwordx4 v103, s[46:47] offset:1024 sc1\n" \
"v_mfma_f32_16x16x32_bf16 v[128:131], a[140:143], v[56:59], v[128:131]\n" \
"s_waitcnt lgkmcnt(6)\n" \
"ds_read_b128 v[56:59], v111 offset:30720\n" \
"v_mfma_f32_16x16x32_bf16 v[116:119], a[132:135], v[60:63], v[116:119]\n" \
"s_add_u32 s62, s62, 49152\n" \
"v_mfma_f32_16x16x32_bf16 v[124:127], a[136:139], v[60:63], v[124:127]\n" \
"s_cmp_eq_u32 s62, 147456\n" \
"s_cselect_b32 s62, 0, s62\n" \
"v_mfma_f32_16x16x32_bf16 v[132:135], a[140:143], v[60:63], v[132:135]\n" \
"s_waitcnt lgkmcnt(6)\n" \
"ds_read_b128 v[60:63], v111 offset:31744\n" \
"v_mfma_f32_16x16x32_bf16 v[112:115], a[144:147], v[32:35], v[112:115]\n" \
"v_mfma_f32_16x16x32_bf16 v[120:123], a[148:151], v[32:35], v[120:123]\n" \
"v_mfma_f32_16x16x32_bf16 v[128:131], a[152:155], v[32:35], v[128:131]\n" \
"s_waitcnt lgkmcnt(6)\n" \
"ds_read_b128 v[32:35], v111 offset:32768\n" \
"v_mfma_f32_16x16x32_bf16 v[116:119], a[144:147], v[36:39], v[116:119]\n" \
"v_mfma_f32_16x16x32_bf16 v[124:127], a[148:151], v[36:39], v[124:127]\n" \
"global_load_lds_dwordx4 v103, s[46:47] offset:2048 sc1\n" \
"v_mfma_f32_16x16x32_bf16 v[132:135], a[152:155], v[36:39], v[132:135]\n" \
"s_waitcnt lgkmcnt(6)\n" \
"ds_read_b128 v[36:39], v111 offset:33792\n" \
"v_mfma_f32_16x16x32_bf16 v[112:115], a[156:159], v[40:43], v[112:115]\n" \
"v_mfma_f32_16x16x32_bf16 v[120:123], a[160:163], v[40:43], v[120:123]\n" \
"v_mfma_f32_16x16x32_bf16 v[128:131], a[164:167], v[40:43], v[128:131]\n" \
"s_waitcnt lgkmcnt(6)\n" \
"ds_read_b128 v[40:43], v111 offset:34816\n" \
"v_mfma_f32_16x16x32_bf16 v[116:119], a[156:159], v[44:47], v[116:119]\n" \
"v_mfma_f32_16x16x32_bf16 v[124:127], a[160:163], v[44:47], v[124:127]\n" \
"v_mfma_f32_16x16x32_bf16 v[132:135], a[164:167], v[44:47], v[132:135]\n" \
"s_waitcnt lgkmcnt(6)\n" \
"ds_read_b128 v[44:47], v111 offset:35840\n" \
"v_mfma_f32_16x16x32_bf16 v[112:115], a[168:171], v[48:51], v[112:115]\n" \
"v_mfma_f32_16x16x32_bf16 v[120:123], a[172:175], v[48:51], v[120:123]\n" \
"global_load_lds_dwordx4 v103, s[46:47] offset:3072 sc1\n" \
"v_mfma_f32_16x16x32_bf16 v[128:131], a[176:179], v[48:51], v[128:131]\n" \
"s_waitcnt lgkmcnt(6)\n" \
"ds_read_b128 v[48:51], v111 offset:36864\n" \
"v_mfma_f32_16x16x32_bf16 v[116:119], a[168:171], v[52:55], v[116:119]\n" \
"v_mfma_f32_16x16x32_bf16 v[124:127], a[172:175], v[52:55], v[124:127]\n" \
"v_mfma_f32_16x16x32_bf16 v[132:135], a[176:179], v[52:55], v[132:135]\n" \
"s_waitcnt lgkmcnt(6)\n" \
"ds_read_b128 v[52:55], v111 offset:37888\n" \
"v_mfma_f32_16x16x32_bf16 v[112:115], a[180:183], v[56:59], v[112:115]\n" \
"v_mfma_f32_16x16x32_bf16 v[120:123], a[184:187], v[56:59], v[120:123]\n" \
"v_mfma_f32_16x16x32_bf16 v[128:131], a[188:191], v[56:59], v[128:131]\n" \
"s_waitcnt lgkmcnt(6)\n" \
"ds_read_b128 v[56:59], v111 offset:38912\n" \
"v_mfma_f32_16x16x32_bf16 v[116:119], a[180:183], v[60:63], v[116:119]\n" \
"s_add_u32 m0, s60, 8192\n" \
"v_mfma_f32_16x16x32_bf16 v[124:127], a[184:187], v[60:63], v[124:127]\n" \
"global_load_lds_dwordx4 v104, s[46:47] offset:0 sc1\n" \
"v_mfma_f32_16x16x32_bf16 v[132:135], a[188:191], v[60:63], v[132:135]\n" \
"s_waitcnt lgkmcnt(6)\n" \
"ds_read_b128 v[60:63], v111 offset:39936\n" \
"v_mfma_f32_16x16x32_bf16 v[112:115], a[192:195], v[32:35], v[112:115]\n" \
"v_mfma_f32_16x16x32_bf16 v[120:123], a[196:199], v[32:35], v[120:123]\n" \
"v_mfma_f32_16x16x32_bf16 v[128:131], a[200:203], v[32:35], v[128:131]\n" \
"s_waitcnt lgkmcnt(6)\n" \
"ds_read_b128 v[32:35], v111 offset:40960\n" \
"v_mfma_f32_16x16x32_bf16 v[116:119], a[192:195], v[36:39], v[116:119]\n" \
"v_mfma_f32_16x16x32_bf16 v[124:127], a[196:199], v[36:39], v[124:127]\n" \
"v_mfma_f32_16x16x32_bf16 v[132:135], a[200:203], v[36:39], v[132:135]\n" \
"s_waitcnt lgkmcnt(6)\n" \
"ds_read_b128 v[36:39], v111 offset:41984\n" \
"v_mfma_f32_16x16x32_bf16 v[112:115], a[204:207], v[40:43], v[112:115]\n" \
"v_mfma_f32_16x16x32_bf16 v[120:123], a[208:211], v[40:43], v[120:123]\n" \
"global_load_lds_dwordx4 v104, s[46:47] offset:1024 sc1\n" \
"v_mfma_f32_16x16x32_bf16 v[128:131], a[212:215], v[40:43], v[128:131]\n" \
"s_waitcnt lgkmcnt(6)\n" \
"ds_read_b128 v[40:43], v111 offset:43008\n" \
"v_mfma_f32_16x16x32_bf16 v[116:119], a[204:207], v[44:47], v[116:119]\n" \
"v_mfma_f32_16x16x32_bf16 v[124:127], a[208:211], v[44:47], v[124:127]\n" \
"v_mfma_f32_16x16x32_bf16 v[132:135], a[212:215], v[44:47], v[132:135]\n" \
"s_waitcnt lgkmcnt(6)\n" \
"ds_read_b128 v[44:47], v111 offset:44032\n" \
"v_mfma_f32_16x16x32_bf16 v[112:115], a[216:219], v[48:51], v[112:115]\n" \
"v_mfma_f32_16x16x32_bf16 v[120:123], a[220:223], v[48:51], v[120:123]\n" \
"v_mfma_f32_16x16x32_bf16 v[128:131], a[224:227], v[48:51], v[128:131]\n" \
"s_waitcnt lgkmcnt(6)\n" \
"ds_read_b128 v[48:51], v111 offset:45056\n" \
"v_mfma_f32_16x16x32_bf16 v[116:119], a[216:219], v[52:55], v[116:119]\n" \
"v_mov_b32 v149, s58\n" \
"v_mfma_f32_16x16x32_bf16 v[124:127], a[220:223], v[52:55], v[124:127]\n" \
"global_load_lds_dwordx4 v104, s[46:47] offset:2048 sc1\n" \
"v_mfma_f32_16x16x32_bf16 v[132:135], a[224:227], v[52:55], v[132:135]\n" \
"s_waitcnt lgkmcnt(6)\n" \
"ds_read_b128 v[52:55], v111 offset:46080\n" \
"v_mfma_f32_16x16x32_bf16 v[112:115], a[228:231], v[56:59], v[112:115]\n" \
"s_waitcnt vmcnt(11)\n" \
"s_barrier\n" \
"v_mfma_f32_16x16x32_bf16 v[120:123], a[232:235], v[56:59], v[120:123]\n" \
"s_mov_b64 exec, 1\n" \
"global_store_dword v107, v149, s[52:53] sc1\n" \
"s_mov_b64 exec, -1\n" \
"v_mfma_f32_16x16x32_bf16 v[128:131], a[236:239], v[56:59], v[128:131]\n" \
"s_waitcnt lgkmcnt(6)\n" \
"ds_read_b128 v[56:59], v111 offset:47104\n" \
"v_mfma_f32_16x16x32_bf16 v[116:119], a[228:231], v[60:63], v[116:119]\n" \
"s_add_u32 s58, s58, 1\n" \
"v_mfma_f32_16x16x32_bf16 v[124:127], a[232:235], v[60:63], v[124:127]\n" \
"v_mfma_f32_16x16x32_bf16 v[132:135], a[236:239], v[60:63], v[132:135]\n" \
"s_waitcnt lgkmcnt(6)\n" \
"ds_read_b128 v[60:63], v111 offset:48128\n" \
"v_mfma_f32_16x16x32_bf16 v[112:115], a[240:243], v[32:35], v[112:115]\n" \
"v_add_u32 v111, s62, v101\n" \
"v_mfma_f32_16x16x32_bf16 v[120:123], a[244:247], v[32:35], v[120:123]\n" \
"global_load_lds_dwordx4 v104, s[46:47] offset:3072 sc1\n" \
"v_mfma_f32_16x16x32_bf16 v[128:131], a[248:251], v[32:35], v[128:131]\n" \
"s_waitcnt lgkmcnt(6)\n" \
"ds_read_b128 v[32:35], v111 offset:0\n" \
"v_mfma_f32_16x16x32_bf16 v[116:119], a[240:243], v[36:39], v[116:119]\n" \
"s_add_u32 s46, s46, 294912\n" \
"s_addc_u32 s47, s47, 0\n" \
"v_mfma_f32_16x16x32_bf16 v[124:127], a[244:247], v[36:39], v[124:127]\n" \
"s_add_u32 s60, s60, 49152\n" \
"v_mfma_f32_16x16x32_bf16 v[132:135], a[248:251], v[36:39], v[132:135]\n" \
"s_waitcnt lgkmcnt(6)\n" \
"ds_read_b128 v[36:39], v111 offset:1024\n" \
"v_mfma_f32_16x16x32_bf16 v[112:115], a[252:255], v[40:43], v[112:115]\n" \
"s_cmp_eq_u32 s60, s61\n" \
"s_cselect_b32 s60, s57, s60\n" \
"v_mfma_f32_16x16x32_bf16 v[120:123], v[224:227], v[40:43], v[120:123]\n" \
"v_mfma_f32_16x16x32_bf16 v[128:131], v[228:231], v[40:43], v[128:131]\n" \
"s_waitcnt lgkmcnt(6)\n" \
"ds_read_b128 v[40:43], v111 offset:2048\n" \
"v_mfma_f32_16x16x32_bf16 v[116:119], a[252:255], v[44:47], v[116:119]\n" \
"v_mfma_f32_16x16x32_bf16 v[124:127], v[224:227], v[44:47], v[124:127]\n" \
"v_mfma_f32_16x16x32_bf16 v[132:135], v[228:231], v[44:47], v[132:135]\n" \
"s_waitcnt lgkmcnt(6)\n" \
"ds_read_b128 v[44:47], v111 offset:3072\n" \
"v_mfma_f32_16x16x32_bf16 v[112:115], v[232:235], v[48:51], v[112:115]\n" \
"v_mfma_f32_16x16x32_bf16 v[120:123], v[236:239], v[48:51], v[120:123]\n" \
"v_mfma_f32_16x16x32_bf16 v[128:131], v[240:243], v[48:51], v[128:131]\n" \
"s_waitcnt lgkmcnt(6)\n" \
"ds_read_b128 v[48:51], v111 offset:4096\n" \
"v_mfma_f32_16x16x32_bf16 v[116:119], v[232:235], v[52:55], v[116:119]\n" \
"v_mfma_f32_16x16x32_bf16 v[124:127], v[236:239], v[52:55], v[124:127]\n" \
"v_mfma_f32_16x16x32_bf16 v[132:135], v[240:243], v[52:55], v[132:135]\n" \
"s_waitcnt lgkmcnt(6)\n" \
"ds_read_b128 v[52:55], v111 offset:5120\n" \
"v_mfma_f32_16x16x32_bf16 v[112:115], v[244:247], v[56:59], v[112:115]\n" \
"v_mfma_f32_16x16x32_bf16 v[120:123], v[248:251], v[56:59], v[120:123]\n" \
"v_mfma_f32_16x16x32_bf16 v[128:131], v[252:255], v[56:59], v[128:131]\n" \
"s_waitcnt lgkmcnt(6)\n" \
"ds_read_b128 v[56:59], v111 offset:6144\n" \
"v_mfma_f32_16x16x32_bf16 v[116:119], v[244:247], v[60:63], v[116:119]\n" \
"v_mfma_f32_16x16x32_bf16 v[124:127], v[248:251], v[60:63], v[124:127]\n" \
"s_sub_u32 s51, s51, 1\n" \
"s_cmp_eq_u32 s51, 0\n" \
"v_mfma_f32_16x16x32_bf16 v[132:135], v[252:255], v[60:63], v[132:135]\n" \
"s_waitcnt lgkmcnt(6)\n" \
"ds_read_b128 v[60:63], v111 offset:7168\n" \
"s_cbranch_scc1 .Lfin%=\n" \
"v_mfma_f32_16x16x32_bf16 v[0:3], a[0:3], v[32:35], 0\n" \
"s_barrier\n" \
"v_mfma_f32_16x16x32_bf16 v[8:11], a[4:7], v[32:35], 0\n" \
"v_cmp_gt_u32 vcc, s64, v99\n" \
"v_mfma_f32_16x16x32_bf16 v[16:19], a[8:11], v[32:35], 0\n" \
"s_waitcnt lgkmcnt(6)\n" \
"ds_read_b128 v[32:35], v111 offset:8192\n" \
"v_mfma_f32_16x16x32_bf16 v[4:7], a[0:3], v[36:39], 0\n" \
"s_cbranch_vccnz .Lhc3_s%=\n" \
".Lhc3_c%=:\n" \
"v_mfma_f32_16x16x32_bf16 v[12:15], a[4:7], v[36:39], 0\n" \
"global_load_dword v99, v105, s[54:55] sc1\n" \
"v_mfma_f32_16x16x32_bf16 v[20:23], a[8:11], v[36:39], 0\n" \
"s_waitcnt lgkmcnt(6)\n" \
"ds_read_b128 v[36:39], v111 offset:9216\n" \
"v_mfma_f32_16x16x32_bf16 v[0:3], a[12:15], v[40:43], v[0:3]\n" \
"v_cvt_pk_bf16_f32 v64, v112, v113\n" \
"v_cvt_pk_bf16_f32 v65, v114, v115\n" \
"v_mfma_f32_16x16x32_bf16 v[8:11], a[16:19], v[40:43], v[8:11]\n" \
"v_cvt_pk_bf16_f32 v66, v116, v117\n" \
"v_cvt_pk_bf16_f32 v67, v118, v119\n" \
"v_mfma_f32_16x16x32_bf16 v[16:19], a[20:23], v[40:43], v[16:19]\n" \
"s_waitcnt lgkmcnt(6)\n" \
"ds_read_b128 v[40:43], v111 offset:10240\n" \
"v_mfma_f32_16x16x32_bf16 v[4:7], a[12:15], v[44:47], v[4:7]\n" \
"global_store_dwordx4 v101, v[64:67], s[48:49] offset:0 sc1\n" \
"v_mfma_f32_16x16x32_bf16 v[12:15], a[16:19], v[44:47], v[12:15]\n" \
"v_cvt_pk_bf16_f32 v68, v120, v121\n" \
"v_cvt_pk_bf16_f32 v69, v122, v123\n" \
"v_mfma_f32_16x16x32_bf16 v[20:23], a[20:23], v[44:47], v[20:23]\n" \
"s_waitcnt lgkmcnt(6)\n" \
"ds_read_b128 v[44:47], v111 offset:11264\n" \
"v_mfma_f32_16x16x32_bf16 v[0:3], a[24:27], v[48:51], v[0:3]\n" \
"v_cvt_pk_bf16_f32 v70, v124, v125\n" \
"v_cvt_pk_bf16_f32 v71, v126, v127\n" \
"v_mfma_f32_16x16x32_bf16 v[8:11], a[28:31], v[48:51], v[8:11]\n" \
"global_store_dwordx4 v101, v[68:71], s[48:49] offset:1024 sc1\n" \
"v_mfma_f32_16x16x32_bf16 v[16:19], a[32:35], v[48:51], v[16:19]\n" \
"s_waitcnt lgkmcnt(6)\n" \
"ds_read_b128 v[48:51], v111 offset:12288\n" \
"v_mfma_f32_16x16x32_bf16 v[4:7], a[24:27], v[52:55], v[4:7]\n" \
"v_cvt_pk_bf16_f32 v72, v128, v129\n" \
"v_cvt_pk_bf16_f32 v73, v130, v131\n" \
"v_mfma_f32_16x16x32_bf16 v[12:15], a[28:31], v[52:55], v[12:15]\n" \
"v_cvt_pk_bf16_f32 v74, v132, v133\n" \
"v_cvt_pk_bf16_f32 v75, v134, v135\n" \
"v_mfma_f32_16x16x32_bf16 v[20:23], a[32:35], v[52:55], v[20:23]\n" \
"s_waitcnt lgkmcnt(6)\n" \
"ds_read_b128 v[52:55], v111 offset:13312\n" \
"v_mfma_f32_16x16x32_bf16 v[0:3], a[36:39], v[56:59], v[0:3]\n" \
"global_store_dwordx4 v101, v[72:75], s[48:49] offset:2048 sc1\n" \
"v_mfma_f32_16x16x32_bf16 v[8:11], a[40:43], v[56:59], v[8:11]\n" \
"s_add_u32 s64, s64, 1\n" \
"s_min_u32 s64, s64, 64\n" \
"v_mfma_f32_16x16x32_bf16 v[16:19], a[44:47], v[56:59], v[16:19]\n" \
"s_waitcnt lgkmcnt(6)\n" \
"ds_read_b128 v[56:59], v111 offset:14336\n" \
"v_mfma_f32_16x16x32_bf16 v[4:7], a[36:39], v[60:63], v[4:7]\n" \
"s_add_u32 m0, s60, 0\n" \
"v_mfma_f32_16x16x32_bf16 v[12:15], a[40:43], v[60:63], v[12:15]\n" \
"global_load_lds_dwordx4 v102, s[46:47] offset:0 sc1\n" \
"v_mfma_f32_16x16x32_bf16 v[20:23], a[44:47], v[60:63], v[20:23]\n" \
"s_waitcnt lgkmcnt(6)\n" \
"ds_read_b128 v[60:63], v111 offset:15360\n" \
"v_mfma_f32_16x16x32_bf16 v[0:3], a[48:51], v[32:35], v[0:3]\n" \
"s_add_u32 s48, s48, 221184\n" \
"s_addc_u32 s49, s49, 0\n" \
"v_mfma_f32_16x16x32_bf16 v[8:11], a[52:55], v[32:35], v[8:11]\n" \
"v_mfma_f32_16x16x32_bf16 v[16:19], a[56:59], v[32:35], v[16:19]\n" \
"s_waitcnt lgkmcnt(6)\n" \
"ds_read_b128 v[32:35], v111 offset:16384\n" \
"v_mfma_f32_16x16x32_bf16 v[4:7], a[48:51], v[36:39], v[4:7]\n" \
"v_mfma_f32_16x16x32_bf16 v[12:15], a[52:55], v[36:39], v[12:15]\n" \
"v_mfma_f32_16x16x32_bf16 v[20:23], a[56:59], v[36:39], v[20:23]\n" \
"s_waitcnt lgkmcnt(6)\n" \
"ds_read_b128 v[36:39], v111 offset:17408\n" \
"v_mfma_f32_16x16x32_bf16 v[0:3], a[60:63], v[40:43], v[0:3]\n" \
"v_mfma_f32_16x16x32_bf16 v[8:11], a[64:67], v[40:43], v[8:11]\n" \
"global_load_lds_dwordx4 v102, s[46:47] offset:1024 sc1\n" \
"v_mfma_f32_16x16x32_bf16 v[16:19], a[68:71], v[40:43], v[16:19]\n" \
"s_waitcnt lgkmcnt(6)\n" \
"ds_read_b128 v[40:43], v111 offset:18432\n" \
"v_mfma_f32_16x16x32_bf16 v[4:7], a[60:63], v[44:47], v[4:7]\n" \
"v_mfma_f32_16x16x32_bf16 v[12:15], a[64:67], v[44:47], v[12:15]\n" \
"v_mfma_f32_16x16x32_bf16 v[20:23], a[68:71], v[44:47], v[20:23]\n" \
"s_waitcnt lgkmcnt(6)\n" \
"ds_read_b128 v[44:47], v111 offset:19456\n" \
"v_mfma_f32_16x16x32_bf16 v[0:3], a[72:75], v[48:51], v[0:3]\n" \
"v_mfma_f32_16x16x32_bf16 v[8:11], a[76:79], v[48:51], v[8:11]\n" \
"v_mfma_f32_16x16x32_bf16 v[16:19], a[80:83], v[48:51], v[16:19]\n" \
"s_waitcnt lgkmcnt(6)\n" \
"ds_read_b128 v[48:51], v111 offset:20480\n" \
"v_mfma_f32_16x16x32_bf16 v[4:7], a[72:75], v[52:55], v[4:7]\n" \
"v_mfma_f32_16x16x32_bf16 v[12:15], a[76:79], v[52:55], v[12:15]\n" \
"global_load_lds_dwordx4 v102, s[46:47] offset:2048 sc1\n" \
"v_mfma_f32_16x16x32_bf16 v[20:23], a[80:83], v[52:55], v[20:23]\n" \
"s_waitcnt lgkmcnt(6)\n" \
"ds_read_b128 v[52:55], v111 offset:21504\n" \
"v_mfma_f32_16x16x32_bf16 v[0:3], a[84:87], v[56:59], v[0:3]\n" \
"v_mfma_f32_16x16x32_bf16 v[8:11], a[88:91], v[56:59], v[8:11]\n" \
"v_mfma_f32_16x16x32_bf16 v[16:19], a[92:95], v[56:59], v[16:19]\n" \
"s_waitcnt lgkmcnt(6)\n" \
"ds_read_b128 v[56:59], v111 offset:22528\n" \
"v_mfma_f32_16x16x32_bf16 v[4:7], a[84:87], v[60:63], v[4:7]\n" \
"v_mfma_f32_16x16x32_bf16 v[12:15], a[88:91], v[60:63], v[12:15]\n" \
"v_mfma_f32_16x16x32_bf16 v[20:23], a[92:95], v[60:63], v[20:23]\n" \
"s_waitcnt lgkmcnt(6)\n" \
"ds_read_b128 v[60:63], v111 offset:23552\n" \
"v_mfma_f32_16x16x32_bf16 v[0:3], a[96:99], v[32:35], v[0:3]\n" \
"v_mfma_f32_16x16x32_bf16 v[8:11], a[100:103], v[32:35], v[8:11]\n" \
"global_load_lds_dwordx4 v102, s[46:47] offset:3072 sc1\n" \
"v_mfma_f32_16x16x32_bf16 v[16:19], a[104:107], v[32:35], v[16:19]\n" \
"s_waitcnt lgkmcnt(6)\n" \
"ds_read_b128 v[32:35], v111 offset:24576\n" \
"v_mfma_f32_16x16x32_bf16 v[4:7], a[96:99], v[36:39], v[4:7]\n" \
"v_mfma_f32_16x16x32_bf16 v[12:15], a[100:103], v[36:39], v[12:15]\n" \
"v_mfma_f32_16x16x32_bf16 v[20:23], a[104:107], v[36:39], v[20:23]\n" \
"s_waitcnt lgkmcnt(6)\n" \
"ds_read_b128 v[36:39], v111 offset:25600\n" \
"v_mfma_f32_16x16x32_bf16 v[0:3], a[108:111], v[40:43], v[0:3]\n" \
"v_mfma_f32_16x16x32_bf16 v[8:11], a[112:115], v[40:43], v[8:11]\n" \
"v_mfma_f32_16x16x32_bf16 v[16:19], a[116:119], v[40:43], v[16:19]\n" \
"s_waitcnt lgkmcnt(6)\n" \
"ds_read_b128 v[40:43], v111 offset:26624\n" \
"v_mfma_f32_16x16x32_bf16 v[4:7], a[108:111], v[44:47], v[4:7]\n" \
"s_add_u32 m0, s60, 4096\n" \
"v_mfma_f32_16x16x32_bf16 v[12:15], a[112:115], v[44:47], v[12:15]\n" \
"global_load_lds_dwordx4 v103, s[46:47] offset:0 sc1\n" \
"v_mfma_f32_16x16x32_bf16 v[20:23], a[116:119], v[44:47], v[20:23]\n" \
"s_waitcnt lgkmcnt(6)\n" \
"ds_read_b128 v[44:47], v111 offset:27648\n" \
"v_mfma_f32_16x16x32_bf16 v[0:3], a[120:123], v[48:51], v[0:3]\n" \
"v_mfma_f32_16x16x32_bf16 v[8:11], a[124:127], v[48:51], v[8:11]\n" \
"v_mfma_f32_16x16x32_bf16 v[16:19], a[128:131], v[48:51], v[16:19]\n" \
"s_waitcnt lgkmcnt(6)\n" \
"ds_read_b128 v[48:51], v111 offset:28672\n" \
"v_mfma_f32_16x16x32_bf16 v[4:7], a[120:123], v[52:55], v[4:7]\n" \
"v_mfma_f32_16x16x32_bf16 v[12:15], a[124:127], v[52:55], v[12:15]\n" \
"v_mfma_f32_16x16x32_bf16 v[20:23], a[128:131], v[52:55], v[20:23]\n" \
"s_waitcnt lgkmcnt(6)\n" \
"ds_read_b128 v[52:55], v111 offset:29696\n" \
"v_mfma_f32_16x16x32_bf16 v[0:3], a[132:135], v[56:59], v[0:3]\n" \
"v_mfma_f32_16x16x32_bf16 v[8:11], a[136:139], v[56:59], v[8:11]\n" \
"global_load_lds_dwordx4 v103, s[46:47] offset:1024 sc1\n" \
"v_mfma_f32_16x16x32_bf16 v[16:19], a[140:143], v[56:59], v[16:19]\n" \
"s_waitcnt lgkmcnt(6)\n" \
"ds_read_b128 v[56:59], v111 offset:30720\n" \
"v_mfma_f32_16x16x32_bf16 v[4:7], a[132:135], v[60:63], v[4:7]\n" \
"s_add_u32 s62, s62, 49152\n" \
"v_mfma_f32_16x16x32_bf16 v[12:15], a[136:139], v[60:63], v[12:15]\n" \
"s_cmp_eq_u32 s62, 147456\n" \
"s_cselect_b32 s62, 0, s62\n" \
"v_mfma_f32_16x16x32_bf16 v[20:23], a[140:143], v[60:63], v[20:23]\n" \
"s_waitcnt lgkmcnt(6)\n" \
"ds_read_b128 v[60:63], v111 offset:31744\n" \
"v_mfma_f32_16x16x32_bf16 v[0:3], a[144:147], v[32:35], v[0:3]\n" \
"v_mfma_f32_16x16x32_bf16 v[8:11], a[148:151], v[32:35], v[8:11]\n" \
"v_mfma_f32_16x16x32_bf16 v[16:19], a[152:155], v[32:35], v[16:19]\n" \
"s_waitcnt lgkmcnt(6)\n" \
"ds_read_b128 v[32:35], v111 offset:32768\n" \
"v_mfma_f32_16x16x32_bf16 v[4:7], a[144:147], v[36:39], v[4:7]\n" \
"v_mfma_f32_16x16x32_bf16 v[12:15], a[148:151], v[36:39], v[12:15]\n" \
"global_load_lds_dwordx4 v103, s[46:47] offset:2048 sc1\n" \
"v_mfma_f32_16x16x32_bf16 v[20:23], a[152:155], v[36:39], v[20:23]\n" \
"s_waitcnt lgkmcnt(6)\n" \
"ds_read_b128 v[36:39], v111 offset:33792\n" \
"v_mfma_f32_16x16x32_bf16 v[0:3], a[156:159], v[40:43], v[0:3]\n" \
"v_mfma_f32_16x16x32_bf16 v[8:11], a[160:163], v[40:43], v[8:11]\n" \
"v_mfma_f32_16x16x32_bf16 v[16:19], a[164:167], v[40:43], v[16:19]\n" \
"s_waitcnt lgkmcnt(6)\n" \
"ds_read_b128 v[40:43], v111 offset:34816\n" \
"v_mfma_f32_16x16x32_bf16 v[4:7], a[156:159], v[44:47], v[4:7]\n" \
"v_mfma_f32_16x16x32_bf16 v[12:15], a[160:163], v[44:47], v[12:15]\n" \
"v_mfma_f32_16x16x32_bf16 v[20:23], a[164:167], v[44:47], v[20:23]\n" \
"s_waitcnt lgkmcnt(6)\n" \
"ds_read_b128 v[44:47], v111 offset:35840\n" \
"v_mfma_f32_16x16x32_bf16 v[0:3], a[168:171], v[48:51], v[0:3]\n" \
"v_mfma_f32_16x16x32_bf16 v[8:11], a[172:175], v[48:51], v[8:11]\n" \
"global_load_lds_dwordx4 v103, s[46:47] offset:3072 sc1\n" \
"v_mfma_f32_16x16x32_bf16 v[16:19], a[176:179], v[48:51], v[16:19]\n" \
"s_waitcnt lgkmcnt(6)\n" \
"ds_read_b128 v[48:51], v111 offset:36864\n" \
"v_mfma_f32_16x16x32_bf16 v[4:7], a[168:171], v[52:55], v[4:7]\n" \
"v_mfma_f32_16x16x32_bf16 v[12:15], a[172:175], v[52:55], v[12:15]\n" \
"v_mfma_f32_16x16x32_bf16 v[20:23], a[176:179], v[52:55], v[20:23]\n" \
"s_waitcnt lgkmcnt(6)\n" \
"ds_read_b128 v[52:55], v111 offset:37888\n" \
"v_mfma_f32_16x16x32_bf16 v[0:3], a[180:183], v[56:59], v[0:3]\n" \
"v_mfma_f32_16x16x32_bf16 v[8:11], a[184:187], v[56:59], v[8:11]\n" \
"v_mfma_f32_16x16x32_bf16 v[16:19], a[188:191], v[56:59], v[16:19]\n" \
"s_waitcnt lgkmcnt(6)\n" \
"ds_read_b128 v[56:59], v111 offset:38912\n" \
"v_mfma_f32_16x16x32_bf16 v[4:7], a[180:183], v[60:63], v[4:7]\n" \
"s_add_u32 m0, s60, 8192\n" \
"v_mfma_f32_16x16x32_bf16 v[12:15], a[184:187], v[60:63], v[12:15]\n" \
"global_load_lds_dwordx4 v104, s[46:47] offset:0 sc1\n" \
"v_mfma_f32_16x16x32_bf16 v[20:23], a[188:191], v[60:63], v[20:23]\n" \
"s_waitcnt lgkmcnt(6)\n" \
"ds_read_b128 v[60:63], v111 offset:39936\n" \
"v_mfma_f32_16x16x32_bf16 v[0:3], a[192:195], v[32:35], v[0:3]\n" \
"v_mfma_f32_16x16x32_bf16 v[8:11], a[196:199], v[32:35], v[8:11]\n" \
"v_mfma_f32_16x16x32_bf16 v[16:19], a[200:203], v[32:35], v[16:19]\n" \
"s_waitcnt lgkmcnt(6)\n" \
"ds_read_b128 v[32:35], v111 offset:40960\n" \
"v_mfma_f32_16x16x32_bf16 v[4:7], a[192:195], v[36:39], v[4:7]\n" \
"v_mfma_f32_16x16x32_bf16 v[12:15], a[196:199], v[36:39], v[12:15]\n" \
"v_mfma_f32_16x16x32_bf16 v[20:23], a[200:203], v[36:39], v[20:23]\n" \
"s_waitcnt lgkmcnt(6)\n" \
"ds_read_b128 v[36:39], v111 offset:41984\n" \
"v_mfma_f32_16x16x32_bf16 v[0:3], a[204:207], v[40:43], v[0:3]\n" \
"v_mfma_f32_16x16x32_bf16 v[8:11], a[208:211], v[40:43], v[8:11]\n" \
"global_load_lds_dwordx4 v104, s[46:47] offset:1024 sc1\n" \
"v_mfma_f32_16x16x32_bf16 v[16:19], a[212:215], v[40:43], v[16:19]\n" \
"s_waitcnt lgkmcnt(6)\n" \
"ds_read_b128 v[40:43], v111 offset:43008\n" \
"v_mfma_f32_16x16x32_bf16 v[4:7], a[204:207], v[44:47], v[4:7]\n" \
"v_mfma_f32_16x16x32_bf16 v[12:15], a[208:211], v[44:47], v[12:15]\n" \
"v_mfma_f32_16x16x32_bf16 v[20:23], a[212:215], v[44:47], v[20:23]\n" \
"s_waitcnt lgkmcnt(6)\n" \
"ds_read_b128 v[44:47], v111 offset:44032\n" \
"v_mfma_f32_16x16x32_bf16 v[0:3], a[216:219], v[48:51], v[0:3]\n" \
"v_mfma_f32_16x16x32_bf16 v[8:11], a[220:223], v[48:51], v[8:11]\n" \
"v_mfma_f32_16x16x32_bf16 v[16:19], a[224:227], v[48:51], v[16:19]\n" \
"s_waitcnt lgkmcnt(6)\n" \
"ds_read_b128 v[48:51], v111 offset:45056\n" \
"v_mfma_f32_16x16x32_bf16 v[4:7], a[216:219], v[52:55], v[4:7]\n" \
"v_mov_b32 v149, s58\n" \
"v_mfma_f32_16x16x32_bf16 v[12:15], a[220:223], v[52:55], v[12:15]\n" \
"global_load_lds_dwordx4 v104, s[46:47] offset:2048 sc1\n" \
"v_mfma_f32_16x16x32_bf16 v[20:23], a[224:227], v[52:55], v[20:23]\n" \
"s_waitcnt lgkmcnt(6)\n" \
"ds_read_b128 v[52:55], v111 offset:46080\n" \
"v_mfma_f32_16x16x32_bf16 v[0:3], a[228:231], v[56:59], v[0:3]\n" \
"s_waitcnt vmcnt(11)\n" \
"s_barrier\n" \
"v_mfma_f32_16x16x32_bf16 v[8:11], a[232:235], v[56:59], v[8:11]\n" \
"s_mov_b64 exec, 1\n" \
"global_store_dword v107, v149, s[52:53] sc1\n" \
"s_mov_b64 exec, -1\n" \
"v_mfma_f32_16x16x32_bf16 v[16:19], a[236:239], v[56:59], v[16:19]\n" \
"s_waitcnt lgkmcnt(6)\n" \
"ds_read_b128 v[56:59], v111 offset:47104\n" \
"v_mfma_f32_16x16x32_bf16 v[4:7], a[228:231], v[60:63], v[4:7]\n" \
"s_add_u32 s58, s58, 1\n" \
"v_mfma_f32_16x16x32_bf16 v[12:15], a[232:235], v[60:63], v[12:15]\n" \
"v_mfma_f32_16x16x32_bf16 v[20:23], a[236:239], v[60:63], v[20:23]\n" \
"s_waitcnt lgkmcnt(6)\n" \
"ds_read_b128 v[60:63], v111 offset:48128\n" \
"v_mfma_f32_16x16x32_bf16 v[0:3], a[240:243], v[32:35], v[0:3]\n" \
"v_add_u32 v111, s62, v101\n" \
"v_mfma_f32_16x16x32_bf16 v[8:11], a[244:247], v[32:35], v[8:11]\n" \
"global_load_lds_dwordx4 v104, s[46:47] offset:3072 sc1\n" \
"v_mfma_f32_16x16x32_bf16 v[16:19], a[248:251], v[32:35], v[16:19]\n" \
"s_waitcnt lgkmcnt(6)\n" \
"ds_read_b128 v[32:35], v111 offset:0\n" \
"v_mfma_f32_16x16x32_bf16 v[4:7], a[240:243], v[36:39], v[4:7]\n" \
"s_add_u32 s46, s46, 294912\n" \
"s_addc_u32 s47, s47, 0\n" \
"v_mfma_f32_16x16x32_bf16 v[12:15], a[244:247], v[36:39], v[12:15]\n" \
"s_add_u32 s60, s60, 49152\n" \
"v_mfma_f32_16x16x32_bf16 v[20:23], a[248:251], v[36:39], v[20:23]\n" \
"s_waitcnt lgkmcnt(6)\n" \
"ds_read_b128 v[36:39], v111 offset:1024\n" \
"v_mfma_f32_16x16x32_bf16 v[0:3], a[252:255], v[40:43], v[0:3]\n" \
"s_cmp_eq_u32 s60, s61\n" \
"s_cselect_b32 s60, s57, s60\n" \
"v_mfma_f32_16x16x32_bf16 v[8:11], v[224:227], v[40:43], v[8:11]\n" \
"v_mfma_f32_16x16x32_bf16 v[16:19], v[228:231], v[40:43], v[16:19]\n" \
"s_waitcnt lgkmcnt(6)\n" \
"ds_read_b128 v[40:43], v111 offset:2048\n" \
"v_mfma_f32_16x16x32_bf16 v[4:7], a[252:255], v[44:47], v[4:7]\n" \
"v_mfma_f32_16x16x32_bf16 v[12:15], v[224:227], v[44:47], v[12:15]\n" \
"v_mfma_f32_16x16x32_bf16 v[20:23], v[228:231], v[44:47], v[20:23]\n" \
"s_waitcnt lgkmcnt(6)\n" \
"ds_read_b128 v[44:47], v111 offset:3072\n" \
"v_mfma_f32_16x16x32_bf16 v[0:3], v[232:235], v[48:51], v[0:3]\n" \
"v_mfma_f32_16x16x32_bf16 v[8:11], v[236:239], v[48:51], v[8:11]\n" \
"v_mfma_f32_16x16x32_bf16 v[16:19], v[240:243], v[48:51], v[16:19]\n" \
"s_waitcnt lgkmcnt(6)\n" \
"ds_read_b128 v[48:51], v111 offset:4096\n" \
"v_mfma_f32_16x16x32_bf16 v[4:7], v[232:235], v[52:55], v[4:7]\n" \
"v_mfma_f32_16x16x32_bf16 v[12:15], v[236:239], v[52:55], v[12:15]\n" \
"v_mfma_f32_16x16x32_bf16 v[20:23], v[240:243], v[52:55], v[20:23]\n" \
"s_waitcnt lgkmcnt(6)\n" \
"ds_read_b128 v[52:55], v111 offset:5120\n" \
"v_mfma_f32_16x16x32_bf16 v[0:3], v[244:247], v[56:59], v[0:3]\n" \
"v_mfma_f32_16x16x32_bf16 v[8:11], v[248:251], v[56:59], v[8:11]\n" \
"v_mfma_f32_16x16x32_bf16 v[16:19], v[252:255], v[56:59], v[16:19]\n" \
"s_waitcnt lgkmcnt(6)\n" \
"ds_read_b128 v[56:59], v111 offset:6144\n" \
"v_mfma_f32_16x16x32_bf16 v[4:7], v[244:247], v[60:63], v[4:7]\n" \
"v_mfma_f32_16x16x32_bf16 v[12:15], v[248:251], v[60:63], v[12:15]\n" \
"v_mfma_f32_16x16x32_bf16 v[20:23], v[252:255], v[60:63], v[20:23]\n" \
"s_waitcnt lgkmcnt(6)\n" \
"ds_read_b128 v[60:63], v111 offset:7168\n" \
"s_branch .Ltop%=\n" \
".Lfin%=:\n" \
"s_nop 15\n" \
"v_cvt_pk_bf16_f32 v64, v112, v113\n" \
"v_cvt_pk_bf16_f32 v65, v114, v115\n" \
"v_cvt_pk_bf16_f32 v66, v116, v117\n" \
"v_cvt_pk_bf16_f32 v67, v118, v119\n" \
"global_store_dwordx4 v101, v[64:67], s[48:49] offset:0 sc1\n" \
"v_cvt_pk_bf16_f32 v68, v120, v121\n" \
"v_cvt_pk_bf16_f32 v69, v122, v123\n" \
"v_cvt_pk_bf16_f32 v70, v124, v125\n" \
"v_cvt_pk_bf16_f32 v71, v126, v127\n" \
"global_store_dwordx4 v101, v[68:71], s[48:49] offset:1024 sc1\n" \
"v_cvt_pk_bf16_f32 v72, v128, v129\n" \
"v_cvt_pk_bf16_f32 v73, v130, v131\n" \
"v_cvt_pk_bf16_f32 v74, v132, v133\n" \
"v_cvt_pk_bf16_f32 v75, v134, v135\n" \
"global_store_dwordx4 v101, v[72:75], s[48:49] offset:2048 sc1\n" \
"s_waitcnt vmcnt(0)\n" \
"v_mov_b32 v80, 64\n" \
"s_mov_b64 exec, 1\n" \
"global_store_dword v107, v80, s[52:53] sc1\n" \
"s_mov_b64 exec, -1\n" \
".Lend%=:\n" \
"s_endpgm\n" \
".Lhb_s%=:\n" \
"s_sub_u32 s73, s73, 1\n" \
"s_cbranch_scc1 .Lend%=\n" \
"global_load_dword v152, v150, s[54:55] sc1\n" \
"s_waitcnt vmcnt(0)\n" \
"s_branch .Lhb_c%=\n" \
".Lh0_s%=:\n" \
"s_sub_u32 s73, s73, 1\n" \
"s_cbranch_scc1 .Lend%=\n" \
"s_sleep 4\n" \
"global_load_dword v99, v105, s[54:55] sc1\n" \
"s_waitcnt vmcnt(0)\n" \
"s_branch .Lh0_c%=\n" \
".Lh1_s%=:\n" \
"s_sub_u32 s73, s73, 1\n" \
"s_cbranch_scc1 .Lend%=\n" \
"s_sleep 4\n" \
"global_load_dword v99, v105, s[54:55] sc1\n" \
"s_waitcnt vmcnt(0)\n" \
"s_branch .Lh1_c%=\n" \
".Lhc1_s%=:\n" \
"s_sub_u32 s73, s73, 1\n" \
"s_cbranch_scc1 .Lend%=\n" \
"global_load_dword v99, v105, s[54:55] sc1\n" \
"s_waitcnt vmcnt(0)\n" \
"v_cmp_gt_u32 vcc, s64, v99\n" \
"s_nop 4\n" \
"s_cbranch_vccz .Lhc1_c%=\n" \
"s_sleep 4\n" \
"s_branch .Lhc1_s%=\n" \
".Lhc2_s%=:\n" \
"s_sub_u32 s73, s73, 1\n" \
"s_cbranch_scc1 .Lend%=\n" \
"global_load_dword v99, v105, s[54:55] sc1\n" \
"s_waitcnt vmcnt(0)\n" \
"v_cmp_gt_u32 vcc, s64, v99\n" \
"s_nop 4\n" \
"s_cbranch_vccz .Lhc2_c%=\n" \
"s_sleep 4\n" \
"s_branch .Lhc2_s%=\n" \
".Lhc3_s%=:\n" \
"s_sub_u32 s73, s73, 1\n" \
"s_cbranch_scc1 .Lend%=\n" \
"global_load_dword v99, v105, s[54:55] sc1\n" \
"s_waitcnt vmcnt(0)\n" \
"v_cmp_gt_u32 vcc, s64, v99\n" \
"s_nop 4\n" \
"s_cbranch_vccz .Lhc3_c%=\n" \
"s_sleep 4\n" \
"s_branch .Lhc3_s%=\n"

#define ATT_ASM \
"s_mov_b32 s73, 2000000\n" \
"s_mov_b32 s75, 0x3e0293ee\n" \
"v_mov_b32 v212, v200\n" \
"v_add_u32 v213, 0x10000, v200\n" \
"v_add_u32 v214, 0x20000, v200\n" \
"v_mov_b32 v150, s65\n" \
"global_load_dword v151, v150, s[54:55] sc1\n" \
"s_memrealtime s[76:77]\n" \
"s_waitcnt lgkmcnt(0)\n" \
".Ldly%=:\n" \
"s_memrealtime s[78:79]\n" \
"s_waitcnt lgkmcnt(0)\n" \
"s_sub_u32 s78, s78, s76\n" \
"s_cmp_lt_u32 s78, s64\n" \
"s_cbranch_scc1 .Ldly%=\n" \
"s_add_u32 m0, s57, 0\n" \
"s_nop 0\n" \
"global_load_lds_dwordx4 v202, s[40:41] offset:0\n" \
"global_load_lds_dwordx4 v202, s[40:41] offset:1024\n" \
"global_load_lds_dwordx4 v202, s[40:41] offset:2048\n" \
"global_load_lds_dwordx4 v202, s[40:41] offset:3072\n" \
"s_add_u32 m0, s57, 4096\n" \
"s_nop 0\n" \
"global_load_lds_dwordx4 v203, s[40:41] offset:0\n" \
"global_load_lds_dwordx4 v203, s[40:41] offset:1024\n" \
"global_load_lds_dwordx4 v203, s[40:41] offset:2048\n" \
"global_load_lds_dwordx4 v203, s[40:41] offset:3072\n" \
"s_add_u32 m0, s57, 8192\n" \
"s_nop 0\n" \
"global_load_lds_dwordx4 v204, s[40:41] offset:0\n" \
"s_add_u32 s40, s40, 36864\n" \
"s_addc_u32 s41, s41, 0\n" \
"s_add_u32 m0, s57, 36864\n" \
"s_nop 0\n" \
"global_load_lds_dwordx4 v202, s[40:41] offset:0\n" \
"global_load_lds_dwordx4 v202, s[40:41] offset:1024\n" \
"global_load_lds_dwordx4 v202, s[40:41] offset:2048\n" \
"global_load_lds_dwordx4 v202, s[40:41] offset:3072\n" \
"s_add_u32 m0, s57, 40960\n" \
"s_nop 0\n" \
"global_load_lds_dwordx4 v203, s[40:41] offset:0\n" \
"global_load_lds_dwordx4 v203, s[40:41] offset:1024\n" \
"global_load_lds_dwordx4 v203, s[40:41] offset:2048\n" \
"global_load_lds_dwordx4 v203, s[40:41] offset:3072\n" \
"s_add_u32 m0, s57, 45056\n" \
"s_nop 0\n" \
"global_load_lds_dwordx4 v204, s[40:41] offset:0\n" \
"s_add_u32 s40, s40, 36864\n" \
"s_addc_u32 s41, s41, 0\n" \
"s_add_u32 m0, s57, 73728\n" \
"s_nop 0\n" \
"global_load_lds_dwordx4 v202, s[40:41] offset:0\n" \
"global_load_lds_dwordx4 v202, s[40:41] offset:1024\n" \
"global_load_lds_dwordx4 v202, s[40:41] offset:2048\n" \
"global_load_lds_dwordx4 v202, s[40:41] offset:3072\n" \
"s_add_u32 m0, s57, 77824\n" \
"s_nop 0\n" \
"global_load_lds_dwordx4 v203, s[40:41] offset:0\n" \
"global_load_lds_dwordx4 v203, s[40:41] offset:1024\n" \
"global_load_lds_dwordx4 v203, s[40:41] offset:2048\n" \
"global_load_lds_dwordx4 v203, s[40:41] offset:3072\n" \
"s_add_u32 m0, s57, 81920\n" \
"s_nop 0\n" \
"global_load_lds_dwordx4 v204, s[40:41] offset:0\n" \
"s_add_u32 s40, s40, 36864\n" \
"s_addc_u32 s41, s41, 0\n" \
"s_add_u32 m0, s57, 110592\n" \
"s_nop 0\n" \
"global_load_lds_dwordx4 v202, s[40:41] offset:0\n" \
"global_load_lds_dwordx4 v202, s[40:41] offset:1024\n" \
"global_load_lds_dwordx4 v202, s[40:41] offset:2048\n" \
"global_load_lds_dwordx4 v202, s[40:41] offset:3072\n" \
"s_add_u32 m0, s57, 114688\n" \
"s_nop 0\n" \
"global_load_lds_dwordx4 v203, s[40:41] offset:0\n" \
"global_load_lds_dwordx4 v203, s[40:41] offset:1024\n" \
"global_load_lds_dwordx4 v203, s[40:41] offset:2048\n" \
"global_load_lds_dwordx4 v203, s[40:41] offset:3072\n" \
"s_add_u32 m0, s57, 118784\n" \
"s_nop 0\n" \
"global_load_lds_dwordx4 v204, s[40:41] offset:0\n" \
"s_add_u32 s40, s40, 36864\n" \
"s_addc_u32 s41, s41, 0\n" \
"s_waitcnt vmcnt(27)\n" \
"s_barrier\n" \
"ds_read_b64_tr_b16 a[0:1], v212 offset:0\n" \
"ds_read_b64_tr_b16 a[2:3], v212 offset:18432\n" \
"ds_read_b64_tr_b16 a[32:33], v212 offset:32\n" \
"ds_read_b64_tr_b16 a[34:35], v212 offset:18464\n" \
"ds_read_b64_tr_b16 a[64:65], v212 offset:64\n" \
"ds_read_b64_tr_b16 a[66:67], v212 offset:18496\n" \
"ds_read_b64_tr_b16 a[96:97], v212 offset:96\n" \
"ds_read_b64_tr_b16 a[98:99], v212 offset:18528\n" \
"ds_read_b64_tr_b16 a[128:129], v212 offset:128\n" \
"ds_read_b64_tr_b16 a[130:131], v212 offset:18560\n" \
"s_waitcnt lgkmcnt(4)\n" \
"ds_read_b64_tr_b16 a[160:161], v212 offset:160\n" \
"ds_read_b64_tr_b16 a[162:163], v212 offset:18592\n" \
"ds_read_b64_tr_b16 a[192:193], v212 offset:192\n" \
"ds_read_b64_tr_b16 a[194:195], v212 offset:18624\n" \
"ds_read_b64_tr_b16 a[224:225], v212 offset:224\n" \
"ds_read_b64_tr_b16 a[226:227], v212 offset:18656\n" \
"ds_read_b64_tr_b16 v[224:225], v212 offset:256\n" \
"ds_read_b64_tr_b16 v[226:227], v212 offset:18688\n" \
"s_waitcnt lgkmcnt(0)\n" \
"s_waitcnt vmcnt(18)\n" \
"s_barrier\n" \
"s_add_u32 m0, s57, 0\n" \
"s_nop 0\n" \
"global_load_lds_dwordx4 v202, s[40:41] offset:0\n" \
"global_load_lds_dwordx4 v202, s[40:41] offset:1024\n" \
"global_load_lds_dwordx4 v202, s[40:41] offset:2048\n" \
"global_load_lds_dwordx4 v202, s[40:41] offset:3072\n" \
"s_add_u32 m0, s57, 4096\n" \
"s_nop 0\n" \
"global_load_lds_dwordx4 v203, s[40:41] offset:0\n" \
"global_load_lds_dwordx4 v203, s[40:41] offset:1024\n" \
"global_load_lds_dwordx4 v203, s[40:41] offset:2048\n" \
"global_load_lds_dwordx4 v203, s[40:41] offset:3072\n" \
"s_add_u32 m0, s57, 8192\n" \
"s_nop 0\n" \
"global_load_lds_dwordx4 v204, s[40:41] offset:0\n" \
"s_add_u32 s40, s40, 36864\n" \
"s_addc_u32 s41, s41, 0\n" \
"ds_read_b64_tr_b16 a[4:5], v212 offset:36864\n" \
"ds_read_b64_tr_b16 a[6:7], v212 offset:55296\n" \
"ds_read_b64_tr_b16 a[36:37], v212 offset:36896\n" \
"ds_read_b64_tr_b16 a[38:39], v212 offset:55328\n" \
"ds_read_b64_tr_b16 a[68:69], v212 offset:36928\n" \
"ds_read_b64_tr_b16 a[70:71], v212 offset:55360\n" \
"ds_read_b64_tr_b16 a[100:101], v212 offset:36960\n" \
"ds_read_b64_tr_b16 a[102:103], v212 offset:55392\n" \
"ds_read_b64_tr_b16 a[132:133], v212 offset:36992\n" \
"ds_read_b64_tr_b16 a[134:135], v212 offset:55424\n" \
"s_waitcnt lgkmcnt(4)\n" \
"ds_read_b64_tr_b16 a[164:165], v212 offset:37024\n" \
"ds_read_b64_tr_b16 a[166:167], v212 offset:55456\n" \
"ds_read_b64_tr_b16 a[196:197], v212 offset:37056\n" \
"ds_read_b64_tr_b16 a[198:199], v212 offset:55488\n" \
"ds_read_b64_tr_b16 a[228:229], v212 offset:37088\n" \
"ds_read_b64_tr_b16 a[230:231], v212 offset:55520\n" \
"ds_read_b64_tr_b16 v[228:229], v212 offset:37120\n" \
"ds_read_b64_tr_b16 v[230:231], v212 offset:55552\n" \
"s_waitcnt lgkmcnt(0)\n" \
"s_waitcnt vmcnt(18)\n" \
"s_barrier\n" \
"s_add_u32 m0, s57, 36864\n" \
"s_nop 0\n" \
"global_load_lds_dwordx4 v202, s[40:41] offset:0\n" \
"global_load_lds_dwordx4 v202, s[40:41] offset:1024\n" \
"global_load_lds_dwordx4 v202, s[40:41] offset:2048\n" \
"global_load_lds_dwordx4 v202, s[40:41] offset:3072\n" \
"s_add_u32 m0, s57, 40960\n" \
"s_nop 0\n" \
"global_load_lds_dwordx4 v203, s[40:41] offset:0\n" \
"global_load_lds_dwordx4 v203, s[40:41] offset:1024\n" \
"global_load_lds_dwordx4 v203, s[40:41] offset:2048\n" \
"global_load_lds_dwordx4 v203, s[40:41] offset:3072\n" \
"s_add_u32 m0, s57, 45056\n" \
"s_nop 0\n" \
"global_load_lds_dwordx4 v204, s[40:41] offset:0\n" \
"s_add_u32 s40, s40, 36864\n" \
"s_addc_u32 s41, s41, 0\n" \
"ds_read_b64_tr_b16 a[8:9], v213 offset:8192\n" \
"ds_read_b64_tr_b16 a[10:11], v213 offset:26624\n" \
"ds_read_b64_tr_b16 a[40:41], v213 offset:8224\n" \
"ds_read_b64_tr_b16 a[42:43], v213 offset:26656\n" \
"ds_read_b64_tr_b16 a[72:73], v213 offset:8256\n" \
"ds_read_b64_tr_b16 a[74:75], v213 offset:26688\n" \
"ds_read_b64_tr_b16 a[104:105], v213 offset:8288\n" \
"ds_read_b64_tr_b16 a[106:107], v213 offset:26720\n" \
"ds_read_b64_tr_b16 a[136:137], v213 offset:8320\n" \
"ds_read_b64_tr_b16 a[138:139], v213 offset:26752\n" \
"s_waitcnt lgkmcnt(4)\n" \
"ds_read_b64_tr_b16 a[168:169], v213 offset:8352\n" \
"ds_read_b64_tr_b16 a[170:171], v213 offset:26784\n" \
"ds_read_b64_tr_b16 a[200:201], v213 offset:8384\n" \
"ds_read_b64_tr_b16 a[202:203], v213 offset:26816\n" \
"ds_read_b64_tr_b16 a[232:233], v213 offset:8416\n" \
"ds_read_b64_tr_b16 a[234:235], v213 offset:26848\n" \
"ds_read_b64_tr_b16 v[232:233], v213 offset:8448\n" \
"ds_read_b64_tr_b16 v[234:235], v213 offset:26880\n" \
"s_waitcnt lgkmcnt(0)\n" \
"s_waitcnt vmcnt(18)\n" \
"s_barrier\n" \
"global_load_dword v152, v150, s[54:55] sc1\n" \
"s_add_u32 m0, s57, 73728\n" \
"s_nop 0\n" \
"global_load_lds_dwordx4 v202, s[40:41] offset:0\n" \
"global_load_lds_dwordx4 v202, s[40:41] offset:1024\n" \
"global_load_lds_dwordx4 v202, s[40:41] offset:2048\n" \
"global_load_lds_dwordx4 v202, s[40:41] offset:3072\n" \
"s_add_u32 m0, s57, 77824\n" \
"s_nop 0\n" \
"global_load_lds_dwordx4 v203, s[40:41] offset:0\n" \
"global_load_lds_dwordx4 v203, s[40:41] offset:1024\n" \
"global_load_lds_dwordx4 v203, s[40:41] offset:2048\n" \
"global_load_lds_dwordx4 v203, s[40:41] offset:3072\n" \
"s_add_u32 m0, s57, 81920\n" \
"s_nop 0\n" \
"global_load_lds_dwordx4 v204, s[40:41] offset:0\n" \
"s_add_u32 s40, s40, 36864\n" \
"s_addc_u32 s41, s41, 0\n" \
"ds_read_b64_tr_b16 a[12:13], v213 offset:45056\n" \
"ds_read_b64_tr_b16 a[14:15], v213 offset:63488\n" \
"ds_read_b64_tr_b16 a[44:45], v213 offset:45088\n" \
"ds_read_b64_tr_b16 a[46:47], v213 offset:63520\n" \
"ds_read_b64_tr_b16 a[76:77], v213 offset:45120\n" \
"ds_read_b64_tr_b16 a[78:79], v213 offset:63552\n" \
"ds_read_b64_tr_b16 a[108:109], v213 offset:45152\n" \
"ds_read_b64_tr_b16 a[110:111], v213 offset:63584\n" \
"ds_read_b64_tr_b16 a[140:141], v213 offset:45184\n" \
"ds_read_b64_tr_b16 a[142:143], v213 offset:63616\n" \
"s_waitcnt lgkmcnt(4)\n" \
"ds_read_b64_tr_b16 a[172:173], v213 offset:45216\n" \
"ds_read_b64_tr_b16 a[174:175], v213 offset:63648\n" \
"ds_read_b64_tr_b16 a[204:205], v213 offset:45248\n" \
"ds_read_b64_tr_b16 a[206:207], v213 offset:63680\n" \
"ds_read_b64_tr_b16 a[236:237], v213 offset:45280\n" \
"ds_read_b64_tr_b16 a[238:239], v213 offset:63712\n" \
"ds_read_b64_tr_b16 v[236:237], v213 offset:45312\n" \
"ds_read_b64_tr_b16 v[238:239], v213 offset:63744\n" \
"s_waitcnt lgkmcnt(0)\n" \
"s_waitcnt vmcnt(19)\n" \
"s_barrier\n" \
"s_add_u32 m0, s57, 110592\n" \
"s_nop 0\n" \
"global_load_lds_dwordx4 v202, s[40:41] offset:0\n" \
"global_load_lds_dwordx4 v202, s[40:41] offset:1024\n" \
"global_load_lds_dwordx4 v202, s[40:41] offset:2048\n" \
"global_load_lds_dwordx4 v202, s[40:41] offset:3072\n" \
"s_add_u32 m0, s57, 114688\n" \
"s_nop 0\n" \
"global_load_lds_dwordx4 v203, s[40:41] offset:0\n" \
"global_load_lds_dwordx4 v203, s[40:41] offset:1024\n" \
"global_load_lds_dwordx4 v203, s[40:41] offset:2048\n" \
"global_load_lds_dwordx4 v203, s[40:41] offset:3072\n" \
"s_add_u32 m0, s57, 118784\n" \
"s_nop 0\n" \
"global_load_lds_dwordx4 v204, s[40:41] offset:0\n" \
"s_add_u32 s40, s40, 36864\n" \
"s_addc_u32 s41, s41, 0\n" \
"ds_read_b64_tr_b16 a[16:17], v212 offset:0\n" \
"ds_read_b64_tr_b16 a[18:19], v212 offset:18432\n" \
"ds_read_b64_tr_b16 a[48:49], v212 offset:32\n" \
"ds_read_b64_tr_b16 a[50:51], v212 offset:18464\n" \
"ds_read_b64_tr_b16 a[80:81], v212 offset:64\n" \
"ds_read_b64_tr_b16 a[82:83], v212 offset:18496\n" \
"ds_read_b64_tr_b16 a[112:113], v212 offset:96\n" \
"ds_read_b64_tr_b16 a[114:115], v212 offset:18528\n" \
"ds_read_b64_tr_b16 a[144:145], v212 offset:128\n" \
"ds_read_b64_tr_b16 a[146:147], v212 offset:18560\n" \
"s_waitcnt lgkmcnt(4)\n" \
"ds_read_b64_tr_b16 a[176:177], v212 offset:160\n" \
"ds_read_b64_tr_b16 a[178:179], v212 offset:18592\n" \
"ds_read_b64_tr_b16 a[208:209], v212 offset:192\n" \
"ds_read_b64_tr_b16 a[210:211], v212 offset:18624\n" \
"ds_read_b64_tr_b16 a[240:241], v212 offset:224\n" \
"ds_read_b64_tr_b16 a[242:243], v212 offset:18656\n" \
"ds_read_b64_tr_b16 v[240:241], v212 offset:256\n" \
"ds_read_b64_tr_b16 v[242:243], v212 offset:18688\n" \
"s_waitcnt lgkmcnt(0)\n" \
"s_waitcnt vmcnt(19)\n" \
"s_barrier\n" \
"ds_read_b64_tr_b16 a[20:21], v212 offset:36864\n" \
"ds_read_b64_tr_b16 a[22:23], v212 offset:55296\n" \
"ds_read_b64_tr_b16 a[52:53], v212 offset:36896\n" \
"ds_read_b64_tr_b16 a[54:55], v212 offset:55328\n" \
"ds_read_b64_tr_b16 a[84:85], v212 offset:36928\n" \
"ds_read_b64_tr_b16 a[86:87], v212 offset:55360\n" \
"ds_read_b64_tr_b16 a[116:117], v212 offset:36960\n" \
"ds_read_b64_tr_b16 a[118:119], v212 offset:55392\n" \
"ds_read_b64_tr_b16 a[148:149], v212 offset:36992\n" \
"ds_read_b64_tr_b16 a[150:151], v212 offset:55424\n" \
"s_waitcnt lgkmcnt(4)\n" \
"ds_read_b64_tr_b16 a[180:181], v212 offset:37024\n" \
"ds_read_b64_tr_b16 a[182:183], v212 offset:55456\n" \
"ds_read_b64_tr_b16 a[212:213], v212 offset:37056\n" \
"ds_read_b64_tr_b16 a[214:215], v212 offset:55488\n" \
"ds_read_b64_tr_b16 a[244:245], v212 offset:37088\n" \
"ds_read_b64_tr_b16 a[246:247], v212 offset:55520\n" \
"ds_read_b64_tr_b16 v[244:245], v212 offset:37120\n" \
"ds_read_b64_tr_b16 v[246:247], v212 offset:55552\n" \
"s_waitcnt lgkmcnt(0)\n" \
"s_waitcnt vmcnt(9)\n" \
"s_barrier\n" \
".Lhb_c%=:\n" \
"v_cmp_eq_u32 vcc, v151, v152\n" \
"s_nop 4\n" \
"s_cbranch_vccnz .Lhb_s%=\n" \
"ds_read_b64_tr_b16 a[24:25], v213 offset:8192\n" \
"ds_read_b64_tr_b16 a[26:27], v213 offset:26624\n" \
"ds_read_b64_tr_b16 a[56:57], v213 offset:8224\n" \
"ds_read_b64_tr_b16 a[58:59], v213 offset:26656\n" \
"ds_read_b64_tr_b16 a[88:89], v213 offset:8256\n" \
"ds_read_b64_tr_b16 a[90:91], v213 offset:26688\n" \
"ds_read_b64_tr_b16 a[120:121], v213 offset:8288\n" \
"ds_read_b64_tr_b16 a[122:123], v213 offset:26720\n" \
"ds_read_b64_tr_b16 a[152:153], v213 offset:8320\n" \
"ds_read_b64_tr_b16 a[154:155], v213 offset:26752\n" \
"s_waitcnt lgkmcnt(4)\n" \
"ds_read_b64_tr_b16 a[184:185], v213 offset:8352\n" \
"ds_read_b64_tr_b16 a[186:187], v213 offset:26784\n" \
"ds_read_b64_tr_b16 a[216:217], v213 offset:8384\n" \
"ds_read_b64_tr_b16 a[218:219], v213 offset:26816\n" \
"ds_read_b64_tr_b16 a[248:249], v213 offset:8416\n" \
"ds_read_b64_tr_b16 a[250:251], v213 offset:26848\n" \
"ds_read_b64_tr_b16 v[248:249], v213 offset:8448\n" \
"ds_read_b64_tr_b16 v[250:251], v213 offset:26880\n" \
"s_waitcnt lgkmcnt(0)\n" \
"s_waitcnt vmcnt(0)\n" \
"s_barrier\n" \
"ds_read_b64_tr_b16 a[28:29], v213 offset:45056\n" \
"ds_read_b64_tr_b16 a[30:31], v213 offset:63488\n" \
"ds_read_b64_tr_b16 a[60:61], v213 offset:45088\n" \
"ds_read_b64_tr_b16 a[62:63], v213 offset:63520\n" \
"ds_read_b64_tr_b16 a[92:93], v213 offset:45120\n" \
"ds_read_b64_tr_b16 a[94:95], v213 offset:63552\n" \
"ds_read_b64_tr_b16 a[124:125], v213 offset:45152\n" \
"ds_read_b64_tr_b16 a[126:127], v213 offset:63584\n" \
"ds_read_b64_tr_b16 a[156:157], v213 offset:45184\n" \
"ds_read_b64_tr_b16 a[158:159], v213 offset:63616\n" \
"s_waitcnt lgkmcnt(4)\n" \
"ds_read_b64_tr_b16 a[188:189], v213 offset:45216\n" \
"ds_read_b64_tr_b16 a[190:191], v213 offset:63648\n" \
"ds_read_b64_tr_b16 a[220:221], v213 offset:45248\n" \
"ds_read_b64_tr_b16 a[222:223], v213 offset:63680\n" \
"ds_read_b64_tr_b16 a[252:253], v213 offset:45280\n" \
"ds_read_b64_tr_b16 a[254:255], v213 offset:63712\n" \
"ds_read_b64_tr_b16 v[252:253], v213 offset:45312\n" \
"ds_read_b64_tr_b16 v[254:255], v213 offset:63744\n" \
"s_waitcnt lgkmcnt(0)\n" \
"s_barrier\n" \
"v_mov_b32 v214, v201\n" \
"v_add_u32 v215, 4096, v201\n" \
"v_add_u32 v212, 4096, v209\n" \
"v_add_u32 v213, 8192, v209\n" \
"v_add_u32 v200, 12288, v209\n" \
"v_mov_b32 v0, -1\n" \
"v_mov_b32 v1, -1\n" \
"v_mov_b32 v2, -1\n" \
"v_mov_b32 v3, -1\n" \
"s_add_u32 s62, s48, 36569088\n" \
"s_addc_u32 s63, s49, 0\n" \
"s_mov_b32 s61, 0\n" \
"global_store_dwordx4 v201, v[0:3], s[62:63] offset:-4096 sc1\n" \
"global_store_dwordx4 v201, v[0:3], s[62:63] offset:-3072 sc1\n" \
"global_store_dwordx4 v201, v[0:3], s[62:63] offset:-2048 sc1\n" \
"global_store_dwordx4 v201, v[0:3], s[62:63] offset:-1024 sc1\n" \
"global_store_dwordx4 v201, v[0:3], s[62:63] offset:0 sc1\n" \
"global_store_dwordx4 v201, v[0:3], s[62:63] offset:1024 sc1\n" \
"global_store_dwordx4 v201, v[0:3], s[62:63] offset:2048 sc1\n" \
"global_store_dwordx4 v201, v[0:3], s[62:63] offset:3072 sc1\n" \
"global_store_dwordx4 v215, v[0:3], s[62:63] offset:0 sc1\n" \
"s_add_u32 s58, s58, 1\n" \
"v_min_u32 v169, 68, v207\n" \
"v_add_u32 v169, 10240, v169\n" \
"global_load_dword v168, v169, s[54:55] sc1\n" \
"s_waitcnt vmcnt(0)\n" \
".Lrdy_c%=:\n" \
"v_cmp_gt_u32 vcc, 1, v168\n" \
"s_cbranch_vccnz .Lrdy_s%=\n" \
"v_min_u32_dpp v168, v168, v168 row_shr:1 row_mask:0xf bank_mask:0xf\n" \
"s_nop 1\n" \
"v_min_u32_dpp v168, v168, v168 row_shr:2 row_mask:0xf bank_mask:0xf\n" \
"s_nop 1\n" \
"v_min_u32_dpp v168, v168, v168 row_shr:4 row_mask:0xf bank_mask:0xf\n" \
"s_nop 1\n" \
"v_min_u32_dpp v168, v168, v168 row_shr:8 row_mask:0xf bank_mask:0xf\n" \
"s_nop 0\n" \
"v_readlane_b32 s76, v168, 15\n" \
"v_readlane_b32 s77, v168, 31\n" \
"s_min_u32 s76, s76, s77\n" \
"s_and_b32 s76, s76, 1\n" \
"s_lshl_b32 s76, s76, 13\n" \
"s_xor_b32 s42, s42, s76\n" \
"s_xor_b32 s44, s44, s76\n" \
"s_xor_b32 s46, s46, s76\n" \
"s_mov_b64 s[50:51], s[44:45]\n" \
"s_xor_b32 s44, s44, 0x2000\n" \
"global_load_dword v168, v206, s[54:55] sc1\n" \
"s_waitcnt vmcnt(0)\n" \
".Lp0_c%=:\n" \
"v_cmp_gt_u32 vcc, s58, v168\n" \
"s_cbranch_vccnz .Lp0_s%=\n" \
"global_load_dwordx4 v[0:3], v201, s[42:43] offset:-4096 sc1\n" \
"global_load_dwordx4 v[4:7], v201, s[42:43] offset:-2048 sc1\n" \
"global_load_dwordx4 v[8:11], v201, s[42:43] offset:0 sc1\n" \
"global_load_dwordx4 v[12:15], v201, s[42:43] offset:2048 sc1\n" \
"s_add_u32 m0, s56, 0\n" \
"s_nop 0\n" \
"global_load_lds_dwordx4 v201, s[44:45] offset:0 sc1\n" \
"global_load_lds_dwordx4 v201, s[44:45] offset:1024 sc1\n" \
"global_load_lds_dwordx4 v201, s[44:45] offset:2048 sc1\n" \
"global_load_lds_dwordx4 v201, s[44:45] offset:3072 sc1\n" \
"s_add_u32 m0, s56, 4096\n" \
"s_nop 0\n" \
"global_load_lds_dwordx4 v215, s[44:45] offset:0 sc1\n" \
"global_load_lds_dwordx4 v215, s[44:45] offset:1024 sc1\n" \
"global_load_lds_dwordx4 v215, s[44:45] offset:2048 sc1\n" \
"global_load_lds_dwordx4 v215, s[44:45] offset:3072 sc1\n" \
"s_add_u32 s42, s42, 32768\n" \
"s_addc_u32 s43, s43, 0\n" \
"s_add_u32 s44, s44, 65536\n" \
"s_addc_u32 s45, s45, 0\n" \
"s_xor_b32 s50, s44, 0x2000\n" \
"s_mov_b32 s51, s45\n" \
"global_load_dword v203, v205, s[54:55] sc1\n" \
"s_waitcnt vmcnt(0)\n" \
".Lpg_c%=:\n" \
"v_cmp_gt_u32 vcc, 1, v203\n" \
"s_cbranch_vccnz .Lpg_s%=\n" \
"v_mov_b32 v204, 12288\n" \
"s_mov_b32 s94, 0\n" \
".Ltop%=:\n" \
"s_barrier\n" \
"ds_read_b128 v[16:19], v208 offset:0\n" \
"ds_read_b128 v[20:23], v208 offset:1024\n" \
"ds_read_b128 v[24:27], v208 offset:4096\n" \
"ds_read_b128 v[28:31], v208 offset:5120\n" \
"ds_read_b128 v[32:35], v208 offset:8192\n" \
"ds_read_b128 v[36:39], v208 offset:9216\n" \
"ds_read_b128 v[40:43], v208 offset:12288\n" \
"ds_read_b128 v[44:47], v208 offset:13312\n" \
"s_waitcnt lgkmcnt(7)\n" \
"v_mfma_f32_16x16x32_bf16 v[104:107], v[16:19], v[0:3], v[192:195]\n" \
"ds_read2_b64 v[48:51], v209 offset0:0 offset1:32\n" \
"s_waitcnt lgkmcnt(7)\n" \
"v_mfma_f32_16x16x32_bf16 v[108:111], v[20:23], v[0:3], v[196:199]\n" \
"ds_read2_b64 v[52:55], v209 offset0:128 offset1:160\n" \
"s_waitcnt lgkmcnt(7)\n" \
"v_mfma_f32_16x16x32_bf16 v[104:107], v[24:27], v[4:7], v[104:107]\n" \
"ds_read2_b64 v[56:59], v212 offset0:0 offset1:32\n" \
"s_waitcnt lgkmcnt(7)\n" \
"v_mfma_f32_16x16x32_bf16 v[108:111], v[28:31], v[4:7], v[108:111]\n" \
"ds_read2_b64 v[60:63], v212 offset0:128 offset1:160\n" \
"s_waitcnt lgkmcnt(7)\n" \
"v_mfma_f32_16x16x32_bf16 v[104:107], v[32:35], v[8:11], v[104:107]\n" \
"ds_read2_b64 v[64:67], v213 offset0:0 offset1:32\n" \
"s_waitcnt lgkmcnt(7)\n" \
"v_mfma_f32_16x16x32_bf16 v[108:111], v[36:39], v[8:11], v[108:111]\n" \
"ds_read2_b64 v[68:71], v213 offset0:128 offset1:160\n" \
"s_waitcnt lgkmcnt(7)\n" \
"v_mfma_f32_16x16x32_bf16 v[104:107], v[40:43], v[12:15], v[104:107]\n" \
"ds_read2_b64 v[72:75], v200 offset0:0 offset1:32\n" \
"s_waitcnt lgkmcnt(7)\n" \
"v_mfma_f32_16x16x32_bf16 v[108:111], v[44:47], v[12:15], v[108:111]\n" \
"ds_read2_b64 v[76:79], v200 offset0:128 offset1:160\n" \
"s_nop 3\n" \
"v_max3_f32 v168, v104, v105, v106\n" \
"s_nop 1\n" \
"v_max3_f32 v168, v168, v107, v108\n" \
"v_max3_f32 v168, v168, v109, v110\n" \
"v_max_f32 v168, v168, v111\n" \
"v_mov_b32 v173, 0\n" \
"v_mov_b32 v174, 0\n" \
"s_cmp_eq_u32 s94, 0\n" \
"s_cbranch_scc0 .Lkm_p%=\n" \
"v_readfirstlane_b32 s69, v203\n" \
"s_cmp_eq_u32 s58, 64\n" \
"s_cselect_b32 s74, s68, s67\n" \
"s_and_b32 s72, s69, 0xffff\n" \
"s_sub_i32 s72, s58, s72\n" \
"s_add_i32 s72, s72, 1\n" \
"s_cmp_lt_i32 s72, 0\n" \
"s_cselect_b32 s91, 0, 0x4000000\n" \
"s_mul_i32 s72, s72, s66\n" \
"s_add_u32 s69, s69, s74\n" \
"s_add_u32 s69, s69, s72\n" \
"s_memrealtime s[92:93]\n" \
"s_waitcnt lgkmcnt(0)\n" \
"s_lshl_b32 s92, s92, 16\n" \
"s_sub_i32 s92, s69, s92\n" \
"s_cmp_ge_u32 s92, s91\n" \
"s_cbranch_scc1 .Lkm_l%=\n" \
"s_cmp_lt_u32 s92, 3276800\n" \
"s_cbranch_scc1 .Lkm_w%=\n" \
"s_waitcnt vmcnt(0)\n" \
"v_mov_b32 v169, s61\n" \
"s_mov_b64 exec, 1\n" \
"global_store_dword v207, v169, s[52:53] sc1\n" \
"global_store_dword v204, v203, s[52:53] sc1\n" \
"s_mov_b64 exec, -1\n" \
"s_mov_b32 s61, 0\n" \
".Lkm_w%=:\n" \
"s_memrealtime s[92:93]\n" \
"s_waitcnt lgkmcnt(0)\n" \
"s_lshl_b32 s92, s92, 16\n" \
"s_sub_i32 s92, s69, s92\n" \
"s_cmp_ge_u32 s92, s91\n" \
"s_cbranch_scc0 .Lkm_w%=\n" \
".Lkm_l%=:\n" \
"global_load_dwordx4 v[176:179], v210, s[46:47] offset:-4096 sc1\n" \
"global_load_dwordx4 v[180:183], v210, s[46:47] offset:-2048 sc1\n" \
"global_load_dwordx4 v[184:187], v210, s[46:47] offset:0 sc1\n" \
"global_load_dwordx4 v[188:191], v210, s[46:47] offset:2048 sc1\n" \
"global_load_dword v216, v207, s[46:47] offset:-3072 sc1\n" \
"global_load_dword v217, v207, s[46:47] offset:-2816 sc1\n" \
"global_load_dword v218, v207, s[46:47] offset:-1024 sc1\n" \
"global_load_dword v219, v207, s[46:47] offset:-768 sc1\n" \
"global_load_dword v220, v207, s[46:47] offset:1024 sc1\n" \
"global_load_dword v221, v207, s[46:47] offset:1280 sc1\n" \
"global_load_dword v222, v207, s[46:47] offset:3072 sc1\n" \
"global_load_dword v223, v207, s[46:47] offset:3328 sc1\n" \
"global_load_dword v202, v206, s[54:55] sc1\n" \
"global_load_dword v203, v205, s[54:55] sc1\n" \
"s_cmp_eq_u32 s61, 0\n" \
"s_cbranch_scc0 .Lkm_u%=\n" \
"s_waitcnt vmcnt(2)\n" \
".Lkm_c%=:\n" \
"v_mfma_f32_16x16x32_bf16 v[112:115], v[176:179], v[0:3], 0\n" \
"v_max_u32 v169, v176, v177\n" \
"v_max3_u32 v169, v169, v178, v179\n" \
"v_max3_u32 v169, v169, v180, v181\n" \
"v_mfma_f32_16x16x32_bf16 v[112:115], v[180:183], v[4:7], v[112:115]\n" \
"v_max3_u32 v169, v169, v182, v183\n" \
"v_max3_u32 v169, v169, v184, v185\n" \
"v_max3_u32 v169, v169, v186, v187\n" \
"v_mfma_f32_16x16x32_bf16 v[112:115], v[184:187], v[8:11], v[112:115]\n" \
"v_max3_u32 v169, v169, v188, v189\n" \
"v_max3_u32 v169, v169, v190, v191\n" \
"v_max3_u32 v169, v169, v216, v217\n" \
"v_mfma_f32_16x16x32_bf16 v[112:115], v[188:191], v[12:15], v[112:115]\n" \
"v_max3_u32 v169, v169, v218, v219\n" \
"v_max3_u32 v169, v169, v220, v221\n" \
"v_max3_u32 v169, v169, v222, v223\n" \
"v_cmp_eq_u32 vcc, -1, v169\n" \
"s_nop 4\n" \
"s_cbranch_vccnz .Lkm_s%=\n" \
"s_add_u32 s46, s46, 32768\n" \
"s_addc_u32 s47, s47, 0\n" \
"v_max3_f32 v168, v168, v112, v113\n" \
"v_mov_b32 v169, v168\n" \
"s_nop 1\n" \
"v_permlane16_swap_b32 v168, v169\n" \
"v_max_f32 v170, v168, v169\n" \
"v_max_f32 v168, v168, v169\n" \
"s_nop 1\n" \
"v_permlane32_swap_b32 v168, v170\n" \
"v_max_f32 v168, v168, v170\n" \
"v_mul_f32 v168, s75, v168\n" \
"v_fma_f32 v104, v104, s75, -v168\n" \
"v_fma_f32 v105, v105, s75, -v168\n" \
"v_fma_f32 v106, v106, s75, -v168\n" \
"v_fma_f32 v107, v107, s75, -v168\n" \
"v_fma_f32 v108, v108, s75, -v168\n" \
"v_fma_f32 v109, v109, s75, -v168\n" \
"v_fma_f32 v110, v110, s75, -v168\n" \
"v_fma_f32 v111, v111, s75, -v168\n" \
"v_fma_f32 v112, v112, s75, -v168\n" \
"v_fma_f32 v113, v113, s75, -v168\n" \
"v_exp_f32 v104, v104\n" \
"v_exp_f32 v105, v105\n" \
"v_exp_f32 v106, v106\n" \
"v_exp_f32 v107, v107\n" \
"v_exp_f32 v108, v108\n" \
"v_exp_f32 v109, v109\n" \
"v_exp_f32 v110, v110\n" \
"v_exp_f32 v111, v111\n" \
"v_exp_f32 v112, v112\n" \
"v_exp_f32 v113, v113\n" \
"v_add_f32 v168, v104, v105\n" \
"v_add_f32 v168, v168, v106\n" \
"v_add_f32 v168, v168, v107\n" \
"v_add_f32 v168, v168, v108\n" \
"v_add_f32 v168, v168, v109\n" \
"v_add_f32 v168, v168, v110\n" \
"v_add_f32 v168, v168, v111\n" \
"v_add_f32 v168, v168, v112\n" \
"v_add_f32 v168, v168, v113\n" \
"v_mov_b32 v169, v168\n" \
"s_nop 1\n" \
"v_permlane16_swap_b32 v168, v169\n" \
"v_add_f32 v170, v168, v169\n" \
"v_add_f32 v168, v168, v169\n" \
"s_nop 1\n" \
"v_permlane32_swap_b32 v168, v170\n" \
"v_add_f32 v168, v168, v170\n" \
"v_rcp_f32 v168, v168\n" \
"s_nop 0\n" \
"v_mul_f32 v104, v168, v104\n" \
"v_mul_f32 v105, v168, v105\n" \
"v_mul_f32 v106, v168, v106\n" \
"v_mul_f32 v107, v168, v107\n" \
"v_mul_f32 v108, v168, v108\n" \
"v_mul_f32 v109, v168, v109\n" \
"v_mul_f32 v110, v168, v110\n" \
"v_mul_f32 v111, v168, v111\n" \
"v_mul_f32 v112, v168, v112\n" \
"v_mul_f32 v113, v168, v113\n" \
"v_cvt_pk_bf16_f32 v116, v104, v105\n" \
"v_cvt_pk_bf16_f32 v117, v108, v109\n" \
"v_cvt_pk_bf16_f32 v118, v106, v107\n" \
"v_cvt_pk_bf16_f32 v119, v110, v111\n" \
"v_cvt_pk_bf16_f32 v172, v112, v113\n" \
"v_cvt_pk_bf16_f32 v175, v112, v113\n" \
"v_mfma_f32_16x16x32_bf16 v[120:123], v[48:51], v[116:119], 0\n" \
"v_mfma_f32_16x16x32_bf16 v[124:127], v[52:55], v[116:119], 0\n" \
"v_mfma_f32_16x16x16_bf16 v[120:123], v[216:217], v[172:173], v[120:123]\n" \
"v_mfma_f32_16x16x16_bf16 v[124:127], v[216:217], v[174:175], v[124:127]\n" \
"v_mfma_f32_16x16x32_bf16 v[128:131], v[56:59], v[116:119], 0\n" \
"v_mfma_f32_16x16x32_bf16 v[132:135], v[60:63], v[116:119], 0\n" \
"v_mfma_f32_16x16x16_bf16 v[128:131], v[218:219], v[172:173], v[128:131]\n" \
"v_mfma_f32_16x16x16_bf16 v[132:135], v[218:219], v[174:175], v[132:135]\n" \
"s_nop 2\n" \
"v_cvt_pk_bf16_f32 v120, v120, v121\n" \
"v_cvt_pk_bf16_f32 v121, v122, v123\n" \
"v_cvt_pk_bf16_f32 v122, v124, v125\n" \
"v_cvt_pk_bf16_f32 v123, v126, v127\n" \
"ds_write_b128 v211, v[120:123] offset:0\n" \
"v_mfma_f32_16x16x32_bf16 v[136:139], v[64:67], v[116:119], 0\n" \
"v_mfma_f32_16x16x32_bf16 v[140:143], v[68:71], v[116:119], 0\n" \
"v_mfma_f32_16x16x16_bf16 v[136:139], v[220:221], v[172:173], v[136:139]\n" \
"v_mfma_f32_16x16x16_bf16 v[140:143], v[220:221], v[174:175], v[140:143]\n" \
"v_cvt_pk_bf16_f32 v128, v128, v129\n" \
"v_cvt_pk_bf16_f32 v129, v130, v131\n" \
"v_cvt_pk_bf16_f32 v130, v132, v133\n" \
"v_cvt_pk_bf16_f32 v131, v134, v135\n" \
"ds_write_b128 v211, v[128:131] offset:2048\n" \
"v_mfma_f32_16x16x32_bf16 v[144:147], v[72:75], v[116:119], 0\n" \
"v_mfma_f32_16x16x32_bf16 v[148:151], v[76:79], v[116:119], 0\n" \
"v_mfma_f32_16x16x16_bf16 v[144:147], v[222:223], v[172:173], v[144:147]\n" \
"v_mfma_f32_16x16x16_bf16 v[148:151], v[222:223], v[174:175], v[148:151]\n" \
"v_cvt_pk_bf16_f32 v136, v136, v137\n" \
"v_cvt_pk_bf16_f32 v137, v138, v139\n" \
"v_cvt_pk_bf16_f32 v138, v140, v141\n" \
"v_cvt_pk_bf16_f32 v139, v142, v143\n" \
"ds_write_b128 v211, v[136:139] offset:4096\n" \
"s_nop 1\n" \
"v_cvt_pk_bf16_f32 v144, v144, v145\n" \
"v_cvt_pk_bf16_f32 v145, v146, v147\n" \
"v_cvt_pk_bf16_f32 v146, v148, v149\n" \
"v_cvt_pk_bf16_f32 v147, v150, v151\n" \
"ds_write_b128 v211, v[144:147] offset:6144\n" \
"s_add_u32 s59, s58, 2\n" \
"s_cmp_gt_u32 s59, 63\n" \
"s_cselect_b32 s60, 0, s59\n" \
"s_cselect_b64 s[64:65], 0, -1\n" \
"s_waitcnt lgkmcnt(0)\n" \
"s_barrier\n" \
"ds_read_b128 v[80:83], v214 offset:0\n" \
"ds_read_b128 v[84:87], v214 offset:1024\n" \
"ds_read_b128 v[88:91], v214 offset:2048\n" \
"ds_read_b128 v[92:95], v214 offset:3072\n" \
"ds_read_b128 v[96:99], v214 offset:4096\n" \
"ds_read_b128 v[100:103], v214 offset:5120\n" \
"ds_read_b128 v[104:107], v214 offset:6144\n" \
"ds_read_b128 v[108:111], v214 offset:7168\n" \
"ds_read_b128 v[112:115], v214 offset:8192\n" \
"ds_read_b128 v[116:119], v214 offset:9216\n" \
"ds_read_b128 v[120:123], v214 offset:10240\n" \
"ds_read_b128 v[124:127], v214 offset:11264\n" \
"ds_read_b128 v[128:131], v214 offset:12288\n" \
"ds_read_b128 v[132:135], v214 offset:13312\n" \
"ds_read_b128 v[136:139], v214 offset:14336\n" \
"s_waitcnt lgkmcnt(14)\n" \
"ds_read_b128 v[140:143], v214 offset:15360\n" \
"v_xor_b32 v211, 0x4000, v211\n" \
"v_xor_b32 v214, 0x4000, v214\n" \
"s_waitcnt vmcnt(0)\n" \
".Lp1_c%=:\n" \
"v_cmp_gt_u32 vcc, s60, v202\n" \
"s_cbranch_vccnz .Lp1_s%=\n" \
"s_waitcnt lgkmcnt(15)\n" \
"v_mfma_f32_16x16x32_bf16 v[144:147], a[0:3], v[80:83], 0\n" \
"s_mov_b64 exec, s[64:65]\n" \
"global_load_dwordx4 v[0:3], v201, s[42:43] offset:-4096 sc1\n" \
"s_mov_b64 exec, -1\n" \
"s_waitcnt lgkmcnt(14)\n" \
"v_mfma_f32_16x16x32_bf16 v[148:151], a[0:3], v[84:87], 0\n" \
"s_waitcnt lgkmcnt(13)\n" \
"v_mfma_f32_16x16x32_bf16 v[144:147], a[4:7], v[88:91], v[144:147]\n" \
"s_waitcnt lgkmcnt(12)\n" \
"v_mfma_f32_16x16x32_bf16 v[148:151], a[4:7], v[92:95], v[148:151]\n" \
"s_waitcnt lgkmcnt(11)\n" \
"v_mfma_f32_16x16x32_bf16 v[144:147], a[8:11], v[96:99], v[144:147]\n" \
"s_waitcnt lgkmcnt(10)\n" \
"v_mfma_f32_16x16x32_bf16 v[148:151], a[8:11], v[100:103], v[148:151]\n" \
"s_waitcnt lgkmcnt(9)\n" \
"v_mfma_f32_16x16x32_bf16 v[144:147], a[12:15], v[104:107], v[144:147]\n" \
"s_waitcnt lgkmcnt(8)\n" \
"v_mfma_f32_16x16x32_bf16 v[148:151], a[12:15], v[108:111], v[148:151]\n" \
"s_waitcnt lgkmcnt(7)\n" \
"v_mfma_f32_16x16x32_bf16 v[144:147], a[16:19], v[112:115], v[144:147]\n" \
"s_mov_b64 exec, s[64:65]\n" \
"global_load_dwordx4 v[4:7], v201, s[42:43] offset:-2048 sc1\n" \
"s_mov_b64 exec, -1\n" \
"s_waitcnt lgkmcnt(6)\n" \
"v_mfma_f32_16x16x32_bf16 v[148:151], a[16:19], v[116:119], v[148:151]\n" \
"s_waitcnt lgkmcnt(5)\n" \
"v_mfma_f32_16x16x32_bf16 v[144:147], a[20:23], v[120:123], v[144:147]\n" \
"s_mov_b64 exec, s[64:65]\n" \
"global_load_dwordx4 v[8:11], v201, s[42:43] offset:0 sc1\n" \
"s_mov_b64 exec, -1\n" \
"s_waitcnt lgkmcnt(4)\n" \
"v_mfma_f32_16x16x32_bf16 v[148:151], a[20:23], v[124:127], v[148:151]\n" \
"s_waitcnt lgkmcnt(3)\n" \
"v_mfma_f32_16x16x32_bf16 v[144:147], a[24:27], v[128:131], v[144:147]\n" \
"s_mov_b64 exec, s[64:65]\n" \
"global_load_dwordx4 v[12:15], v201, s[42:43] offset:2048 sc1\n" \
"s_mov_b64 exec, -1\n" \
"s_waitcnt lgkmcnt(2)\n" \
"v_mfma_f32_16x16x32_bf16 v[148:151], a[24:27], v[132:135], v[148:151]\n" \
"s_waitcnt lgkmcnt(1)\n" \
"v_mfma_f32_16x16x32_bf16 v[144:147], a[28:31], v[136:139], v[144:147]\n" \
"s_mov_b64 exec, s[64:65]\n" \
"s_add_u32 m0, s56, 0\n" \
"s_nop 0\n" \
"global_load_lds_dwordx4 v201, s[44:45] offset:0 sc1\n" \
"s_mov_b64 exec, -1\n" \
"s_waitcnt lgkmcnt(0)\n" \
"v_mfma_f32_16x16x32_bf16 v[148:151], a[28:31], v[140:143], v[148:151]\n" \
"v_mfma_f32_16x16x32_bf16 v[152:155], a[32:35], v[80:83], 0\n" \
"s_mov_b64 exec, s[64:65]\n" \
"global_load_lds_dwordx4 v201, s[44:45] offset:1024 sc1\n" \
"s_mov_b64 exec, -1\n" \
"v_mfma_f32_16x16x32_bf16 v[156:159], a[32:35], v[84:87], 0\n" \
"v_mfma_f32_16x16x32_bf16 v[152:155], a[36:39], v[88:91], v[152:155]\n" \
"v_mfma_f32_16x16x32_bf16 v[156:159], a[36:39], v[92:95], v[156:159]\n" \
"v_cvt_pk_bf16_f32 v168, v144, v145\n" \
"v_mfma_f32_16x16x32_bf16 v[152:155], a[40:43], v[96:99], v[152:155]\n" \
"v_cvt_pk_bf16_f32 v169, v146, v147\n" \
"v_mfma_f32_16x16x32_bf16 v[156:159], a[40:43], v[100:103], v[156:159]\n" \
"v_cvt_pk_bf16_f32 v170, v148, v149\n" \
"v_mfma_f32_16x16x32_bf16 v[152:155], a[44:47], v[104:107], v[152:155]\n" \
"v_cvt_pk_bf16_f32 v171, v150, v151\n" \
"v_mfma_f32_16x16x32_bf16 v[156:159], a[44:47], v[108:111], v[156:159]\n" \
"global_store_dwordx4 v201, v[168:171], s[48:49] offset:-4096 sc1\n" \
"v_mfma_f32_16x16x32_bf16 v[152:155], a[48:51], v[112:115], v[152:155]\n" \
"s_mov_b64 exec, s[64:65]\n" \
"global_load_lds_dwordx4 v201, s[44:45] offset:2048 sc1\n" \
"s_mov_b64 exec, -1\n" \
"v_mfma_f32_16x16x32_bf16 v[156:159], a[48:51], v[116:119], v[156:159]\n" \
"v_mfma_f32_16x16x32_bf16 v[152:155], a[52:55], v[120:123], v[152:155]\n" \
"s_mov_b64 exec, s[64:65]\n" \
"global_load_lds_dwordx4 v201, s[44:45] offset:3072 sc1\n" \
"s_mov_b64 exec, -1\n" \
"v_mfma_f32_16x16x32_bf16 v[156:159], a[52:55], v[124:127], v[156:159]\n" \
"v_mfma_f32_16x16x32_bf16 v[152:155], a[56:59], v[128:131], v[152:155]\n" \
"s_mov_b64 exec, s[64:65]\n" \
"s_add_u32 m0, s56, 4096\n" \
"s_nop 0\n" \
"global_load_lds_dwordx4 v215, s[44:45] offset:0 sc1\n" \
"s_mov_b64 exec, -1\n" \
"v_mfma_f32_16x16x32_bf16 v[156:159], a[56:59], v[132:135], v[156:159]\n" \
"v_mfma_f32_16x16x32_bf16 v[152:155], a[60:63], v[136:139], v[152:155]\n" \
"s_mov_b64 exec, s[64:65]\n" \
"global_load_lds_dwordx4 v215, s[44:45] offset:1024 sc1\n" \
"s_mov_b64 exec, -1\n" \
"v_mfma_f32_16x16x32_bf16 v[156:159], a[60:63], v[140:143], v[156:159]\n" \
"v_mfma_f32_16x16x32_bf16 v[160:163], a[64:67], v[80:83], 0\n" \
"s_mov_b64 exec, s[64:65]\n" \
"global_load_lds_dwordx4 v215, s[44:45] offset:2048 sc1\n" \
"s_mov_b64 exec, -1\n" \
"v_mfma_f32_16x16x32_bf16 v[164:167], a[64:67], v[84:87], 0\n" \
"v_mfma_f32_16x16x32_bf16 v[160:163], a[68:71], v[88:91], v[160:163]\n" \
"v_mfma_f32_16x16x32_bf16 v[164:167], a[68:71], v[92:95], v[164:167]\n" \
"v_cvt_pk_bf16_f32 v172, v152, v153\n" \
"v_mfma_f32_16x16x32_bf16 v[160:163], a[72:75], v[96:99], v[160:163]\n" \
"v_cvt_pk_bf16_f32 v173, v154, v155\n" \
"v_mfma_f32_16x16x32_bf16 v[164:167], a[72:75], v[100:103], v[164:167]\n" \
"v_cvt_pk_bf16_f32 v174, v156, v157\n" \
"v_mfma_f32_16x16x32_bf16 v[160:163], a[76:79], v[104:107], v[160:163]\n" \
"v_cvt_pk_bf16_f32 v175, v158, v159\n" \
"v_mfma_f32_16x16x32_bf16 v[164:167], a[76:79], v[108:111], v[164:167]\n" \
"global_store_dwordx4 v201, v[172:175], s[48:49] offset:-3072 sc1\n" \
"v_mfma_f32_16x16x32_bf16 v[160:163], a[80:83], v[112:115], v[160:163]\n" \
"s_mov_b64 exec, s[64:65]\n" \
"global_load_lds_dwordx4 v215, s[44:45] offset:3072 sc1\n" \
"s_mov_b64 exec, -1\n" \
"v_mfma_f32_16x16x32_bf16 v[164:167], a[80:83], v[116:119], v[164:167]\n" \
"v_mfma_f32_16x16x32_bf16 v[160:163], a[84:87], v[120:123], v[160:163]\n" \
"s_add_u32 s42, s42, 32768\n" \
"s_addc_u32 s43, s43, 0\n" \
"v_mfma_f32_16x16x32_bf16 v[164:167], a[84:87], v[124:127], v[164:167]\n" \
"v_mfma_f32_16x16x32_bf16 v[160:163], a[88:91], v[128:131], v[160:163]\n" \
"s_add_u32 s44, s44, 65536\n" \
"s_addc_u32 s45, s45, 0\n" \
"s_xor_b32 s50, s44, 0x2000\n" \
"s_mov_b32 s51, s45\n" \
"v_mfma_f32_16x16x32_bf16 v[164:167], a[88:91], v[132:135], v[164:167]\n" \
"v_mfma_f32_16x16x32_bf16 v[160:163], a[92:95], v[136:139], v[160:163]\n" \
"v_mfma_f32_16x16x32_bf16 v[164:167], a[92:95], v[140:143], v[164:167]\n" \
"v_mfma_f32_16x16x32_bf16 v[144:147], a[96:99], v[80:83], 0\n" \
"v_readfirstlane_b32 s69, v203\n" \
"s_cmp_eq_u32 s59, 64\n" \
"s_cselect_b32 s74, s68, s67\n" \
"v_mfma_f32_16x16x32_bf16 v[148:151], a[96:99], v[84:87], 0\n" \
"s_and_b32 s72, s69, 0xffff\n" \
"s_sub_i32 s72, s59, s72\n" \
"s_add_i32 s72, s72, 1\n" \
"v_mfma_f32_16x16x32_bf16 v[144:147], a[100:103], v[88:91], v[144:147]\n" \
"s_cmp_lt_i32 s72, 0\n" \
"s_cselect_b32 s91, 0, 0x4000000\n" \
"s_mul_i32 s72, s72, s66\n" \
"v_mfma_f32_16x16x32_bf16 v[148:151], a[100:103], v[92:95], v[148:151]\n" \
"v_cvt_pk_bf16_f32 v168, v160, v161\n" \
"v_mfma_f32_16x16x32_bf16 v[144:147], a[104:107], v[96:99], v[144:147]\n" \
"v_cvt_pk_bf16_f32 v169, v162, v163\n" \
"v_mfma_f32_16x16x32_bf16 v[148:151], a[104:107], v[100:103], v[148:151]\n" \
"v_cvt_pk_bf16_f32 v170, v164, v165\n" \
"v_mfma_f32_16x16x32_bf16 v[144:147], a[108:111], v[104:107], v[144:147]\n" \
"v_cvt_pk_bf16_f32 v171, v166, v167\n" \
"v_mfma_f32_16x16x32_bf16 v[148:151], a[108:111], v[108:111], v[148:151]\n" \
"global_store_dwordx4 v201, v[168:171], s[48:49] offset:-2048 sc1\n" \
"v_mfma_f32_16x16x32_bf16 v[144:147], a[112:115], v[112:115], v[144:147]\n" \
"s_add_u32 s69, s69, s74\n" \
"s_add_u32 s69, s69, s72\n" \
"s_memrealtime s[92:93]\n" \
"v_mfma_f32_16x16x32_bf16 v[148:151], a[112:115], v[116:119], v[148:151]\n" \
"s_nop 0\n" \
"v_mfma_f32_16x16x32_bf16 v[144:147], a[116:119], v[120:123], v[144:147]\n" \
"s_nop 0\n" \
"v_mfma_f32_16x16x32_bf16 v[148:151], a[116:119], v[124:127], v[148:151]\n" \
"s_nop 0\n" \
"v_mfma_f32_16x16x32_bf16 v[144:147], a[120:123], v[128:131], v[144:147]\n" \
"s_waitcnt lgkmcnt(0)\n" \
"s_lshl_b32 s92, s92, 16\n" \
"s_sub_i32 s92, s69, s92\n" \
"v_mfma_f32_16x16x32_bf16 v[148:151], a[120:123], v[132:135], v[148:151]\n" \
"v_mfma_f32_16x16x32_bf16 v[144:147], a[124:127], v[136:139], v[144:147]\n" \
"v_mfma_f32_16x16x32_bf16 v[148:151], a[124:127], v[140:143], v[148:151]\n" \
"s_cmp_ge_u32 s92, s91\n" \
"s_cselect_b32 s94, s60, 0\n" \
"s_cmp_eq_u32 s94, 0\n" \
"s_cbranch_scc0 .Lkp%=\n" \
"v_mfma_f32_16x16x32_bf16 v[152:155], a[128:131], v[80:83], 0\n" \
"v_mfma_f32_16x16x32_bf16 v[156:159], a[128:131], v[84:87], 0\n" \
"v_mfma_f32_16x16x32_bf16 v[152:155], a[132:135], v[88:91], v[152:155]\n" \
"v_mfma_f32_16x16x32_bf16 v[156:159], a[132:135], v[92:95], v[156:159]\n" \
"v_cvt_pk_bf16_f32 v172, v144, v145\n" \
"v_mfma_f32_16x16x32_bf16 v[152:155], a[136:139], v[96:99], v[152:155]\n" \
"v_cvt_pk_bf16_f32 v173, v146, v147\n" \
"v_mfma_f32_16x16x32_bf16 v[156:159], a[136:139], v[100:103], v[156:159]\n" \
"v_cvt_pk_bf16_f32 v174, v148, v149\n" \
"v_mfma_f32_16x16x32_bf16 v[152:155], a[140:143], v[104:107], v[152:155]\n" \
"v_cvt_pk_bf16_f32 v175, v150, v151\n" \
"v_mfma_f32_16x16x32_bf16 v[156:159], a[140:143], v[108:111], v[156:159]\n" \
"global_store_dwordx4 v201, v[172:175], s[48:49] offset:-1024 sc1\n" \
"v_mfma_f32_16x16x32_bf16 v[152:155], a[144:147], v[112:115], v[152:155]\n" \
"v_mfma_f32_16x16x32_bf16 v[156:159], a[144:147], v[116:119], v[156:159]\n" \
"v_mfma_f32_16x16x32_bf16 v[152:155], a[148:151], v[120:123], v[152:155]\n" \
"v_mfma_f32_16x16x32_bf16 v[156:159], a[148:151], v[124:127], v[156:159]\n" \
"v_mfma_f32_16x16x32_bf16 v[152:155], a[152:155], v[128:131], v[152:155]\n" \
"v_mfma_f32_16x16x32_bf16 v[156:159], a[152:155], v[132:135], v[156:159]\n" \
"v_mfma_f32_16x16x32_bf16 v[152:155], a[156:159], v[136:139], v[152:155]\n" \
"v_mfma_f32_16x16x32_bf16 v[156:159], a[156:159], v[140:143], v[156:159]\n" \
".Lkq%=:\n" \
"v_mfma_f32_16x16x32_bf16 v[160:163], a[160:163], v[80:83], 0\n" \
"global_load_dword v203, v205, s[54:55] sc1\n" \
"v_mfma_f32_16x16x32_bf16 v[164:167], a[160:163], v[84:87], 0\n" \
"v_mfma_f32_16x16x32_bf16 v[160:163], a[164:167], v[88:91], v[160:163]\n" \
"v_mfma_f32_16x16x32_bf16 v[164:167], a[164:167], v[92:95], v[164:167]\n" \
"s_nop 1\n" \
"v_cvt_pk_bf16_f32 v168, v152, v153\n" \
"v_mfma_f32_16x16x32_bf16 v[160:163], a[168:171], v[96:99], v[160:163]\n" \
"v_cvt_pk_bf16_f32 v169, v154, v155\n" \
"v_mfma_f32_16x16x32_bf16 v[164:167], a[168:171], v[100:103], v[164:167]\n" \
"v_cvt_pk_bf16_f32 v170, v156, v157\n" \
"v_mfma_f32_16x16x32_bf16 v[160:163], a[172:175], v[104:107], v[160:163]\n" \
"v_cvt_pk_bf16_f32 v171, v158, v159\n" \
"v_mfma_f32_16x16x32_bf16 v[164:167], a[172:175], v[108:111], v[164:167]\n" \
"global_store_dwordx4 v201, v[168:171], s[48:49] offset:0 sc1\n" \
"v_mfma_f32_16x16x32_bf16 v[160:163], a[176:179], v[112:115], v[160:163]\n" \
"v_mfma_f32_16x16x32_bf16 v[164:167], a[176:179], v[116:119], v[164:167]\n" \
"v_mfma_f32_16x16x32_bf16 v[160:163], a[180:183], v[120:123], v[160:163]\n" \
"v_mfma_f32_16x16x32_bf16 v[164:167], a[180:183], v[124:127], v[164:167]\n" \
"v_mfma_f32_16x16x32_bf16 v[160:163], a[184:187], v[128:131], v[160:163]\n" \
"v_mfma_f32_16x16x32_bf16 v[164:167], a[184:187], v[132:135], v[164:167]\n" \
"v_mfma_f32_16x16x32_bf16 v[160:163], a[188:191], v[136:139], v[160:163]\n" \
"v_mfma_f32_16x16x32_bf16 v[164:167], a[188:191], v[140:143], v[164:167]\n" \
"s_cmp_eq_u32 s61, 0\n" \
"s_cbranch_scc1 .Lnp%=\n" \
"v_mov_b32 v202, s61\n" \
"s_mov_b64 exec, 1\n" \
"global_store_dword v207, v202, s[52:53] sc1\n" \
"global_store_dword v204, v203, s[52:53] sc1\n" \
"s_mov_b64 exec, -1\n" \
".Lnp%=:\n" \
"v_mfma_f32_16x16x32_bf16 v[144:147], a[192:195], v[80:83], 0\n" \
"v_mfma_f32_16x16x32_bf16 v[148:151], a[192:195], v[84:87], 0\n" \
"v_mfma_f32_16x16x32_bf16 v[144:147], a[196:199], v[88:91], v[144:147]\n" \
"v_mfma_f32_16x16x32_bf16 v[148:151], a[196:199], v[92:95], v[148:151]\n" \
"v_cvt_pk_bf16_f32 v172, v160, v161\n" \
"v_mfma_f32_16x16x32_bf16 v[144:147], a[200:203], v[96:99], v[144:147]\n" \
"v_cvt_pk_bf16_f32 v173, v162, v163\n" \
"v_mfma_f32_16x16x32_bf16 v[148:151], a[200:203], v[100:103], v[148:151]\n" \
"v_cvt_pk_bf16_f32 v174, v164, v165\n" \
"v_mfma_f32_16x16x32_bf16 v[144:147], a[204:207], v[104:107], v[144:147]\n" \
"v_cvt_pk_bf16_f32 v175, v166, v167\n" \
"v_mfma_f32_16x16x32_bf16 v[148:151], a[204:207], v[108:111], v[148:151]\n" \
"global_store_dwordx4 v201, v[172:175], s[48:49] offset:1024 sc1\n" \
"v_mfma_f32_16x16x32_bf16 v[144:147], a[208:211], v[112:115], v[144:147]\n" \
"v_mfma_f32_16x16x32_bf16 v[148:151], a[208:211], v[116:119], v[148:151]\n" \
"v_mfma_f32_16x16x32_bf16 v[144:147], a[212:215], v[120:123], v[144:147]\n" \
"v_mfma_f32_16x16x32_bf16 v[148:151], a[212:215], v[124:127], v[148:151]\n" \
"v_mfma_f32_16x16x32_bf16 v[144:147], a[216:219], v[128:131], v[144:147]\n" \
"v_mfma_f32_16x16x32_bf16 v[148:151], a[216:219], v[132:135], v[148:151]\n" \
"v_mfma_f32_16x16x32_bf16 v[144:147], a[220:223], v[136:139], v[144:147]\n" \
"v_mfma_f32_16x16x32_bf16 v[148:151], a[220:223], v[140:143], v[148:151]\n" \
"v_mfma_f32_16x16x32_bf16 v[152:155], a[224:227], v[80:83], 0\n" \
"v_mfma_f32_16x16x32_bf16 v[156:159], a[224:227], v[84:87], 0\n" \
"v_mfma_f32_16x16x32_bf16 v[152:155], a[228:231], v[88:91], v[152:155]\n" \
"v_mfma_f32_16x16x32_bf16 v[156:159], a[228:231], v[92:95], v[156:159]\n" \
"s_nop 2\n" \
"v_cvt_pk_bf16_f32 v168, v144, v145\n" \
"v_mfma_f32_16x16x32_bf16 v[152:155], a[232:235], v[96:99], v[152:155]\n" \
"v_cvt_pk_bf16_f32 v169, v146, v147\n" \
"v_mfma_f32_16x16x32_bf16 v[156:159], a[232:235], v[100:103], v[156:159]\n" \
"v_cvt_pk_bf16_f32 v170, v148, v149\n" \
"v_mfma_f32_16x16x32_bf16 v[152:155], a[236:239], v[104:107], v[152:155]\n" \
"v_cvt_pk_bf16_f32 v171, v150, v151\n" \
"v_mfma_f32_16x16x32_bf16 v[156:159], a[236:239], v[108:111], v[156:159]\n" \
"global_store_dwordx4 v201, v[168:171], s[48:49] offset:2048 sc1\n" \
"v_mfma_f32_16x16x32_bf16 v[152:155], a[240:243], v[112:115], v[152:155]\n" \
"v_mfma_f32_16x16x32_bf16 v[156:159], a[240:243], v[116:119], v[156:159]\n" \
"v_mfma_f32_16x16x32_bf16 v[152:155], a[244:247], v[120:123], v[152:155]\n" \
"v_mfma_f32_16x16x32_bf16 v[156:159], a[244:247], v[124:127], v[156:159]\n" \
"v_mfma_f32_16x16x32_bf16 v[152:155], a[248:251], v[128:131], v[152:155]\n" \
"v_mfma_f32_16x16x32_bf16 v[156:159], a[248:251], v[132:135], v[156:159]\n" \
"v_mfma_f32_16x16x32_bf16 v[152:155], a[252:255], v[136:139], v[152:155]\n" \
"v_mfma_f32_16x16x32_bf16 v[156:159], a[252:255], v[140:143], v[156:159]\n" \
"v_mfma_f32_16x16x32_bf16 v[160:163], v[224:227], v[80:83], 0\n" \
"v_mfma_f32_16x16x32_bf16 v[164:167], v[224:227], v[84:87], 0\n" \
"v_mfma_f32_16x16x32_bf16 v[160:163], v[228:231], v[88:91], v[160:163]\n" \
"v_mfma_f32_16x16x32_bf16 v[164:167], v[228:231], v[92:95], v[164:167]\n" \
"s_nop 2\n" \
"v_cvt_pk_bf16_f32 v172, v152, v153\n" \
"v_mfma_f32_16x16x32_bf16 v[160:163], v[232:235], v[96:99], v[160:163]\n" \
"v_cvt_pk_bf16_f32 v173, v154, v155\n" \
"v_mfma_f32_16x16x32_bf16 v[164:167], v[232:235], v[100:103], v[164:167]\n" \
"v_cvt_pk_bf16_f32 v174, v156, v157\n" \
"v_mfma_f32_16x16x32_bf16 v[160:163], v[236:239], v[104:107], v[160:163]\n" \
"v_cvt_pk_bf16_f32 v175, v158, v159\n" \
"v_mfma_f32_16x16x32_bf16 v[164:167], v[236:239], v[108:111], v[164:167]\n" \
"global_store_dwordx4 v201, v[172:175], s[48:49] offset:3072 sc1\n" \
"v_mfma_f32_16x16x32_bf16 v[160:163], v[240:243], v[112:115], v[160:163]\n" \
"v_mfma_f32_16x16x32_bf16 v[164:167], v[240:243], v[116:119], v[164:167]\n" \
"v_mfma_f32_16x16x32_bf16 v[160:163], v[244:247], v[120:123], v[160:163]\n" \
"v_mfma_f32_16x16x32_bf16 v[164:167], v[244:247], v[124:127], v[164:167]\n" \
"v_mfma_f32_16x16x32_bf16 v[160:163], v[248:251], v[128:131], v[160:163]\n" \
"v_mfma_f32_16x16x32_bf16 v[164:167], v[248:251], v[132:135], v[164:167]\n" \
"v_mfma_f32_16x16x32_bf16 v[160:163], v[252:255], v[136:139], v[160:163]\n" \
"v_mfma_f32_16x16x32_bf16 v[164:167], v[252:255], v[140:143], v[164:167]\n" \
"s_nop 6\n" \
"v_cvt_pk_bf16_f32 v168, v160, v161\n" \
"v_cvt_pk_bf16_f32 v169, v162, v163\n" \
"v_cvt_pk_bf16_f32 v170, v164, v165\n" \
"v_cvt_pk_bf16_f32 v171, v166, v167\n" \
"global_store_dwordx4 v215, v[168:171], s[48:49] offset:0 sc1\n" \
"s_add_u32 s48, s48, 1179648\n" \
"s_addc_u32 s49, s49, 0\n" \
"s_cmp_eq_u32 s61, 0\n" \
"s_waitcnt vmcnt(7)\n" \
"s_cbranch_scc0 .Lnq%=\n" \
"s_waitcnt vmcnt(5)\n" \
".Lnq%=:\n" \
"s_mov_b32 s61, s58\n" \
"s_mov_b32 s58, s59\n" \
"s_cmp_le_u32 s59, 63\n" \
"s_cbranch_scc1 .Ltop%=\n" \
"s_waitcnt vmcnt(0)\n" \
"v_mov_b32 v169, s61\n" \
"s_mov_b64 exec, 1\n" \
"global_store_dword v207, v169, s[52:53] sc1\n" \
"global_store_dword v204, v203, s[52:53] sc1\n" \
"s_mov_b64 exec, -1\n" \
".Lend%=:\n" \
"s_endpgm\n" \
".Lhb_s%=:\n" \
"s_sub_u32 s73, s73, 1\n" \
"s_cbranch_scc1 .Lend%=\n" \
"global_load_dword v152, v150, s[54:55] sc1\n" \
"s_waitcnt vmcnt(0)\n" \
"s_branch .Lhb_c%=\n" \
".Lrdy_s%=:\n" \
"s_sub_u32 s73, s73, 1\n" \
"s_cbranch_scc1 .Lend%=\n" \
"s_sleep 4\n" \
"global_load_dword v168, v169, s[54:55] sc1\n" \
"s_waitcnt vmcnt(0)\n" \
"s_branch .Lrdy_c%=\n" \
".Lp0_s%=:\n" \
"s_sub_u32 s73, s73, 1\n" \
"s_cbranch_scc1 .Lend%=\n" \
"s_sleep 4\n" \
"global_load_dword v168, v206, s[54:55] sc1\n" \
"s_waitcnt vmcnt(0)\n" \
"s_branch .Lp0_c%=\n" \
".Lpg_s%=:\n" \
"s_sub_u32 s73, s73, 1\n" \
"s_cbranch_scc1 .Lend%=\n" \
"s_sleep 4\n" \
"global_load_dword v203, v205, s[54:55] sc1\n" \
"s_waitcnt vmcnt(0)\n" \
"s_branch .Lpg_c%=\n" \
".Lkm_s%=:\n" \
"s_sub_u32 s73, s73, 1\n" \
"s_cbranch_scc1 .Lend%=\n" \
"s_cmp_ge_u32 s58, 63\n" \
"s_cbranch_scc1 .Lkm_l%=\n" \
"global_load_dword v203, v205, s[54:55] sc1\n" \
"s_waitcnt vmcnt(0)\n" \
"v_readfirstlane_b32 s69, v203\n" \
"s_cmp_eq_u32 s58, 64\n" \
"s_cselect_b32 s74, s68, s67\n" \
"s_and_b32 s72, s69, 0xffff\n" \
"s_sub_i32 s72, s58, s72\n" \
"s_add_i32 s72, s72, 1\n" \
"s_cmp_lt_i32 s72, 0\n" \
"s_cselect_b32 s91, 0, 0x4000000\n" \
"s_mul_i32 s72, s72, s66\n" \
"s_add_u32 s69, s69, s74\n" \
"s_add_u32 s69, s69, s72\n" \
"s_branch .Lkm_w%=\n" \
".Lkm_u%=:\n" \
"s_waitcnt vmcnt(14)\n" \
"v_mov_b32 v169, s61\n" \
"s_mov_b64 exec, 1\n" \
"global_store_dword v207, v169, s[52:53] sc1\n" \
"global_store_dword v204, v203, s[52:53] sc1\n" \
"s_mov_b64 exec, -1\n" \
"s_mov_b32 s61, 0\n" \
"s_waitcnt vmcnt(4)\n" \
"s_branch .Lkm_c%=\n" \
".Lkm_p%=:\n" \
"global_load_dword v202, v206, s[54:55] sc1\n" \
"global_load_dword v203, v205, s[54:55] sc1\n" \
"s_waitcnt lgkmcnt(0)\n" \
"s_branch .Lkm_c%=\n" \
".Lp1_s%=:\n" \
"s_sub_u32 s73, s73, 1\n" \
"s_cbranch_scc1 .Lend%=\n" \
"s_sleep 4\n" \
"global_load_dword v202, v206, s[54:55] sc1\n" \
"s_waitcnt vmcnt(0)\n" \
"s_branch .Lp1_c%=\n" \
".Lkp%=:\n" \
"v_mfma_f32_16x16x32_bf16 v[152:155], a[128:131], v[80:83], 0\n" \
"global_load_dwordx4 v[176:179], v210, s[46:47] offset:-4096 sc1\n" \
"v_mfma_f32_16x16x32_bf16 v[156:159], a[128:131], v[84:87], 0\n" \
"global_load_dwordx4 v[180:183], v210, s[46:47] offset:-2048 sc1\n" \
"v_mfma_f32_16x16x32_bf16 v[152:155], a[132:135], v[88:91], v[152:155]\n" \
"global_load_dwordx4 v[184:187], v210, s[46:47] offset:0 sc1\n" \
"v_mfma_f32_16x16x32_bf16 v[156:159], a[132:135], v[92:95], v[156:159]\n" \
"v_cvt_pk_bf16_f32 v172, v144, v145\n" \
"v_mfma_f32_16x16x32_bf16 v[152:155], a[136:139], v[96:99], v[152:155]\n" \
"v_cvt_pk_bf16_f32 v173, v146, v147\n" \
"v_mfma_f32_16x16x32_bf16 v[156:159], a[136:139], v[100:103], v[156:159]\n" \
"v_cvt_pk_bf16_f32 v174, v148, v149\n" \
"v_mfma_f32_16x16x32_bf16 v[152:155], a[140:143], v[104:107], v[152:155]\n" \
"v_cvt_pk_bf16_f32 v175, v150, v151\n" \
"v_mfma_f32_16x16x32_bf16 v[156:159], a[140:143], v[108:111], v[156:159]\n" \
"global_store_dwordx4 v201, v[172:175], s[48:49] offset:-1024 sc1\n" \
"v_mfma_f32_16x16x32_bf16 v[152:155], a[144:147], v[112:115], v[152:155]\n" \
"global_load_dwordx4 v[188:191], v210, s[46:47] offset:2048 sc1\n" \
"v_mfma_f32_16x16x32_bf16 v[156:159], a[144:147], v[116:119], v[156:159]\n" \
"global_load_dword v216, v207, s[46:47] offset:-3072 sc1\n" \
"v_mfma_f32_16x16x32_bf16 v[152:155], a[148:151], v[120:123], v[152:155]\n" \
"global_load_dword v217, v207, s[46:47] offset:-2816 sc1\n" \
"v_mfma_f32_16x16x32_bf16 v[156:159], a[148:151], v[124:127], v[156:159]\n" \
"global_load_dword v218, v207, s[46:47] offset:-1024 sc1\n" \
"v_mfma_f32_16x16x32_bf16 v[152:155], a[152:155], v[128:131], v[152:155]\n" \
"global_load_dword v219, v207, s[46:47] offset:-768 sc1\n" \
"v_mfma_f32_16x16x32_bf16 v[156:159], a[152:155], v[132:135], v[156:159]\n" \
"global_load_dword v220, v207, s[46:47] offset:1024 sc1\n" \
"v_mfma_f32_16x16x32_bf16 v[152:155], a[156:159], v[136:139], v[152:155]\n" \
"global_load_dword v221, v207, s[46:47] offset:1280 sc1\n" \
"v_mfma_f32_16x16x32_bf16 v[156:159], a[156:159], v[140:143], v[156:159]\n" \
"global_load_dword v222, v207, s[46:47] offset:3072 sc1\n" \
"global_load_dword v223, v207, s[46:47] offset:3328 sc1\n" \
"s_branch .Lkq%=\n"

#define ATA_ASM \
"s_mov_b32 s73, 2000000\n" \
"s_mov_b32 s75, 0x3e0293ee\n" \
"v_mov_b32 v212, v200\n" \
"v_add_u32 v213, 0x10000, v200\n" \
"v_add_u32 v214, 0x20000, v200\n" \
"v_mov_b32 v150, s65\n" \
"s_memrealtime s[76:77]\n" \
"s_waitcnt lgkmcnt(0)\n" \
".Ldly%=:\n" \
"s_memrealtime s[78:79]\n" \
"s_waitcnt lgkmcnt(0)\n" \
"s_sub_u32 s78, s78, s76\n" \
"s_cmp_lt_u32 s78, s64\n" \
"s_cbranch_scc1 .Ldly%=\n" \
"s_add_u32 m0, s57, 0\n" \
"s_nop 0\n" \
"global_load_lds_dwordx4 v202, s[40:41] offset:0\n" \
"s_sleep 3\n" \
"global_load_lds_dwordx4 v202, s[40:41] offset:1024\n" \
"s_sleep 3\n" \
"global_load_lds_dwordx4 v202, s[40:41] offset:2048\n" \
"s_sleep 3\n" \
"global_load_lds_dwordx4 v202, s[40:41] offset:3072\n" \
"s_sleep 3\n" \
"s_add_u32 m0, s57, 4096\n" \
"s_nop 0\n" \
"global_load_lds_dwordx4 v203, s[40:41] offset:0\n" \
"s_sleep 3\n" \
"global_load_lds_dwordx4 v203, s[40:41] offset:1024\n" \
"s_sleep 3\n" \
"global_load_lds_dwordx4 v203, s[40:41] offset:2048\n" \
"s_sleep 3\n" \
"global_load_lds_dwordx4 v203, s[40:41] offset:3072\n" \
"s_sleep 3\n" \
"s_add_u32 m0, s57, 8192\n" \
"s_nop 0\n" \
"global_load_lds_dwordx4 v204, s[40:41] offset:0\n" \
"s_sleep 3\n" \
"s_add_u32 s40, s40, 36864\n" \
"s_addc_u32 s41, s41, 0\n" \
"s_add_u32 m0, s57, 36864\n" \
"s_nop 0\n" \
"global_load_lds_dwordx4 v202, s[40:41] offset:0\n" \
"s_sleep 3\n" \
"global_load_lds_dwordx4 v202, s[40:41] offset:1024\n" \
"s_sleep 3\n" \
"global_load_lds_dwordx4 v202, s[40:41] offset:2048\n" \
"s_sleep 3\n" \
"global_load_lds_dwordx4 v202, s[40:41] offset:3072\n" \
"s_sleep 3\n" \
"s_add_u32 m0, s57, 40960\n" \
"s_nop 0\n" \
"global_load_lds_dwordx4 v203, s[40:41] offset:0\n" \
"s_sleep 3\n" \
"global_load_lds_dwordx4 v203, s[40:41] offset:1024\n" \
"s_sleep 3\n" \
"global_load_lds_dwordx4 v203, s[40:41] offset:2048\n" \
"s_sleep 3\n" \
"global_load_lds_dwordx4 v203, s[40:41] offset:3072\n" \
"s_sleep 3\n" \
"s_add_u32 m0, s57, 45056\n" \
"s_nop 0\n" \
"global_load_lds_dwordx4 v204, s[40:41] offset:0\n" \
"s_sleep 3\n" \
"s_add_u32 s40, s40, 36864\n" \
"s_addc_u32 s41, s41, 0\n" \
"s_add_u32 m0, s57, 73728\n" \
"s_nop 0\n" \
"global_load_lds_dwordx4 v202, s[40:41] offset:0\n" \
"s_sleep 3\n" \
"global_load_lds_dwordx4 v202, s[40:41] offset:1024\n" \
"s_sleep 3\n" \
"global_load_lds_dwordx4 v202, s[40:41] offset:2048\n" \
"s_sleep 3\n" \
"global_load_lds_dwordx4 v202, s[40:41] offset:3072\n" \
"s_sleep 3\n" \
"s_add_u32 m0, s57, 77824\n" \
"s_nop 0\n" \
"global_load_lds_dwordx4 v203, s[40:41] offset:0\n" \
"s_sleep 3\n" \
"global_load_lds_dwordx4 v203, s[40:41] offset:1024\n" \
"s_sleep 3\n" \
"global_load_lds_dwordx4 v203, s[40:41] offset:2048\n" \
"s_sleep 3\n" \
"global_load_lds_dwordx4 v203, s[40:41] offset:3072\n" \
"s_sleep 3\n" \
"s_add_u32 m0, s57, 81920\n" \
"s_nop 0\n" \
"global_load_lds_dwordx4 v204, s[40:41] offset:0\n" \
"s_sleep 3\n" \
"s_add_u32 s40, s40, 36864\n" \
"s_addc_u32 s41, s41, 0\n" \
"s_add_u32 m0, s57, 110592\n" \
"s_nop 0\n" \
"global_load_lds_dwordx4 v202, s[40:41] offset:0\n" \
"s_sleep 3\n" \
"global_load_lds_dwordx4 v202, s[40:41] offset:1024\n" \
"s_sleep 3\n" \
"global_load_lds_dwordx4 v202, s[40:41] offset:2048\n" \
"s_sleep 3\n" \
"global_load_lds_dwordx4 v202, s[40:41] offset:3072\n" \
"s_sleep 3\n" \
"s_add_u32 m0, s57, 114688\n" \
"s_nop 0\n" \
"global_load_lds_dwordx4 v203, s[40:41] offset:0\n" \
"s_sleep 3\n" \
"global_load_lds_dwordx4 v203, s[40:41] offset:1024\n" \
"s_sleep 3\n" \
"global_load_lds_dwordx4 v203, s[40:41] offset:2048\n" \
"s_sleep 3\n" \
"global_load_lds_dwordx4 v203, s[40:41] offset:3072\n" \
"s_sleep 3\n" \
"s_add_u32 m0, s57, 118784\n" \
"s_nop 0\n" \
"global_load_lds_dwordx4 v204, s[40:41] offset:0\n" \
"s_sleep 3\n" \
"s_add_u32 s40, s40, 36864\n" \
"s_addc_u32 s41, s41, 0\n" \
"s_waitcnt vmcnt(27)\n" \
"s_barrier\n" \
"ds_read_b64_tr_b16 a[0:1], v212 offset:0\n" \
"ds_read_b64_tr_b16 a[2:3], v212 offset:18432\n" \
"ds_read_b64_tr_b16 a[32:33], v212 offset:32\n" \
"ds_read_b64_tr_b16 a[34:35], v212 offset:18464\n" \
"ds_read_b64_tr_b16 a[64:65], v212 offset:64\n" \
"ds_read_b64_tr_b16 a[66:67], v212 offset:18496\n" \
"ds_read_b64_tr_b16 a[96:97], v212 offset:96\n" \
"ds_read_b64_tr_b16 a[98:99], v212 offset:18528\n" \
"ds_read_b64_tr_b16 a[128:129], v212 offset:128\n" \
"ds_read_b64_tr_b16 a[130:131], v212 offset:18560\n" \
"s_waitcnt lgkmcnt(4)\n" \
"s_waitcnt lgkmcnt(0)\n" \
"s_waitcnt vmcnt(18)\n" \
"s_barrier\n" \
"s_add_u32 m0, s57, 0\n" \
"s_nop 0\n" \
"global_load_lds_dwordx4 v202, s[40:41] offset:0\n" \
"s_sleep 3\n" \
"global_load_lds_dwordx4 v202, s[40:41] offset:1024\n" \
"s_sleep 3\n" \
"global_load_lds_dwordx4 v202, s[40:41] offset:2048\n" \
"s_sleep 3\n" \
"global_load_lds_dwordx4 v202, s[40:41] offset:3072\n" \
"s_sleep 3\n" \
"s_add_u32 m0, s57, 4096\n" \
"s_nop 0\n" \
"global_load_lds_dwordx4 v203, s[40:41] offset:0\n" \
"s_sleep 3\n" \
"global_load_lds_dwordx4 v203, s[40:41] offset:1024\n" \
"s_sleep 3\n" \
"global_load_lds_dwordx4 v203, s[40:41] offset:2048\n" \
"s_sleep 3\n" \
"global_load_lds_dwordx4 v203, s[40:41] offset:3072\n" \
"s_sleep 3\n" \
"s_add_u32 m0, s57, 8192\n" \
"s_nop 0\n" \
"global_load_lds_dwordx4 v204, s[40:41] offset:0\n" \
"s_sleep 3\n" \
"s_add_u32 s40, s40, 36864\n" \
"s_addc_u32 s41, s41, 0\n" \
"ds_read_b64_tr_b16 a[4:5], v212 offset:36864\n" \
"ds_read_b64_tr_b16 a[6:7], v212 offset:55296\n" \
"ds_read_b64_tr_b16 a[36:37], v212 offset:36896\n" \
"ds_read_b64_tr_b16 a[38:39], v212 offset:55328\n" \
"ds_read_b64_tr_b16 a[68:69], v212 offset:36928\n" \
"ds_read_b64_tr_b16 a[70:71], v212 offset:55360\n" \
"ds_read_b64_tr_b16 a[100:101], v212 offset:36960\n" \
"ds_read_b64_tr_b16 a[102:103], v212 offset:55392\n" \
"ds_read_b64_tr_b16 a[132:133], v212 offset:36992\n" \
"ds_read_b64_tr_b16 a[134:135], v212 offset:55424\n" \
"s_waitcnt lgkmcnt(4)\n" \
"s_waitcnt lgkmcnt(0)\n" \
"s_waitcnt vmcnt(18)\n" \
"s_barrier\n" \
"s_add_u32 m0, s57, 36864\n" \
"s_nop 0\n" \
"global_load_lds_dwordx4 v202, s[40:41] offset:0\n" \
"s_sleep 3\n" \
"global_load_lds_dwordx4 v202, s[40:41] offset:1024\n" \
"s_sleep 3\n" \
"global_load_lds_dwordx4 v202, s[40:41] offset:2048\n" \
"s_sleep 3\n" \
"global_load_lds_dwordx4 v202, s[40:41] offset:3072\n" \
"s_sleep 3\n" \
"s_add_u32 m0, s57, 40960\n" \
"s_nop 0\n" \
"global_load_lds_dwordx4 v203, s[40:41] offset:0\n" \
"s_sleep 3\n" \
"global_load_lds_dwordx4 v203, s[40:41] offset:1024\n" \
"s_sleep 3\n" \
"global_load_lds_dwordx4 v203, s[40:41] offset:2048\n" \
"s_sleep 3\n" \
"global_load_lds_dwordx4 v203, s[40:41] offset:3072\n" \
"s_sleep 3\n" \
"s_add_u32 m0, s57, 45056\n" \
"s_nop 0\n" \
"global_load_lds_dwordx4 v204, s[40:41] offset:0\n" \
"s_sleep 3\n" \
"s_add_u32 s40, s40, 36864\n" \
"s_addc_u32 s41, s41, 0\n" \
"ds_read_b64_tr_b16 a[8:9], v213 offset:8192\n" \
"ds_read_b64_tr_b16 a[10:11], v213 offset:26624\n" \
"ds_read_b64_tr_b16 a[40:41], v213 offset:8224\n" \
"ds_read_b64_tr_b16 a[42:43], v213 offset:26656\n" \
"ds_read_b64_tr_b16 a[72:73], v213 offset:8256\n" \
"ds_read_b64_tr_b16 a[74:75], v213 offset:26688\n" \
"ds_read_b64_tr_b16 a[104:105], v213 offset:8288\n" \
"ds_read_b64_tr_b16 a[106:107], v213 offset:26720\n" \
"ds_read_b64_tr_b16 a[136:137], v213 offset:8320\n" \
"ds_read_b64_tr_b16 a[138:139], v213 offset:26752\n" \
"s_waitcnt lgkmcnt(4)\n" \
"s_waitcnt lgkmcnt(0)\n" \
"s_waitcnt vmcnt(18)\n" \
"s_barrier\n" \
"s_add_u32 m0, s57, 73728\n" \
"s_nop 0\n" \
"global_load_lds_dwordx4 v202, s[40:41] offset:0\n" \
"s_sleep 3\n" \
"global_load_lds_dwordx4 v202, s[40:41] offset:1024\n" \
"s_sleep 3\n" \
"global_load_lds_dwordx4 v202, s[40:41] offset:2048\n" \
"s_sleep 3\n" \
"global_load_lds_dwordx4 v202, s[40:41] offset:3072\n" \
"s_sleep 3\n" \
"s_add_u32 m0, s57, 77824\n" \
"s_nop 0\n" \
"global_load_lds_dwordx4 v203, s[40:41] offset:0\n" \
"s_sleep 3\n" \
"global_load_lds_dwordx4 v203, s[40:41] offset:1024\n" \
"s_sleep 3\n" \
"global_load_lds_dwordx4 v203, s[40:41] offset:2048\n" \
"s_sleep 3\n" \
"global_load_lds_dwordx4 v203, s[40:41] offset:3072\n" \
"s_sleep 3\n" \
"s_add_u32 m0, s57, 81920\n" \
"s_nop 0\n" \
"global_load_lds_dwordx4 v204, s[40:41] offset:0\n" \
"s_sleep 3\n" \
"s_add_u32 s40, s40, 36864\n" \
"s_addc_u32 s41, s41, 0\n" \
"ds_read_b64_tr_b16 a[12:13], v213 offset:45056\n" \
"ds_read_b64_tr_b16 a[14:15], v213 offset:63488\n" \
"ds_read_b64_tr_b16 a[44:45], v213 offset:45088\n" \
"ds_read_b64_tr_b16 a[46:47], v213 offset:63520\n" \
"ds_read_b64_tr_b16 a[76:77], v213 offset:45120\n" \
"ds_read_b64_tr_b16 a[78:79], v213 offset:63552\n" \
"ds_read_b64_tr_b16 a[108:109], v213 offset:45152\n" \
"ds_read_b64_tr_b16 a[110:111], v213 offset:63584\n" \
"ds_read_b64_tr_b16 a[140:141], v213 offset:45184\n" \
"ds_read_b64_tr_b16 a[142:143], v213 offset:63616\n" \
"s_waitcnt lgkmcnt(4)\n" \
"s_waitcnt lgkmcnt(0)\n" \
"s_waitcnt vmcnt(18)\n" \
"s_barrier\n" \
"s_add_u32 m0, s57, 110592\n" \
"s_nop 0\n" \
"global_load_lds_dwordx4 v202, s[40:41] offset:0\n" \
"s_sleep 3\n" \
"global_load_lds_dwordx4 v202, s[40:41] offset:1024\n" \
"s_sleep 3\n" \
"global_load_lds_dwordx4 v202, s[40:41] offset:2048\n" \
"s_sleep 3\n" \
"global_load_lds_dwordx4 v202, s[40:41] offset:3072\n" \
"s_sleep 3\n" \
"s_add_u32 m0, s57, 114688\n" \
"s_nop 0\n" \
"global_load_lds_dwordx4 v203, s[40:41] offset:0\n" \
"s_sleep 3\n" \
"global_load_lds_dwordx4 v203, s[40:41] offset:1024\n" \
"s_sleep 3\n" \
"global_load_lds_dwordx4 v203, s[40:41] offset:2048\n" \
"s_sleep 3\n" \
"global_load_lds_dwordx4 v203, s[40:41] offset:3072\n" \
"s_sleep 3\n" \
"s_add_u32 m0, s57, 118784\n" \
"s_nop 0\n" \
"global_load_lds_dwordx4 v204, s[40:41] offset:0\n" \
"s_sleep 3\n" \
"s_add_u32 s40, s40, 36864\n" \
"s_addc_u32 s41, s41, 0\n" \
"ds_read_b64_tr_b16 a[16:17], v212 offset:0\n" \
"ds_read_b64_tr_b16 a[18:19], v212 offset:18432\n" \
"ds_read_b64_tr_b16 a[48:49], v212 offset:32\n" \
"ds_read_b64_tr_b16 a[50:51], v212 offset:18464\n" \
"ds_read_b64_tr_b16 a[80:81], v212 offset:64\n" \
"ds_read_b64_tr_b16 a[82:83], v212 offset:18496\n" \
"ds_read_b64_tr_b16 a[112:113], v212 offset:96\n" \
"ds_read_b64_tr_b16 a[114:115], v212 offset:18528\n" \
"ds_read_b64_tr_b16 a[144:145], v212 offset:128\n" \
"ds_read_b64_tr_b16 a[146:147], v212 offset:18560\n" \
"s_waitcnt lgkmcnt(4)\n" \
"s_waitcnt lgkmcnt(0)\n" \
"s_waitcnt vmcnt(18)\n" \
"s_barrier\n" \
"ds_read_b64_tr_b16 a[20:21], v212 offset:36864\n" \
"ds_read_b64_tr_b16 a[22:23], v212 offset:55296\n" \
"ds_read_b64_tr_b16 a[52:53], v212 offset:36896\n" \
"ds_read_b64_tr_b16 a[54:55], v212 offset:55328\n" \
"ds_read_b64_tr_b16 a[84:85], v212 offset:36928\n" \
"ds_read_b64_tr_b16 a[86:87], v212 offset:55360\n" \
"ds_read_b64_tr_b16 a[116:117], v212 offset:36960\n" \
"ds_read_b64_tr_b16 a[118:119], v212 offset:55392\n" \
"ds_read_b64_tr_b16 a[148:149], v212 offset:36992\n" \
"ds_read_b64_tr_b16 a[150:151], v212 offset:55424\n" \
"s_waitcnt lgkmcnt(4)\n" \
"s_waitcnt lgkmcnt(0)\n" \
"s_waitcnt vmcnt(9)\n" \
"s_barrier\n" \
"ds_read_b64_tr_b16 a[24:25], v213 offset:8192\n" \
"ds_read_b64_tr_b16 a[26:27], v213 offset:26624\n" \
"ds_read_b64_tr_b16 a[56:57], v213 offset:8224\n" \
"ds_read_b64_tr_b16 a[58:59], v213 offset:26656\n" \
"ds_read_b64_tr_b16 a[88:89], v213 offset:8256\n" \
"ds_read_b64_tr_b16 a[90:91], v213 offset:26688\n" \
"ds_read_b64_tr_b16 a[120:121], v213 offset:8288\n" \
"ds_read_b64_tr_b16 a[122:123], v213 offset:26720\n" \
"ds_read_b64_tr_b16 a[152:153], v213 offset:8320\n" \
"ds_read_b64_tr_b16 a[154:155], v213 offset:26752\n" \
"s_waitcnt lgkmcnt(4)\n" \
"s_waitcnt lgkmcnt(0)\n" \
"s_waitcnt vmcnt(0)\n" \
"s_barrier\n" \
"ds_read_b64_tr_b16 a[28:29], v213 offset:45056\n" \
"ds_read_b64_tr_b16 a[30:31], v213 offset:63488\n" \
"ds_read_b64_tr_b16 a[60:61], v213 offset:45088\n" \
"ds_read_b64_tr_b16 a[62:63], v213 offset:63520\n" \
"ds_read_b64_tr_b16 a[92:93], v213 offset:45120\n" \
"ds_read_b64_tr_b16 a[94:95], v213 offset:63552\n" \
"ds_read_b64_tr_b16 a[124:125], v213 offset:45152\n" \
"ds_read_b64_tr_b16 a[126:127], v213 offset:63584\n" \
"ds_read_b64_tr_b16 a[156:157], v213 offset:45184\n" \
"ds_read_b64_tr_b16 a[158:159], v213 offset:63616\n" \
"s_waitcnt lgkmcnt(4)\n" \
"s_waitcnt lgkmcnt(0)\n" \
"s_barrier\n" \
"v_mov_b32 v214, v201\n" \
"v_add_u32 v215, 4096, v201\n" \
"v_add_u32 v212, 4096, v209\n" \
"v_add_u32 v213, 8192, v209\n" \
"v_add_u32 v200, 12288, v209\n" \
"v_mov_b32 v0, -1\n" \
"v_mov_b32 v1, -1\n" \
"v_mov_b32 v2, -1\n" \
"v_mov_b32 v3, -1\n" \
"s_mov_b32 s61, 0\n" \
"v_mov_b32 v169, 0\n" \
"global_load_dword v168, v169, s[52:53] sc1\n" \
"s_waitcnt vmcnt(0)\n" \
".Larm_c%=:\n" \
"v_cmp_gt_u32 vcc, 1, v168\n" \
"s_cbranch_vccnz .Larm_s%=\n" \
"s_add_u32 s58, s58, 1\n" \
"v_min_u32 v169, 68, v207\n" \
"v_add_u32 v169, 10240, v169\n" \
"global_load_dword v168, v169, s[54:55] sc1\n" \
"s_waitcnt vmcnt(0)\n" \
".Lrdy_c%=:\n" \
"v_cmp_gt_u32 vcc, 1, v168\n" \
"s_cbranch_vccnz .Lrdy_s%=\n" \
"v_min_u32_dpp v168, v168, v168 row_shr:1 row_mask:0xf bank_mask:0xf\n" \
"s_nop 1\n" \
"v_min_u32_dpp v168, v168, v168 row_shr:2 row_mask:0xf bank_mask:0xf\n" \
"s_nop 1\n" \
"v_min_u32_dpp v168, v168, v168 row_shr:4 row_mask:0xf bank_mask:0xf\n" \
"s_nop 1\n" \
"v_min_u32_dpp v168, v168, v168 row_shr:8 row_mask:0xf bank_mask:0xf\n" \
"s_nop 0\n" \
"v_readlane_b32 s76, v168, 15\n" \
"v_readlane_b32 s77, v168, 31\n" \
"s_min_u32 s76, s76, s77\n" \
"s_and_b32 s76, s76, 1\n" \
"s_lshl_b32 s76, s76, 13\n" \
"s_xor_b32 s42, s42, s76\n" \
"s_xor_b32 s44, s44, s76\n" \
"s_xor_b32 s46, s46, s76\n" \
"s_mov_b64 s[50:51], s[44:45]\n" \
"s_xor_b32 s44, s44, 0x2000\n" \
"global_load_dword v168, v206, s[54:55] sc1\n" \
"s_waitcnt vmcnt(0)\n" \
".Lp0_c%=:\n" \
"v_cmp_gt_u32 vcc, s58, v168\n" \
"s_cbranch_vccnz .Lp0_s%=\n" \
"global_load_dwordx4 v[0:3], v201, s[42:43] offset:-4096 sc1\n" \
"global_load_dwordx4 v[4:7], v201, s[42:43] offset:-2048 sc1\n" \
"global_load_dwordx4 v[8:11], v201, s[42:43] offset:0 sc1\n" \
"global_load_dwordx4 v[12:15], v201, s[42:43] offset:2048 sc1\n" \
"s_add_u32 m0, s56, 0\n" \
"s_nop 0\n" \
"global_load_lds_dwordx4 v201, s[44:45] offset:0 sc1\n" \
"global_load_lds_dwordx4 v201, s[44:45] offset:1024 sc1\n" \
"global_load_lds_dwordx4 v201, s[44:45] offset:2048 sc1\n" \
"global_load_lds_dwordx4 v201, s[44:45] offset:3072 sc1\n" \
"s_add_u32 m0, s56, 4096\n" \
"s_nop 0\n" \
"global_load_lds_dwordx4 v215, s[44:45] offset:0 sc1\n" \
"global_load_lds_dwordx4 v215, s[44:45] offset:1024 sc1\n" \
"global_load_lds_dwordx4 v215, s[44:45] offset:2048 sc1\n" \
"global_load_lds_dwordx4 v215, s[44:45] offset:3072 sc1\n" \
"s_add_u32 s42, s42, 32768\n" \
"s_addc_u32 s43, s43, 0\n" \
"s_add_u32 s44, s44, 65536\n" \
"s_addc_u32 s45, s45, 0\n" \
"s_xor_b32 s50, s44, 0x2000\n" \
"s_mov_b32 s51, s45\n" \
"global_load_dword v203, v205, s[54:55] sc1\n" \
"s_waitcnt vmcnt(0)\n" \
".Lpg_c%=:\n" \
"v_cmp_gt_u32 vcc, 1, v203\n" \
"s_cbranch_vccnz .Lpg_s%=\n" \
"v_mov_b32 v204, 12288\n" \
"s_mov_b32 s94, 0\n" \
".Lsp%=:\n" \
"v_readfirstlane_b32 s69, v203\n" \
"s_cmp_eq_u32 63, 64\n" \
"s_cselect_b32 s74, s68, s67\n" \
"s_and_b32 s72, s69, 0xffff\n" \
"s_sub_i32 s72, 63, s72\n" \
"s_add_i32 s72, s72, 1\n" \
"s_cmp_lt_i32 s72, 0\n" \
"s_cselect_b32 s91, 0, 0x4000000\n" \
"s_mul_i32 s72, s72, s66\n" \
"s_add_u32 s69, s69, s74\n" \
"s_add_u32 s69, s69, s72\n" \
"s_cmp_le_i32 s72, 0\n" \
"s_cbranch_scc1 .Lsq%=\n" \
"s_sub_u32 s73, s73, 1\n" \
"s_cbranch_scc1 .Lend%=\n" \
"s_sub_u32 s69, s69, s67\n" \
"s_sub_u32 s69, s69, 1310720\n" \
"s_mov_b32 s74, 40\n" \
".Lsr%=:\n" \
"s_sleep 5\n" \
"s_memrealtime s[92:93]\n" \
"s_waitcnt lgkmcnt(0)\n" \
"s_lshl_b32 s92, s92, 16\n" \
"s_sub_i32 s92, s69, s92\n" \
"s_sub_u32 s74, s74, 1\n" \
"s_cselect_b32 s92, 0, s92\n" \
"s_cmp_gt_i32 s92, 0\n" \
"s_cbranch_scc1 .Lsr%=\n" \
"global_load_dword v203, v205, s[54:55] sc1\n" \
"s_waitcnt vmcnt(0)\n" \
"s_branch .Lsp%=\n" \
".Lsq%=:\n" \
".Ltop%=:\n" \
"s_barrier\n" \
"ds_read_b128 v[16:19], v208 offset:0\n" \
"ds_read_b128 v[20:23], v208 offset:1024\n" \
"ds_read_b128 v[24:27], v208 offset:4096\n" \
"ds_read_b128 v[28:31], v208 offset:5120\n" \
"ds_read_b128 v[32:35], v208 offset:8192\n" \
"ds_read_b128 v[36:39], v208 offset:9216\n" \
"ds_read_b128 v[40:43], v208 offset:12288\n" \
"ds_read_b128 v[44:47], v208 offset:13312\n" \
"s_waitcnt lgkmcnt(7)\n" \
"v_mfma_f32_16x16x32_bf16 v[104:107], v[16:19], v[0:3], v[192:195]\n" \
"ds_read2_b64 v[48:51], v209 offset0:0 offset1:32\n" \
"s_waitcnt lgkmcnt(7)\n" \
"v_mfma_f32_16x16x32_bf16 v[108:111], v[20:23], v[0:3], v[196:199]\n" \
"ds_read2_b64 v[52:55], v209 offset0:128 offset1:160\n" \
"s_waitcnt lgkmcnt(7)\n" \
"v_mfma_f32_16x16x32_bf16 v[104:107], v[24:27], v[4:7], v[104:107]\n" \
"ds_read2_b64 v[56:59], v212 offset0:0 offset1:32\n" \
"s_waitcnt lgkmcnt(7)\n" \
"v_mfma_f32_16x16x32_bf16 v[108:111], v[28:31], v[4:7], v[108:111]\n" \
"ds_read2_b64 v[60:63], v212 offset0:128 offset1:160\n" \
"s_waitcnt lgkmcnt(7)\n" \
"v_mfma_f32_16x16x32_bf16 v[104:107], v[32:35], v[8:11], v[104:107]\n" \
"ds_read2_b64 v[64:67], v213 offset0:0 offset1:32\n" \
"s_waitcnt lgkmcnt(7)\n" \
"v_mfma_f32_16x16x32_bf16 v[108:111], v[36:39], v[8:11], v[108:111]\n" \
"ds_read2_b64 v[68:71], v213 offset0:128 offset1:160\n" \
"s_waitcnt lgkmcnt(7)\n" \
"v_mfma_f32_16x16x32_bf16 v[104:107], v[40:43], v[12:15], v[104:107]\n" \
"ds_read2_b64 v[72:75], v200 offset0:0 offset1:32\n" \
"s_waitcnt lgkmcnt(7)\n" \
"v_mfma_f32_16x16x32_bf16 v[108:111], v[44:47], v[12:15], v[108:111]\n" \
"ds_read2_b64 v[76:79], v200 offset0:128 offset1:160\n" \
"s_nop 3\n" \
"v_max3_f32 v168, v104, v105, v106\n" \
"s_nop 1\n" \
"v_max3_f32 v168, v168, v107, v108\n" \
"v_max3_f32 v168, v168, v109, v110\n" \
"v_max_f32 v168, v168, v111\n" \
"v_mov_b32 v173, 0\n" \
"v_mov_b32 v174, 0\n" \
"s_cmp_eq_u32 s94, 0\n" \
"s_cbranch_scc0 .Lkm_p%=\n" \
"v_readfirstlane_b32 s69, v203\n" \
"s_cmp_eq_u32 s58, 64\n" \
"s_cselect_b32 s74, s68, s67\n" \
"s_and_b32 s72, s69, 0xffff\n" \
"s_sub_i32 s72, s58, s72\n" \
"s_add_i32 s72, s72, 1\n" \
"s_cmp_lt_i32 s72, 0\n" \
"s_cselect_b32 s91, 0, 0x4000000\n" \
"s_mul_i32 s72, s72, s66\n" \
"s_add_u32 s69, s69, s74\n" \
"s_add_u32 s69, s69, s72\n" \
"s_memrealtime s[92:93]\n" \
"s_waitcnt lgkmcnt(0)\n" \
"s_lshl_b32 s92, s92, 16\n" \
"s_sub_i32 s92, s69, s92\n" \
"s_cmp_ge_u32 s92, s91\n" \
"s_cbranch_scc1 .Lkm_l%=\n" \
"s_cmp_lt_u32 s92, 3276800\n" \
"s_cbranch_scc1 .Lkm_w%=\n" \
".Lkm_w%=:\n" \
"s_memrealtime s[92:93]\n" \
"s_waitcnt lgkmcnt(0)\n" \
"s_lshl_b32 s92, s92, 16\n" \
"s_sub_i32 s92, s69, s92\n" \
"s_cmp_ge_u32 s92, s91\n" \
"s_cbranch_scc0 .Lkm_w%=\n" \
".Lkm_l%=:\n" \
"global_load_dwordx4 v[176:179], v210, s[46:47] offset:-4096 sc1\n" \
"global_load_dwordx4 v[180:183], v210, s[46:47] offset:-2048 sc1\n" \
"global_load_dwordx4 v[184:187], v210, s[46:47] offset:0 sc1\n" \
"global_load_dwordx4 v[188:191], v210, s[46:47] offset:2048 sc1\n" \
"global_load_dword v216, v207, s[46:47] offset:-3072 sc1\n" \
"global_load_dword v217, v207, s[46:47] offset:-2816 sc1\n" \
"global_load_dword v218, v207, s[46:47] offset:-1024 sc1\n" \
"global_load_dword v219, v207, s[46:47] offset:-768 sc1\n" \
"global_load_dword v220, v207, s[46:47] offset:1024 sc1\n" \
"global_load_dword v221, v207, s[46:47] offset:1280 sc1\n" \
"global_load_dword v222, v207, s[46:47] offset:3072 sc1\n" \
"global_load_dword v223, v207, s[46:47] offset:3328 sc1\n" \
"global_load_dword v202, v206, s[54:55] sc1\n" \
"global_load_dword v203, v205, s[54:55] sc1\n" \
"s_cmp_eq_u32 s61, 0\n" \
"s_cbranch_scc0 .Lkm_u%=\n" \
"s_waitcnt vmcnt(2)\n" \
".Lkm_c%=:\n" \
"v_mfma_f32_16x16x32_bf16 v[112:115], v[176:179], v[0:3], 0\n" \
"v_max_u32 v169, v176, v177\n" \
"v_max3_u32 v169, v169, v178, v179\n" \
"v_max3_u32 v169, v169, v180, v181\n" \
"v_mfma_f32_16x16x32_bf16 v[112:115], v[180:183], v[4:7], v[112:115]\n" \
"v_max3_u32 v169, v169, v182, v183\n" \
"v_max3_u32 v169, v169, v184, v185\n" \
"v_max3_u32 v169, v169, v186, v187\n" \
"v_mfma_f32_16x16x32_bf16 v[112:115], v[184:187], v[8:11], v[112:115]\n" \
"v_max3_u32 v169, v169, v188, v189\n" \
"v_max3_u32 v169, v169, v190, v191\n" \
"v_max3_u32 v169, v169, v216, v217\n" \
"v_mfma_f32_16x16x32_bf16 v[112:115], v[188:191], v[12:15], v[112:115]\n" \
"v_max3_u32 v169, v169, v218, v219\n" \
"v_max3_u32 v169, v169, v220, v221\n" \
"v_max3_u32 v169, v169, v222, v223\n" \
"v_cmp_eq_u32 vcc, -1, v169\n" \
"s_nop 4\n" \
"s_cbranch_vccnz .Lkm_s%=\n" \
"s_add_u32 s46, s46, 32768\n" \
"s_addc_u32 s47, s47, 0\n" \
"v_max3_f32 v168, v168, v112, v113\n" \
"v_mov_b32 v169, v168\n" \
"s_nop 1\n" \
"v_permlane16_swap_b32 v168, v169\n" \
"v_max_f32 v170, v168, v169\n" \
"v_max_f32 v168, v168, v169\n" \
"s_nop 1\n" \
"v_permlane32_swap_b32 v168, v170\n" \
"v_max_f32 v168, v168, v170\n" \
"v_mul_f32 v168, s75, v168\n" \
"v_fma_f32 v104, v104, s75, -v168\n" \
"v_fma_f32 v105, v105, s75, -v168\n" \
"v_fma_f32 v106, v106, s75, -v168\n" \
"v_fma_f32 v107, v107, s75, -v168\n" \
"v_fma_f32 v108, v108, s75, -v168\n" \
"v_fma_f32 v109, v109, s75, -v168\n" \
"v_fma_f32 v110, v110, s75, -v168\n" \
"v_fma_f32 v111, v111, s75, -v168\n" \
"v_fma_f32 v112, v112, s75, -v168\n" \
"v_fma_f32 v113, v113, s75, -v168\n" \
"v_exp_f32 v104, v104\n" \
"v_exp_f32 v105, v105\n" \
"v_exp_f32 v106, v106\n" \
"v_exp_f32 v107, v107\n" \
"v_exp_f32 v108, v108\n" \
"v_exp_f32 v109, v109\n" \
"v_exp_f32 v110, v110\n" \
"v_exp_f32 v111, v111\n" \
"v_exp_f32 v112, v112\n" \
"v_exp_f32 v113, v113\n" \
"v_add_f32 v168, v104, v105\n" \
"v_add_f32 v168, v168, v106\n" \
"v_add_f32 v168, v168, v107\n" \
"v_add_f32 v168, v168, v108\n" \
"v_add_f32 v168, v168, v109\n" \
"v_add_f32 v168, v168, v110\n" \
"v_add_f32 v168, v168, v111\n" \
"v_add_f32 v168, v168, v112\n" \
"v_add_f32 v168, v168, v113\n" \
"v_mov_b32 v169, v168\n" \
"s_nop 1\n" \
"v_permlane16_swap_b32 v168, v169\n" \
"v_add_f32 v170, v168, v169\n" \
"v_add_f32 v168, v168, v169\n" \
"s_nop 1\n" \
"v_permlane32_swap_b32 v168, v170\n" \
"v_add_f32 v168, v168, v170\n" \
"v_rcp_f32 v168, v168\n" \
"s_nop 0\n" \
"v_mul_f32 v104, v168, v104\n" \
"v_mul_f32 v105, v168, v105\n" \
"v_mul_f32 v106, v168, v106\n" \
"v_mul_f32 v107, v168, v107\n" \
"v_mul_f32 v108, v168, v108\n" \
"v_mul_f32 v109, v168, v109\n" \
"v_mul_f32 v110, v168, v110\n" \
"v_mul_f32 v111, v168, v111\n" \
"v_mul_f32 v112, v168, v112\n" \
"v_mul_f32 v113, v168, v113\n" \
"v_cvt_pk_bf16_f32 v116, v104, v105\n" \
"v_cvt_pk_bf16_f32 v117, v108, v109\n" \
"v_cvt_pk_bf16_f32 v118, v106, v107\n" \
"v_cvt_pk_bf16_f32 v119, v110, v111\n" \
"v_cvt_pk_bf16_f32 v172, v112, v113\n" \
"v_cvt_pk_bf16_f32 v175, v112, v113\n" \
"v_mfma_f32_16x16x32_bf16 v[120:123], v[48:51], v[116:119], 0\n" \
"v_mfma_f32_16x16x32_bf16 v[124:127], v[52:55], v[116:119], 0\n" \
"v_mfma_f32_16x16x16_bf16 v[120:123], v[216:217], v[172:173], v[120:123]\n" \
"v_mfma_f32_16x16x16_bf16 v[124:127], v[216:217], v[174:175], v[124:127]\n" \
"v_mfma_f32_16x16x32_bf16 v[128:131], v[56:59], v[116:119], 0\n" \
"v_mfma_f32_16x16x32_bf16 v[132:135], v[60:63], v[116:119], 0\n" \
"v_mfma_f32_16x16x16_bf16 v[128:131], v[218:219], v[172:173], v[128:131]\n" \
"v_mfma_f32_16x16x16_bf16 v[132:135], v[218:219], v[174:175], v[132:135]\n" \
"s_nop 2\n" \
"v_cvt_pk_bf16_f32 v120, v120, v121\n" \
"v_cvt_pk_bf16_f32 v121, v122, v123\n" \
"v_cvt_pk_bf16_f32 v122, v124, v125\n" \
"v_cvt_pk_bf16_f32 v123, v126, v127\n" \
"ds_write_b128 v211, v[120:123] offset:0\n" \
"v_mfma_f32_16x16x32_bf16 v[136:139], v[64:67], v[116:119], 0\n" \
"v_mfma_f32_16x16x32_bf16 v[140:143], v[68:71], v[116:119], 0\n" \
"v_mfma_f32_16x16x16_bf16 v[136:139], v[220:221], v[172:173], v[136:139]\n" \
"v_mfma_f32_16x16x16_bf16 v[140:143], v[220:221], v[174:175], v[140:143]\n" \
"v_cvt_pk_bf16_f32 v128, v128, v129\n" \
"v_cvt_pk_bf16_f32 v129, v130, v131\n" \
"v_cvt_pk_bf16_f32 v130, v132, v133\n" \
"v_cvt_pk_bf16_f32 v131, v134, v135\n" \
"ds_write_b128 v211, v[128:131] offset:2048\n" \
"v_mfma_f32_16x16x32_bf16 v[144:147], v[72:75], v[116:119], 0\n" \
"v_mfma_f32_16x16x32_bf16 v[148:151], v[76:79], v[116:119], 0\n" \
"v_mfma_f32_16x16x16_bf16 v[144:147], v[222:223], v[172:173], v[144:147]\n" \
"v_mfma_f32_16x16x16_bf16 v[148:151], v[222:223], v[174:175], v[148:151]\n" \
"v_cvt_pk_bf16_f32 v136, v136, v137\n" \
"v_cvt_pk_bf16_f32 v137, v138, v139\n" \
"v_cvt_pk_bf16_f32 v138, v140, v141\n" \
"v_cvt_pk_bf16_f32 v139, v142, v143\n" \
"ds_write_b128 v211, v[136:139] offset:4096\n" \
"s_nop 1\n" \
"v_cvt_pk_bf16_f32 v144, v144, v145\n" \
"v_cvt_pk_bf16_f32 v145, v146, v147\n" \
"v_cvt_pk_bf16_f32 v146, v148, v149\n" \
"v_cvt_pk_bf16_f32 v147, v150, v151\n" \
"ds_write_b128 v211, v[144:147] offset:6144\n" \
"s_add_u32 s59, s58, 2\n" \
"s_cmp_gt_u32 s59, 63\n" \
"s_cselect_b32 s60, 0, s59\n" \
"s_cselect_b64 s[64:65], 0, -1\n" \
"s_waitcnt lgkmcnt(0)\n" \
"s_barrier\n" \
"ds_read_b128 v[80:83], v214 offset:0\n" \
"ds_read_b128 v[84:87], v214 offset:1024\n" \
"ds_read_b128 v[88:91], v214 offset:2048\n" \
"ds_read_b128 v[92:95], v214 offset:3072\n" \
"ds_read_b128 v[96:99], v214 offset:4096\n" \
"ds_read_b128 v[100:103], v214 offset:5120\n" \
"ds_read_b128 v[104:107], v214 offset:6144\n" \
"ds_read_b128 v[108:111], v214 offset:7168\n" \
"ds_read_b128 v[112:115], v214 offset:8192\n" \
"ds_read_b128 v[116:119], v214 offset:9216\n" \
"ds_read_b128 v[120:123], v214 offset:10240\n" \
"ds_read_b128 v[124:127], v214 offset:11264\n" \
"ds_read_b128 v[128:131], v214 offset:12288\n" \
"ds_read_b128 v[132:135], v214 offset:13312\n" \
"ds_read_b128 v[136:139], v214 offset:14336\n" \
"s_waitcnt lgkmcnt(14)\n" \
"ds_read_b128 v[140:143], v214 offset:15360\n" \
"v_xor_b32 v211, 0x4000, v211\n" \
"v_xor_b32 v214, 0x4000, v214\n" \
"s_waitcnt vmcnt(0)\n" \
".Lp1_c%=:\n" \
"v_cmp_gt_u32 vcc, s60, v202\n" \
"s_cbranch_vccnz .Lp1_s%=\n" \
"s_waitcnt lgkmcnt(15)\n" \
"v_mfma_f32_16x16x32_bf16 v[144:147], a[0:3], v[80:83], 0\n" \
"s_waitcnt lgkmcnt(14)\n" \
"v_mfma_f32_16x16x32_bf16 v[148:151], a[0:3], v[84:87], 0\n" \
"s_waitcnt lgkmcnt(13)\n" \
"v_mfma_f32_16x16x32_bf16 v[144:147], a[4:7], v[88:91], v[144:147]\n" \
"s_waitcnt lgkmcnt(12)\n" \
"v_mfma_f32_16x16x32_bf16 v[148:151], a[4:7], v[92:95], v[148:151]\n" \
"s_waitcnt lgkmcnt(11)\n" \
"v_mfma_f32_16x16x32_bf16 v[144:147], a[8:11], v[96:99], v[144:147]\n" \
"s_waitcnt lgkmcnt(10)\n" \
"v_mfma_f32_16x16x32_bf16 v[148:151], a[8:11], v[100:103], v[148:151]\n" \
"s_waitcnt lgkmcnt(9)\n" \
"v_mfma_f32_16x16x32_bf16 v[144:147], a[12:15], v[104:107], v[144:147]\n" \
"s_waitcnt lgkmcnt(8)\n" \
"v_mfma_f32_16x16x32_bf16 v[148:151], a[12:15], v[108:111], v[148:151]\n" \
"s_waitcnt lgkmcnt(7)\n" \
"v_mfma_f32_16x16x32_bf16 v[144:147], a[16:19], v[112:115], v[144:147]\n" \
"s_waitcnt lgkmcnt(6)\n" \
"v_mfma_f32_16x16x32_bf16 v[148:151], a[16:19], v[116:119], v[148:151]\n" \
"s_waitcnt lgkmcnt(5)\n" \
"v_mfma_f32_16x16x32_bf16 v[144:147], a[20:23], v[120:123], v[144:147]\n" \
"s_waitcnt lgkmcnt(4)\n" \
"v_mfma_f32_16x16x32_bf16 v[148:151], a[20:23], v[124:127], v[148:151]\n" \
"s_waitcnt lgkmcnt(3)\n" \
"v_mfma_f32_16x16x32_bf16 v[144:147], a[24:27], v[128:131], v[144:147]\n" \
"s_waitcnt lgkmcnt(2)\n" \
"v_mfma_f32_16x16x32_bf16 v[148:151], a[24:27], v[132:135], v[148:151]\n" \
"s_waitcnt lgkmcnt(1)\n" \
"v_mfma_f32_16x16x32_bf16 v[144:147], a[28:31], v[136:139], v[144:147]\n" \
"s_waitcnt lgkmcnt(0)\n" \
"v_mfma_f32_16x16x32_bf16 v[148:151], a[28:31], v[140:143], v[148:151]\n" \
"v_mfma_f32_16x16x32_bf16 v[152:155], a[32:35], v[80:83], 0\n" \
"v_mfma_f32_16x16x32_bf16 v[156:159], a[32:35], v[84:87], 0\n" \
"v_mfma_f32_16x16x32_bf16 v[152:155], a[36:39], v[88:91], v[152:155]\n" \
"v_mfma_f32_16x16x32_bf16 v[156:159], a[36:39], v[92:95], v[156:159]\n" \
"s_nop 1\n" \
"v_cvt_pk_bf16_f32 v168, v144, v145\n" \
"v_mfma_f32_16x16x32_bf16 v[152:155], a[40:43], v[96:99], v[152:155]\n" \
"v_cvt_pk_bf16_f32 v169, v146, v147\n" \
"v_mfma_f32_16x16x32_bf16 v[156:159], a[40:43], v[100:103], v[156:159]\n" \
"v_cvt_pk_bf16_f32 v170, v148, v149\n" \
"v_mfma_f32_16x16x32_bf16 v[152:155], a[44:47], v[104:107], v[152:155]\n" \
"v_cvt_pk_bf16_f32 v171, v150, v151\n" \
"v_mfma_f32_16x16x32_bf16 v[156:159], a[44:47], v[108:111], v[156:159]\n" \
"global_store_dwordx4 v201, v[168:171], s[48:49] offset:-4096 sc1\n" \
"v_mfma_f32_16x16x32_bf16 v[152:155], a[48:51], v[112:115], v[152:155]\n" \
"v_mfma_f32_16x16x32_bf16 v[156:159], a[48:51], v[116:119], v[156:159]\n" \
"v_mfma_f32_16x16x32_bf16 v[152:155], a[52:55], v[120:123], v[152:155]\n" \
"v_mfma_f32_16x16x32_bf16 v[156:159], a[52:55], v[124:127], v[156:159]\n" \
"v_mfma_f32_16x16x32_bf16 v[152:155], a[56:59], v[128:131], v[152:155]\n" \
"v_mfma_f32_16x16x32_bf16 v[156:159], a[56:59], v[132:135], v[156:159]\n" \
"v_mfma_f32_16x16x32_bf16 v[152:155], a[60:63], v[136:139], v[152:155]\n" \
"v_mfma_f32_16x16x32_bf16 v[156:159], a[60:63], v[140:143], v[156:159]\n" \
"v_mfma_f32_16x16x32_bf16 v[160:163], a[64:67], v[80:83], 0\n" \
"v_mfma_f32_16x16x32_bf16 v[164:167], a[64:67], v[84:87], 0\n" \
"v_mfma_f32_16x16x32_bf16 v[160:163], a[68:71], v[88:91], v[160:163]\n" \
"v_mfma_f32_16x16x32_bf16 v[164:167], a[68:71], v[92:95], v[164:167]\n" \
"s_nop 2\n" \
"v_cvt_pk_bf16_f32 v172, v152, v153\n" \
"v_mfma_f32_16x16x32_bf16 v[160:163], a[72:75], v[96:99], v[160:163]\n" \
"v_cvt_pk_bf16_f32 v173, v154, v155\n" \
"v_mfma_f32_16x16x32_bf16 v[164:167], a[72:75], v[100:103], v[164:167]\n" \
"v_cvt_pk_bf16_f32 v174, v156, v157\n" \
"v_mfma_f32_16x16x32_bf16 v[160:163], a[76:79], v[104:107], v[160:163]\n" \
"v_cvt_pk_bf16_f32 v175, v158, v159\n" \
"v_mfma_f32_16x16x32_bf16 v[164:167], a[76:79], v[108:111], v[164:167]\n" \
"global_store_dwordx4 v201, v[172:175], s[48:49] offset:-3072 sc1\n" \
"v_mfma_f32_16x16x32_bf16 v[160:163], a[80:83], v[112:115], v[160:163]\n" \
"v_mfma_f32_16x16x32_bf16 v[164:167], a[80:83], v[116:119], v[164:167]\n" \
"v_mfma_f32_16x16x32_bf16 v[160:163], a[84:87], v[120:123], v[160:163]\n" \
"v_mfma_f32_16x16x32_bf16 v[164:167], a[84:87], v[124:127], v[164:167]\n" \
"v_mfma_f32_16x16x32_bf16 v[160:163], a[88:91], v[128:131], v[160:163]\n" \
"v_mfma_f32_16x16x32_bf16 v[164:167], a[88:91], v[132:135], v[164:167]\n" \
"v_mfma_f32_16x16x32_bf16 v[160:163], a[92:95], v[136:139], v[160:163]\n" \
"v_mfma_f32_16x16x32_bf16 v[164:167], a[92:95], v[140:143], v[164:167]\n" \
"v_mfma_f32_16x16x32_bf16 v[144:147], a[96:99], v[80:83], 0\n" \
"v_mfma_f32_16x16x32_bf16 v[148:151], a[96:99], v[84:87], 0\n" \
"v_mfma_f32_16x16x32_bf16 v[144:147], a[100:103], v[88:91], v[144:147]\n" \
"v_mfma_f32_16x16x32_bf16 v[148:151], a[100:103], v[92:95], v[148:151]\n" \
"s_nop 2\n" \
"v_cvt_pk_bf16_f32 v168, v160, v161\n" \
"v_mfma_f32_16x16x32_bf16 v[144:147], a[104:107], v[96:99], v[144:147]\n" \
"v_cvt_pk_bf16_f32 v169, v162, v163\n" \
"v_mfma_f32_16x16x32_bf16 v[148:151], a[104:107], v[100:103], v[148:151]\n" \
"v_cvt_pk_bf16_f32 v170, v164, v165\n" \
"v_mfma_f32_16x16x32_bf16 v[144:147], a[108:111], v[104:107], v[144:147]\n" \
"v_cvt_pk_bf16_f32 v171, v166, v167\n" \
"v_mfma_f32_16x16x32_bf16 v[148:151], a[108:111], v[108:111], v[148:151]\n" \
"global_store_dwordx4 v201, v[168:171], s[48:49] offset:-2048 sc1\n" \
"v_mfma_f32_16x16x32_bf16 v[144:147], a[112:115], v[112:115], v[144:147]\n" \
"v_mfma_f32_16x16x32_bf16 v[148:151], a[112:115], v[116:119], v[148:151]\n" \
"v_mfma_f32_16x16x32_bf16 v[144:147], a[116:119], v[120:123], v[144:147]\n" \
"v_mfma_f32_16x16x32_bf16 v[148:151], a[116:119], v[124:127], v[148:151]\n" \
"v_mfma_f32_16x16x32_bf16 v[144:147], a[120:123], v[128:131], v[144:147]\n" \
"v_mfma_f32_16x16x32_bf16 v[148:151], a[120:123], v[132:135], v[148:151]\n" \
"v_mfma_f32_16x16x32_bf16 v[144:147], a[124:127], v[136:139], v[144:147]\n" \
"v_mfma_f32_16x16x32_bf16 v[148:151], a[124:127], v[140:143], v[148:151]\n" \
"v_mfma_f32_16x16x32_bf16 v[152:155], a[128:131], v[80:83], 0\n" \
"v_mfma_f32_16x16x32_bf16 v[156:159], a[128:131], v[84:87], 0\n" \
"v_mfma_f32_16x16x32_bf16 v[152:155], a[132:135], v[88:91], v[152:155]\n" \
"v_mfma_f32_16x16x32_bf16 v[156:159], a[132:135], v[92:95], v[156:159]\n" \
"s_nop 2\n" \
"v_cvt_pk_bf16_f32 v172, v144, v145\n" \
"v_mfma_f32_16x16x32_bf16 v[152:155], a[136:139], v[96:99], v[152:155]\n" \
"v_cvt_pk_bf16_f32 v173, v146, v147\n" \
"v_mfma_f32_16x16x32_bf16 v[156:159], a[136:139], v[100:103], v[156:159]\n" \
"v_cvt_pk_bf16_f32 v174, v148, v149\n" \
"v_mfma_f32_16x16x32_bf16 v[152:155], a[140:143], v[104:107], v[152:155]\n" \
"v_cvt_pk_bf16_f32 v175, v150, v151\n" \
"v_mfma_f32_16x16x32_bf16 v[156:159], a[140:143], v[108:111], v[156:159]\n" \
"global_store_dwordx4 v201, v[172:175], s[48:49] offset:-1024 sc1\n" \
"v_mfma_f32_16x16x32_bf16 v[152:155], a[144:147], v[112:115], v[152:155]\n" \
"v_mfma_f32_16x16x32_bf16 v[156:159], a[144:147], v[116:119], v[156:159]\n" \
"v_mfma_f32_16x16x32_bf16 v[152:155], a[148:151], v[120:123], v[152:155]\n" \
"v_mfma_f32_16x16x32_bf16 v[156:159], a[148:151], v[124:127], v[156:159]\n" \
"v_mfma_f32_16x16x32_bf16 v[152:155], a[152:155], v[128:131], v[152:155]\n" \
"v_mfma_f32_16x16x32_bf16 v[156:159], a[152:155], v[132:135], v[156:159]\n" \
"v_mfma_f32_16x16x32_bf16 v[152:155], a[156:159], v[136:139], v[152:155]\n" \
"v_mfma_f32_16x16x32_bf16 v[156:159], a[156:159], v[140:143], v[156:159]\n" \
"s_nop 6\n" \
"v_cvt_pk_bf16_f32 v168, v152, v153\n" \
"v_cvt_pk_bf16_f32 v169, v154, v155\n" \
"v_cvt_pk_bf16_f32 v170, v156, v157\n" \
"v_cvt_pk_bf16_f32 v171, v158, v159\n" \
"global_store_dwordx4 v201, v[168:171], s[48:49] offset:0 sc1\n" \
"s_add_u32 s48, s48, 1179648\n" \
"s_addc_u32 s49, s49, 0\n" \
"s_cmp_eq_u32 s61, 0\n" \
"s_waitcnt vmcnt(7)\n" \
"s_cbranch_scc0 .Lnq%=\n" \
"s_waitcnt vmcnt(5)\n" \
".Lnq%=:\n" \
"s_mov_b32 s61, s58\n" \
"s_mov_b32 s58, s59\n" \
"s_cmp_le_u32 s59, 63\n" \
"s_cbranch_scc1 .Ltop%=\n" \
"s_waitcnt vmcnt(0)\n" \
".Lend%=:\n" \
"s_endpgm\n" \
".Larm_s%=:\n" \
"s_sub_u32 s73, s73, 1\n" \
"s_cbranch_scc1 .Lend%=\n" \
"s_sleep 4\n" \
"global_load_dword v168, v169, s[52:53] sc1\n" \
"s_waitcnt vmcnt(0)\n" \
"s_branch .Larm_c%=\n" \
".Lrdy_s%=:\n" \
"s_sub_u32 s73, s73, 1\n" \
"s_cbranch_scc1 .Lend%=\n" \
"s_sleep 4\n" \
"global_load_dword v168, v169, s[54:55] sc1\n" \
"s_waitcnt vmcnt(0)\n" \
"s_branch .Lrdy_c%=\n" \
".Lp0_s%=:\n" \
"s_sub_u32 s73, s73, 1\n" \
"s_cbranch_scc1 .Lend%=\n" \
"s_sleep 4\n" \
"global_load_dword v168, v206, s[54:55] sc1\n" \
"s_waitcnt vmcnt(0)\n" \
"s_branch .Lp0_c%=\n" \
".Lpg_s%=:\n" \
"s_sub_u32 s73, s73, 1\n" \
"s_cbranch_scc1 .Lend%=\n" \
"s_sleep 4\n" \
"global_load_dword v203, v205, s[54:55] sc1\n" \
"s_waitcnt vmcnt(0)\n" \
"s_branch .Lpg_c%=\n" \
".Lkm_s%=:\n" \
"s_sub_u32 s73, s73, 1\n" \
"s_cbranch_scc1 .Lend%=\n" \
"s_cmp_ge_u32 s58, 63\n" \
"s_cbranch_scc1 .Lkm_l%=\n" \
"global_load_dword v203, v205, s[54:55] sc1\n" \
"s_waitcnt vmcnt(0)\n" \
"v_readfirstlane_b32 s69, v203\n" \
"s_cmp_eq_u32 s58, 64\n" \
"s_cselect_b32 s74, s68, s67\n" \
"s_and_b32 s72, s69, 0xffff\n" \
"s_sub_i32 s72, s58, s72\n" \
"s_add_i32 s72, s72, 1\n" \
"s_cmp_lt_i32 s72, 0\n" \
"s_cselect_b32 s91, 0, 0x4000000\n" \
"s_mul_i32 s72, s72, s66\n" \
"s_add_u32 s69, s69, s74\n" \
"s_add_u32 s69, s69, s72\n" \
"s_branch .Lkm_w%=\n" \
".Lkm_u%=:\n" \
"s_waitcnt vmcnt(14)\n" \
"v_mov_b32 v169, s61\n" \
"s_mov_b64 exec, 1\n" \
"global_store_dword v207, v169, s[52:53] sc1\n" \
"global_store_dword v204, v203, s[52:53] sc1\n" \
"s_mov_b64 exec, -1\n" \
"s_mov_b32 s61, 0\n" \
"s_waitcnt vmcnt(4)\n" \
"s_branch .Lkm_c%=\n" \
".Lkm_p%=:\n" \
"global_load_dword v202, v206, s[54:55] sc1\n" \
"global_load_dword v203, v205, s[54:55] sc1\n" \
"s_waitcnt lgkmcnt(0)\n" \
"s_branch .Lkm_c%=\n" \
".Lp1_s%=:\n" \
"s_sub_u32 s73, s73, 1\n" \
"s_cbranch_scc1 .Lend%=\n" \
"s_sleep 4\n" \
"global_load_dword v202, v206, s[54:55] sc1\n" \
"s_waitcnt vmcnt(0)\n" \
"s_branch .Lp1_c%=\n"

#define ATB_ASM \
"s_mov_b32 s73, 2000000\n" \
"s_mov_b32 s75, 0x3e0293ee\n" \
"v_mov_b32 v212, v200\n" \
"v_add_u32 v213, 0x10000, v200\n" \
"v_add_u32 v214, 0x20000, v200\n" \
"v_mov_b32 v150, s65\n" \
"s_memrealtime s[76:77]\n" \
"s_waitcnt lgkmcnt(0)\n" \
".Ldly%=:\n" \
"s_memrealtime s[78:79]\n" \
"s_waitcnt lgkmcnt(0)\n" \
"s_sub_u32 s78, s78, s76\n" \
"s_cmp_lt_u32 s78, s64\n" \
"s_cbranch_scc1 .Ldly%=\n" \
"s_add_u32 m0, s57, 0\n" \
"s_nop 0\n" \
"global_load_lds_dwordx4 v202, s[40:41] offset:0\n" \
"s_sleep 3\n" \
"global_load_lds_dwordx4 v202, s[40:41] offset:1024\n" \
"s_sleep 3\n" \
"global_load_lds_dwordx4 v202, s[40:41] offset:2048\n" \
"s_sleep 3\n" \
"global_load_lds_dwordx4 v202, s[40:41] offset:3072\n" \
"s_sleep 3\n" \
"s_add_u32 m0, s57, 4096\n" \
"s_nop 0\n" \
"global_load_lds_dwordx4 v203, s[40:41] offset:0\n" \
"s_sleep 3\n" \
"global_load_lds_dwordx4 v203, s[40:41] offset:1024\n" \
"s_sleep 3\n" \
"global_load_lds_dwordx4 v203, s[40:41] offset:2048\n" \
"s_sleep 3\n" \
"global_load_lds_dwordx4 v203, s[40:41] offset:3072\n" \
"s_sleep 3\n" \
"s_add_u32 m0, s57, 8192\n" \
"s_nop 0\n" \
"global_load_lds_dwordx4 v204, s[40:41] offset:0\n" \
"s_sleep 3\n" \
"s_add_u32 s40, s40, 36864\n" \
"s_addc_u32 s41, s41, 0\n" \
"s_add_u32 m0, s57, 36864\n" \
"s_nop 0\n" \
"global_load_lds_dwordx4 v202, s[40:41] offset:0\n" \
"s_sleep 3\n" \
"global_load_lds_dwordx4 v202, s[40:41] offset:1024\n" \
"s_sleep 3\n" \
"global_load_lds_dwordx4 v202, s[40:41] offset:2048\n" \
"s_sleep 3\n" \
"global_load_lds_dwordx4 v202, s[40:41] offset:3072\n" \
"s_sleep 3\n" \
"s_add_u32 m0, s57, 40960\n" \
"s_nop 0\n" \
"global_load_lds_dwordx4 v203, s[40:41] offset:0\n" \
"s_sleep 3\n" \
"global_load_lds_dwordx4 v203, s[40:41] offset:1024\n" \
"s_sleep 3\n" \
"global_load_lds_dwordx4 v203, s[40:41] offset:2048\n" \
"s_sleep 3\n" \
"global_load_lds_dwordx4 v203, s[40:41] offset:3072\n" \
"s_sleep 3\n" \
"s_add_u32 m0, s57, 45056\n" \
"s_nop 0\n" \
"global_load_lds_dwordx4 v204, s[40:41] offset:0\n" \
"s_sleep 3\n" \
"s_add_u32 s40, s40, 36864\n" \
"s_addc_u32 s41, s41, 0\n" \
"s_add_u32 m0, s57, 73728\n" \
"s_nop 0\n" \
"global_load_lds_dwordx4 v202, s[40:41] offset:0\n" \
"s_sleep 3\n" \
"global_load_lds_dwordx4 v202, s[40:41] offset:1024\n" \
"s_sleep 3\n" \
"global_load_lds_dwordx4 v202, s[40:41] offset:2048\n" \
"s_sleep 3\n" \
"global_load_lds_dwordx4 v202, s[40:41] offset:3072\n" \
"s_sleep 3\n" \
"s_add_u32 m0, s57, 77824\n" \
"s_nop 0\n" \
"global_load_lds_dwordx4 v203, s[40:41] offset:0\n" \
"s_sleep 3\n" \
"global_load_lds_dwordx4 v203, s[40:41] offset:1024\n" \
"s_sleep 3\n" \
"global_load_lds_dwordx4 v203, s[40:41] offset:2048\n" \
"s_sleep 3\n" \
"global_load_lds_dwordx4 v203, s[40:41] offset:3072\n" \
"s_sleep 3\n" \
"s_add_u32 m0, s57, 81920\n" \
"s_nop 0\n" \
"global_load_lds_dwordx4 v204, s[40:41] offset:0\n" \
"s_sleep 3\n" \
"s_add_u32 s40, s40, 36864\n" \
"s_addc_u32 s41, s41, 0\n" \
"s_add_u32 m0, s57, 110592\n" \
"s_nop 0\n" \
"global_load_lds_dwordx4 v202, s[40:41] offset:0\n" \
"s_sleep 3\n" \
"global_load_lds_dwordx4 v202, s[40:41] offset:1024\n" \
"s_sleep 3\n" \
"global_load_lds_dwordx4 v202, s[40:41] offset:2048\n" \
"s_sleep 3\n" \
"global_load_lds_dwordx4 v202, s[40:41] offset:3072\n" \
"s_sleep 3\n" \
"s_add_u32 m0, s57, 114688\n" \
"s_nop 0\n" \
"global_load_lds_dwordx4 v203, s[40:41] offset:0\n" \
"s_sleep 3\n" \
"global_load_lds_dwordx4 v203, s[40:41] offset:1024\n" \
"s_sleep 3\n" \
"global_load_lds_dwordx4 v203, s[40:41] offset:2048\n" \
"s_sleep 3\n" \
"global_load_lds_dwordx4 v203, s[40:41] offset:3072\n" \
"s_sleep 3\n" \
"s_add_u32 m0, s57, 118784\n" \
"s_nop 0\n" \
"global_load_lds_dwordx4 v204, s[40:41] offset:0\n" \
"s_sleep 3\n" \
"s_add_u32 s40, s40, 36864\n" \
"s_addc_u32 s41, s41, 0\n" \
"s_waitcnt vmcnt(27)\n" \
"s_barrier\n" \
"ds_read_b64_tr_b16 a[160:161], v212 offset:160\n" \
"ds_read_b64_tr_b16 a[162:163], v212 offset:18592\n" \
"ds_read_b64_tr_b16 a[192:193], v212 offset:192\n" \
"ds_read_b64_tr_b16 a[194:195], v212 offset:18624\n" \
"ds_read_b64_tr_b16 a[224:225], v212 offset:224\n" \
"ds_read_b64_tr_b16 a[226:227], v212 offset:18656\n" \
"ds_read_b64_tr_b16 v[224:225], v212 offset:256\n" \
"ds_read_b64_tr_b16 v[226:227], v212 offset:18688\n" \
"s_waitcnt lgkmcnt(0)\n" \
"s_waitcnt vmcnt(18)\n" \
"s_barrier\n" \
"s_add_u32 m0, s57, 0\n" \
"s_nop 0\n" \
"global_load_lds_dwordx4 v202, s[40:41] offset:0\n" \
"s_sleep 3\n" \
"global_load_lds_dwordx4 v202, s[40:41] offset:1024\n" \
"s_sleep 3\n" \
"global_load_lds_dwordx4 v202, s[40:41] offset:2048\n" \
"s_sleep 3\n" \
"global_load_lds_dwordx4 v202, s[40:41] offset:3072\n" \
"s_sleep 3\n" \
"s_add_u32 m0, s57, 4096\n" \
"s_nop 0\n" \
"global_load_lds_dwordx4 v203, s[40:41] offset:0\n" \
"s_sleep 3\n" \
"global_load_lds_dwordx4 v203, s[40:41] offset:1024\n" \
"s_sleep 3\n" \
"global_load_lds_dwordx4 v203, s[40:41] offset:2048\n" \
"s_sleep 3\n" \
"global_load_lds_dwordx4 v203, s[40:41] offset:3072\n" \
"s_sleep 3\n" \
"s_add_u32 m0, s57, 8192\n" \
"s_nop 0\n" \
"global_load_lds_dwordx4 v204, s[40:41] offset:0\n" \
"s_sleep 3\n" \
"s_add_u32 s40, s40, 36864\n" \
"s_addc_u32 s41, s41, 0\n" \
"ds_read_b64_tr_b16 a[164:165], v212 offset:37024\n" \
"ds_read_b64_tr_b16 a[166:167], v212 offset:55456\n" \
"ds_read_b64_tr_b16 a[196:197], v212 offset:37056\n" \
"ds_read_b64_tr_b16 a[198:199], v212 offset:55488\n" \
"ds_read_b64_tr_b16 a[228:229], v212 offset:37088\n" \
"ds_read_b64_tr_b16 a[230:231], v212 offset:55520\n" \
"ds_read_b64_tr_b16 v[228:229], v212 offset:37120\n" \
"ds_read_b64_tr_b16 v[230:231], v212 offset:55552\n" \
"s_waitcnt lgkmcnt(0)\n" \
"s_waitcnt vmcnt(18)\n" \
"s_barrier\n" \
"s_add_u32 m0, s57, 36864\n" \
"s_nop 0\n" \
"global_load_lds_dwordx4 v202, s[40:41] offset:0\n" \
"s_sleep 3\n" \
"global_load_lds_dwordx4 v202, s[40:41] offset:1024\n" \
"s_sleep 3\n" \
"global_load_lds_dwordx4 v202, s[40:41] offset:2048\n" \
"s_sleep 3\n" \
"global_load_lds_dwordx4 v202, s[40:41] offset:3072\n" \
"s_sleep 3\n" \
"s_add_u32 m0, s57, 40960\n" \
"s_nop 0\n" \
"global_load_lds_dwordx4 v203, s[40:41] offset:0\n" \
"s_sleep 3\n" \
"global_load_lds_dwordx4 v203, s[40:41] offset:1024\n" \
"s_sleep 3\n" \
"global_load_lds_dwordx4 v203, s[40:41] offset:2048\n" \
"s_sleep 3\n" \
"global_load_lds_dwordx4 v203, s[40:41] offset:3072\n" \
"s_sleep 3\n" \
"s_add_u32 m0, s57, 45056\n" \
"s_nop 0\n" \
"global_load_lds_dwordx4 v204, s[40:41] offset:0\n" \
"s_sleep 3\n" \
"s_add_u32 s40, s40, 36864\n" \
"s_addc_u32 s41, s41, 0\n" \
"ds_read_b64_tr_b16 a[168:169], v213 offset:8352\n" \
"ds_read_b64_tr_b16 a[170:171], v213 offset:26784\n" \
"ds_read_b64_tr_b16 a[200:201], v213 offset:8384\n" \
"ds_read_b64_tr_b16 a[202:203], v213 offset:26816\n" \
"ds_read_b64_tr_b16 a[232:233], v213 offset:8416\n" \
"ds_read_b64_tr_b16 a[234:235], v213 offset:26848\n" \
"ds_read_b64_tr_b16 v[232:233], v213 offset:8448\n" \
"ds_read_b64_tr_b16 v[234:235], v213 offset:26880\n" \
"s_waitcnt lgkmcnt(0)\n" \
"s_waitcnt vmcnt(18)\n" \
"s_barrier\n" \
"s_add_u32 m0, s57, 73728\n" \
"s_nop 0\n" \
"global_load_lds_dwordx4 v202, s[40:41] offset:0\n" \
"s_sleep 3\n" \
"global_load_lds_dwordx4 v202, s[40:41] offset:1024\n" \
"s_sleep 3\n" \
"global_load_lds_dwordx4 v202, s[40:41] offset:2048\n" \
"s_sleep 3\n" \
"global_load_lds_dwordx4 v202, s[40:41] offset:3072\n" \
"s_sleep 3\n" \
"s_add_u32 m0, s57, 77824\n" \
"s_nop 0\n" \
"global_load_lds_dwordx4 v203, s[40:41] offset:0\n" \
"s_sleep 3\n" \
"global_load_lds_dwordx4 v203, s[40:41] offset:1024\n" \
"s_sleep 3\n" \
"global_load_lds_dwordx4 v203, s[40:41] offset:2048\n" \
"s_sleep 3\n" \
"global_load_lds_dwordx4 v203, s[40:41] offset:3072\n" \
"s_sleep 3\n" \
"s_add_u32 m0, s57, 81920\n" \
"s_nop 0\n" \
"global_load_lds_dwordx4 v204, s[40:41] offset:0\n" \
"s_sleep 3\n" \
"s_add_u32 s40, s40, 36864\n" \
"s_addc_u32 s41, s41, 0\n" \
"ds_read_b64_tr_b16 a[172:173], v213 offset:45216\n" \
"ds_read_b64_tr_b16 a[174:175], v213 offset:63648\n" \
"ds_read_b64_tr_b16 a[204:205], v213 offset:45248\n" \
"ds_read_b64_tr_b16 a[206:207], v213 offset:63680\n" \
"ds_read_b64_tr_b16 a[236:237], v213 offset:45280\n" \
"ds_read_b64_tr_b16 a[238:239], v213 offset:63712\n" \
"ds_read_b64_tr_b16 v[236:237], v213 offset:45312\n" \
"ds_read_b64_tr_b16 v[238:239], v213 offset:63744\n" \
"s_waitcnt lgkmcnt(0)\n" \
"s_waitcnt vmcnt(18)\n" \
"s_barrier\n" \
"s_add_u32 m0, s57, 110592\n" \
"s_nop 0\n" \
"global_load_lds_dwordx4 v202, s[40:41] offset:0\n" \
"s_sleep 3\n" \
"global_load_lds_dwordx4 v202, s[40:41] offset:1024\n" \
"s_sleep 3\n" \
"global_load_lds_dwordx4 v202, s[40:41] offset:2048\n" \
"s_sleep 3\n" \
"global_load_lds_dwordx4 v202, s[40:41] offset:3072\n" \
"s_sleep 3\n" \
"s_add_u32 m0, s57, 114688\n" \
"s_nop 0\n" \
"global_load_lds_dwordx4 v203, s[40:41] offset:0\n" \
"s_sleep 3\n" \
"global_load_lds_dwordx4 v203, s[40:41] offset:1024\n" \
"s_sleep 3\n" \
"global_load_lds_dwordx4 v203, s[40:41] offset:2048\n" \
"s_sleep 3\n" \
"global_load_lds_dwordx4 v203, s[40:41] offset:3072\n" \
"s_sleep 3\n" \
"s_add_u32 m0, s57, 118784\n" \
"s_nop 0\n" \
"global_load_lds_dwordx4 v204, s[40:41] offset:0\n" \
"s_sleep 3\n" \
"s_add_u32 s40, s40, 36864\n" \
"s_addc_u32 s41, s41, 0\n" \
"ds_read_b64_tr_b16 a[176:177], v212 offset:160\n" \
"ds_read_b64_tr_b16 a[178:179], v212 offset:18592\n" \
"ds_read_b64_tr_b16 a[208:209], v212 offset:192\n" \
"ds_read_b64_tr_b16 a[210:211], v212 offset:18624\n" \
"ds_read_b64_tr_b16 a[240:241], v212 offset:224\n" \
"ds_read_b64_tr_b16 a[242:243], v212 offset:18656\n" \
"ds_read_b64_tr_b16 v[240:241], v212 offset:256\n" \
"ds_read_b64_tr_b16 v[242:243], v212 offset:18688\n" \
"s_waitcnt lgkmcnt(0)\n" \
"s_waitcnt vmcnt(18)\n" \
"s_barrier\n" \
"ds_read_b64_tr_b16 a[180:181], v212 offset:37024\n" \
"ds_read_b64_tr_b16 a[182:183], v212 offset:55456\n" \
"ds_read_b64_tr_b16 a[212:213], v212 offset:37056\n" \
"ds_read_b64_tr_b16 a[214:215], v212 offset:55488\n" \
"ds_read_b64_tr_b16 a[244:245], v212 offset:37088\n" \
"ds_read_b64_tr_b16 a[246:247], v212 offset:55520\n" \
"ds_read_b64_tr_b16 v[244:245], v212 offset:37120\n" \
"ds_read_b64_tr_b16 v[246:247], v212 offset:55552\n" \
"s_waitcnt lgkmcnt(0)\n" \
"s_waitcnt vmcnt(9)\n" \
"s_barrier\n" \
"ds_read_b64_tr_b16 a[184:185], v213 offset:8352\n" \
"ds_read_b64_tr_b16 a[186:187], v213 offset:26784\n" \
"ds_read_b64_tr_b16 a[216:217], v213 offset:8384\n" \
"ds_read_b64_tr_b16 a[218:219], v213 offset:26816\n" \
"ds_read_b64_tr_b16 a[248:249], v213 offset:8416\n" \
"ds_read_b64_tr_b16 a[250:251], v213 offset:26848\n" \
"ds_read_b64_tr_b16 v[248:249], v213 offset:8448\n" \
"ds_read_b64_tr_b16 v[250:251], v213 offset:26880\n" \
"s_waitcnt lgkmcnt(0)\n" \
"s_waitcnt vmcnt(0)\n" \
"s_barrier\n" \
"ds_read_b64_tr_b16 a[188:189], v213 offset:45216\n" \
"ds_read_b64_tr_b16 a[190:191], v213 offset:63648\n" \
"ds_read_b64_tr_b16 a[220:221], v213 offset:45248\n" \
"ds_read_b64_tr_b16 a[222:223], v213 offset:63680\n" \
"ds_read_b64_tr_b16 a[252:253], v213 offset:45280\n" \
"ds_read_b64_tr_b16 a[254:255], v213 offset:63712\n" \
"ds_read_b64_tr_b16 v[252:253], v213 offset:45312\n" \
"ds_read_b64_tr_b16 v[254:255], v213 offset:63744\n" \
"s_waitcnt lgkmcnt(0)\n" \
"s_barrier\n" \
"v_mov_b32 v214, v201\n" \
"v_add_u32 v215, 4096, v201\n" \
"v_add_u32 v212, 4096, v209\n" \
"v_add_u32 v213, 8192, v209\n" \
"v_add_u32 v200, 12288, v209\n" \
"v_mov_b32 v0, -1\n" \
"v_mov_b32 v1, -1\n" \
"v_mov_b32 v2, -1\n" \
"v_mov_b32 v3, -1\n" \
"s_mov_b32 s61, 0\n" \
"v_mov_b32 v169, 0\n" \
"global_load_dword v168, v169, s[52:53] sc1\n" \
"s_waitcnt vmcnt(0)\n" \
".Larm_c%=:\n" \
"v_cmp_gt_u32 vcc, 1, v168\n" \
"s_cbranch_vccnz .Larm_s%=\n" \
"s_add_u32 s58, s58, 1\n" \
"v_min_u32 v169, 68, v207\n" \
"v_add_u32 v169, 10240, v169\n" \
"global_load_dword v168, v169, s[54:55] sc1\n" \
"s_waitcnt vmcnt(0)\n" \
".Lrdy_c%=:\n" \
"v_cmp_gt_u32 vcc, 1, v168\n" \
"s_cbranch_vccnz .Lrdy_s%=\n" \
"v_min_u32_dpp v168, v168, v168 row_shr:1 row_mask:0xf bank_mask:0xf\n" \
"s_nop 1\n" \
"v_min_u32_dpp v168, v168, v168 row_shr:2 row_mask:0xf bank_mask:0xf\n" \
"s_nop 1\n" \
"v_min_u32_dpp v168, v168, v168 row_shr:4 row_mask:0xf bank_mask:0xf\n" \
"s_nop 1\n" \
"v_min_u32_dpp v168, v168, v168 row_shr:8 row_mask:0xf bank_mask:0xf\n" \
"s_nop 0\n" \
"v_readlane_b32 s76, v168, 15\n" \
"v_readlane_b32 s77, v168, 31\n" \
"s_min_u32 s76, s76, s77\n" \
"s_and_b32 s76, s76, 1\n" \
"s_lshl_b32 s76, s76, 13\n" \
"s_xor_b32 s42, s42, s76\n" \
"s_xor_b32 s44, s44, s76\n" \
"s_xor_b32 s46, s46, s76\n" \
"s_mov_b64 s[50:51], s[44:45]\n" \
"s_xor_b32 s44, s44, 0x2000\n" \
"global_load_dword v168, v206, s[54:55] sc1\n" \
"s_waitcnt vmcnt(0)\n" \
".Lp0_c%=:\n" \
"v_cmp_gt_u32 vcc, s58, v168\n" \
"s_cbranch_vccnz .Lp0_s%=\n" \
"global_load_dwordx4 v[0:3], v201, s[42:43] offset:-4096 sc1\n" \
"global_load_dwordx4 v[4:7], v201, s[42:43] offset:-2048 sc1\n" \
"global_load_dwordx4 v[8:11], v201, s[42:43] offset:0 sc1\n" \
"global_load_dwordx4 v[12:15], v201, s[42:43] offset:2048 sc1\n" \
"s_add_u32 m0, s56, 0\n" \
"s_nop 0\n" \
"global_load_lds_dwordx4 v201, s[44:45] offset:0 sc1\n" \
"global_load_lds_dwordx4 v201, s[44:45] offset:1024 sc1\n" \
"global_load_lds_dwordx4 v201, s[44:45] offset:2048 sc1\n" \
"global_load_lds_dwordx4 v201, s[44:45] offset:3072 sc1\n" \
"s_add_u32 m0, s56, 4096\n" \
"s_nop 0\n" \
"global_load_lds_dwordx4 v215, s[44:45] offset:0 sc1\n" \
"global_load_lds_dwordx4 v215, s[44:45] offset:1024 sc1\n" \
"global_load_lds_dwordx4 v215, s[44:45] offset:2048 sc1\n" \
"global_load_lds_dwordx4 v215, s[44:45] offset:3072 sc1\n" \
"s_add_u32 s42, s42, 32768\n" \
"s_addc_u32 s43, s43, 0\n" \
"s_add_u32 s44, s44, 65536\n" \
"s_addc_u32 s45, s45, 0\n" \
"s_xor_b32 s50, s44, 0x2000\n" \
"s_mov_b32 s51, s45\n" \
"global_load_dword v203, v205, s[54:55] sc1\n" \
"s_waitcnt vmcnt(0)\n" \
".Lpg_c%=:\n" \
"v_cmp_gt_u32 vcc, 1, v203\n" \
"s_cbranch_vccnz .Lpg_s%=\n" \
"v_mov_b32 v204, 12288\n" \
"s_mov_b32 s94, 0\n" \
".Lsp%=:\n" \
"v_readfirstlane_b32 s69, v203\n" \
"s_cmp_eq_u32 63, 64\n" \
"s_cselect_b32 s74, s68, s67\n" \
"s_and_b32 s72, s69, 0xffff\n" \
"s_sub_i32 s72, 63, s72\n" \
"s_add_i32 s72, s72, 1\n" \
"s_cmp_lt_i32 s72, 0\n" \
"s_cselect_b32 s91, 0, 0x4000000\n" \
"s_mul_i32 s72, s72, s66\n" \
"s_add_u32 s69, s69, s74\n" \
"s_add_u32 s69, s69, s72\n" \
"s_cmp_le_i32 s72, 0\n" \
"s_cbranch_scc1 .Lsq%=\n" \
"s_sub_u32 s73, s73, 1\n" \
"s_cbranch_scc1 .Lend%=\n" \
"s_sub_u32 s69, s69, s67\n" \
"s_sub_u32 s69, s69, 1310720\n" \
"s_mov_b32 s74, 40\n" \
".Lsr%=:\n" \
"s_sleep 5\n" \
"s_memrealtime s[92:93]\n" \
"s_waitcnt lgkmcnt(0)\n" \
"s_lshl_b32 s92, s92, 16\n" \
"s_sub_i32 s92, s69, s92\n" \
"s_sub_u32 s74, s74, 1\n" \
"s_cselect_b32 s92, 0, s92\n" \
"s_cmp_gt_i32 s92, 0\n" \
"s_cbranch_scc1 .Lsr%=\n" \
"global_load_dword v203, v205, s[54:55] sc1\n" \
"s_waitcnt vmcnt(0)\n" \
"s_branch .Lsp%=\n" \
".Lsq%=:\n" \
".Ltop%=:\n" \
"s_barrier\n" \
"ds_read_b128 v[16:19], v208 offset:0\n" \
"ds_read_b128 v[20:23], v208 offset:1024\n" \
"ds_read_b128 v[24:27], v208 offset:4096\n" \
"ds_read_b128 v[28:31], v208 offset:5120\n" \
"ds_read_b128 v[32:35], v208 offset:8192\n" \
"ds_read_b128 v[36:39], v208 offset:9216\n" \
"ds_read_b128 v[40:43], v208 offset:12288\n" \
"ds_read_b128 v[44:47], v208 offset:13312\n" \
"s_waitcnt lgkmcnt(7)\n" \
"v_mfma_f32_16x16x32_bf16 v[104:107], v[16:19], v[0:3], v[192:195]\n" \
"ds_read2_b64 v[48:51], v209 offset0:0 offset1:32\n" \
"s_waitcnt lgkmcnt(7)\n" \
"v_mfma_f32_16x16x32_bf16 v[108:111], v[20:23], v[0:3], v[196:199]\n" \
"ds_read2_b64 v[52:55], v209 offset0:128 offset1:160\n" \
"s_waitcnt lgkmcnt(7)\n" \
"v_mfma_f32_16x16x32_bf16 v[104:107], v[24:27], v[4:7], v[104:107]\n" \
"ds_read2_b64 v[56:59], v212 offset0:0 offset1:32\n" \
"s_waitcnt lgkmcnt(7)\n" \
"v_mfma_f32_16x16x32_bf16 v[108:111], v[28:31], v[4:7], v[108:111]\n" \
"ds_read2_b64 v[60:63], v212 offset0:128 offset1:160\n" \
"s_waitcnt lgkmcnt(7)\n" \
"v_mfma_f32_16x16x32_bf16 v[104:107], v[32:35], v[8:11], v[104:107]\n" \
"ds_read2_b64 v[64:67], v213 offset0:0 offset1:32\n" \
"s_waitcnt lgkmcnt(7)\n" \
"v_mfma_f32_16x16x32_bf16 v[108:111], v[36:39], v[8:11], v[108:111]\n" \
"ds_read2_b64 v[68:71], v213 offset0:128 offset1:160\n" \
"s_waitcnt lgkmcnt(7)\n" \
"v_mfma_f32_16x16x32_bf16 v[104:107], v[40:43], v[12:15], v[104:107]\n" \
"ds_read2_b64 v[72:75], v200 offset0:0 offset1:32\n" \
"s_waitcnt lgkmcnt(7)\n" \
"v_mfma_f32_16x16x32_bf16 v[108:111], v[44:47], v[12:15], v[108:111]\n" \
"ds_read2_b64 v[76:79], v200 offset0:128 offset1:160\n" \
"s_nop 3\n" \
"v_max3_f32 v168, v104, v105, v106\n" \
"s_nop 1\n" \
"v_max3_f32 v168, v168, v107, v108\n" \
"v_max3_f32 v168, v168, v109, v110\n" \
"v_max_f32 v168, v168, v111\n" \
"v_mov_b32 v173, 0\n" \
"v_mov_b32 v174, 0\n" \
"s_cmp_eq_u32 s94, 0\n" \
"s_cbranch_scc0 .Lkm_p%=\n" \
"v_readfirstlane_b32 s69, v203\n" \
"s_cmp_eq_u32 s58, 64\n" \
"s_cselect_b32 s74, s68, s67\n" \
"s_and_b32 s72, s69, 0xffff\n" \
"s_sub_i32 s72, s58, s72\n" \
"s_add_i32 s72, s72, 1\n" \
"s_cmp_lt_i32 s72, 0\n" \
"s_cselect_b32 s91, 0, 0x4000000\n" \
"s_mul_i32 s72, s72, s66\n" \
"s_add_u32 s69, s69, s74\n" \
"s_add_u32 s69, s69, s72\n" \
"s_memrealtime s[92:93]\n" \
"s_waitcnt lgkmcnt(0)\n" \
"s_lshl_b32 s92, s92, 16\n" \
"s_sub_i32 s92, s69, s92\n" \
"s_cmp_ge_u32 s92, s91\n" \
"s_cbranch_scc1 .Lkm_l%=\n" \
"s_cmp_lt_u32 s92, 3276800\n" \
"s_cbranch_scc1 .Lkm_w%=\n" \
".Lkm_w%=:\n" \
"s_memrealtime s[92:93]\n" \
"s_waitcnt lgkmcnt(0)\n" \
"s_lshl_b32 s92, s92, 16\n" \
"s_sub_i32 s92, s69, s92\n" \
"s_cmp_ge_u32 s92, s91\n" \
"s_cbranch_scc0 .Lkm_w%=\n" \
".Lkm_l%=:\n" \
"global_load_dwordx4 v[176:179], v210, s[46:47] offset:-4096 sc1\n" \
"global_load_dwordx4 v[180:183], v210, s[46:47] offset:-2048 sc1\n" \
"global_load_dwordx4 v[184:187], v210, s[46:47] offset:0 sc1\n" \
"global_load_dwordx4 v[188:191], v210, s[46:47] offset:2048 sc1\n" \
"global_load_dword v216, v207, s[46:47] offset:-3072 sc1\n" \
"global_load_dword v217, v207, s[46:47] offset:-2816 sc1\n" \
"global_load_dword v218, v207, s[46:47] offset:-1024 sc1\n" \
"global_load_dword v219, v207, s[46:47] offset:-768 sc1\n" \
"global_load_dword v220, v207, s[46:47] offset:1024 sc1\n" \
"global_load_dword v221, v207, s[46:47] offset:1280 sc1\n" \
"global_load_dword v222, v207, s[46:47] offset:3072 sc1\n" \
"global_load_dword v223, v207, s[46:47] offset:3328 sc1\n" \
"global_load_dword v202, v206, s[54:55] sc1\n" \
"global_load_dword v203, v205, s[54:55] sc1\n" \
"s_cmp_eq_u32 s61, 0\n" \
"s_cbranch_scc0 .Lkm_u%=\n" \
"s_waitcnt vmcnt(2)\n" \
".Lkm_c%=:\n" \
"v_mfma_f32_16x16x32_bf16 v[112:115], v[176:179], v[0:3], 0\n" \
"v_max_u32 v169, v176, v177\n" \
"v_max3_u32 v169, v169, v178, v179\n" \
"v_max3_u32 v169, v169, v180, v181\n" \
"v_mfma_f32_16x16x32_bf16 v[112:115], v[180:183], v[4:7], v[112:115]\n" \
"v_max3_u32 v169, v169, v182, v183\n" \
"v_max3_u32 v169, v169, v184, v185\n" \
"v_max3_u32 v169, v169, v186, v187\n" \
"v_mfma_f32_16x16x32_bf16 v[112:115], v[184:187], v[8:11], v[112:115]\n" \
"v_max3_u32 v169, v169, v188, v189\n" \
"v_max3_u32 v169, v169, v190, v191\n" \
"v_max3_u32 v169, v169, v216, v217\n" \
"v_mfma_f32_16x16x32_bf16 v[112:115], v[188:191], v[12:15], v[112:115]\n" \
"v_max3_u32 v169, v169, v218, v219\n" \
"v_max3_u32 v169, v169, v220, v221\n" \
"v_max3_u32 v169, v169, v222, v223\n" \
"v_cmp_eq_u32 vcc, -1, v169\n" \
"s_nop 4\n" \
"s_cbranch_vccnz .Lkm_s%=\n" \
"s_add_u32 s46, s46, 32768\n" \
"s_addc_u32 s47, s47, 0\n" \
"v_max3_f32 v168, v168, v112, v113\n" \
"v_mov_b32 v169, v168\n" \
"s_nop 1\n" \
"v_permlane16_swap_b32 v168, v169\n" \
"v_max_f32 v170, v168, v169\n" \
"v_max_f32 v168, v168, v169\n" \
"s_nop 1\n" \
"v_permlane32_swap_b32 v168, v170\n" \
"v_max_f32 v168, v168, v170\n" \
"v_mul_f32 v168, s75, v168\n" \
"v_fma_f32 v104, v104, s75, -v168\n" \
"v_fma_f32 v105, v105, s75, -v168\n" \
"v_fma_f32 v106, v106, s75, -v168\n" \
"v_fma_f32 v107, v107, s75, -v168\n" \
"v_fma_f32 v108, v108, s75, -v168\n" \
"v_fma_f32 v109, v109, s75, -v168\n" \
"v_fma_f32 v110, v110, s75, -v168\n" \
"v_fma_f32 v111, v111, s75, -v168\n" \
"v_fma_f32 v112, v112, s75, -v168\n" \
"v_fma_f32 v113, v113, s75, -v168\n" \
"v_exp_f32 v104, v104\n" \
"v_exp_f32 v105, v105\n" \
"v_exp_f32 v106, v106\n" \
"v_exp_f32 v107, v107\n" \
"v_exp_f32 v108, v108\n" \
"v_exp_f32 v109, v109\n" \
"v_exp_f32 v110, v110\n" \
"v_exp_f32 v111, v111\n" \
"v_exp_f32 v112, v112\n" \
"v_exp_f32 v113, v113\n" \
"v_add_f32 v168, v104, v105\n" \
"v_add_f32 v168, v168, v106\n" \
"v_add_f32 v168, v168, v107\n" \
"v_add_f32 v168, v168, v108\n" \
"v_add_f32 v168, v168, v109\n" \
"v_add_f32 v168, v168, v110\n" \
"v_add_f32 v168, v168, v111\n" \
"v_add_f32 v168, v168, v112\n" \
"v_add_f32 v168, v168, v113\n" \
"v_mov_b32 v169, v168\n" \
"s_nop 1\n" \
"v_permlane16_swap_b32 v168, v169\n" \
"v_add_f32 v170, v168, v169\n" \
"v_add_f32 v168, v168, v169\n" \
"s_nop 1\n" \
"v_permlane32_swap_b32 v168, v170\n" \
"v_add_f32 v168, v168, v170\n" \
"v_rcp_f32 v168, v168\n" \
"s_nop 0\n" \
"v_mul_f32 v104, v168, v104\n" \
"v_mul_f32 v105, v168, v105\n" \
"v_mul_f32 v106, v168, v106\n" \
"v_mul_f32 v107, v168, v107\n" \
"v_mul_f32 v108, v168, v108\n" \
"v_mul_f32 v109, v168, v109\n" \
"v_mul_f32 v110, v168, v110\n" \
"v_mul_f32 v111, v168, v111\n" \
"v_mul_f32 v112, v168, v112\n" \
"v_mul_f32 v113, v168, v113\n" \
"v_cvt_pk_bf16_f32 v116, v104, v105\n" \
"v_cvt_pk_bf16_f32 v117, v108, v109\n" \
"v_cvt_pk_bf16_f32 v118, v106, v107\n" \
"v_cvt_pk_bf16_f32 v119, v110, v111\n" \
"v_cvt_pk_bf16_f32 v172, v112, v113\n" \
"v_cvt_pk_bf16_f32 v175, v112, v113\n" \
"v_mfma_f32_16x16x32_bf16 v[120:123], v[48:51], v[116:119], 0\n" \
"v_mfma_f32_16x16x32_bf16 v[124:127], v[52:55], v[116:119], 0\n" \
"v_mfma_f32_16x16x16_bf16 v[120:123], v[216:217], v[172:173], v[120:123]\n" \
"v_mfma_f32_16x16x16_bf16 v[124:127], v[216:217], v[174:175], v[124:127]\n" \
"v_mfma_f32_16x16x32_bf16 v[128:131], v[56:59], v[116:119], 0\n" \
"v_mfma_f32_16x16x32_bf16 v[132:135], v[60:63], v[116:119], 0\n" \
"v_mfma_f32_16x16x16_bf16 v[128:131], v[218:219], v[172:173], v[128:131]\n" \
"v_mfma_f32_16x16x16_bf16 v[132:135], v[218:219], v[174:175], v[132:135]\n" \
"s_nop 2\n" \
"v_cvt_pk_bf16_f32 v120, v120, v121\n" \
"v_cvt_pk_bf16_f32 v121, v122, v123\n" \
"v_cvt_pk_bf16_f32 v122, v124, v125\n" \
"v_cvt_pk_bf16_f32 v123, v126, v127\n" \
"ds_write_b128 v211, v[120:123] offset:0\n" \
"v_mfma_f32_16x16x32_bf16 v[136:139], v[64:67], v[116:119], 0\n" \
"v_mfma_f32_16x16x32_bf16 v[140:143], v[68:71], v[116:119], 0\n" \
"v_mfma_f32_16x16x16_bf16 v[136:139], v[220:221], v[172:173], v[136:139]\n" \
"v_mfma_f32_16x16x16_bf16 v[140:143], v[220:221], v[174:175], v[140:143]\n" \
"v_cvt_pk_bf16_f32 v128, v128, v129\n" \
"v_cvt_pk_bf16_f32 v129, v130, v131\n" \
"v_cvt_pk_bf16_f32 v130, v132, v133\n" \
"v_cvt_pk_bf16_f32 v131, v134, v135\n" \
"ds_write_b128 v211, v[128:131] offset:2048\n" \
"v_mfma_f32_16x16x32_bf16 v[144:147], v[72:75], v[116:119], 0\n" \
"v_mfma_f32_16x16x32_bf16 v[148:151], v[76:79], v[116:119], 0\n" \
"v_mfma_f32_16x16x16_bf16 v[144:147], v[222:223], v[172:173], v[144:147]\n" \
"v_mfma_f32_16x16x16_bf16 v[148:151], v[222:223], v[174:175], v[148:151]\n" \
"v_cvt_pk_bf16_f32 v136, v136, v137\n" \
"v_cvt_pk_bf16_f32 v137, v138, v139\n" \
"v_cvt_pk_bf16_f32 v138, v140, v141\n" \
"v_cvt_pk_bf16_f32 v139, v142, v143\n" \
"ds_write_b128 v211, v[136:139] offset:4096\n" \
"s_nop 1\n" \
"v_cvt_pk_bf16_f32 v144, v144, v145\n" \
"v_cvt_pk_bf16_f32 v145, v146, v147\n" \
"v_cvt_pk_bf16_f32 v146, v148, v149\n" \
"v_cvt_pk_bf16_f32 v147, v150, v151\n" \
"ds_write_b128 v211, v[144:147] offset:6144\n" \
"s_add_u32 s59, s58, 2\n" \
"s_cmp_gt_u32 s59, 63\n" \
"s_cselect_b32 s60, 0, s59\n" \
"s_cselect_b64 s[64:65], 0, -1\n" \
"s_waitcnt lgkmcnt(0)\n" \
"s_barrier\n" \
"ds_read_b128 v[80:83], v214 offset:0\n" \
"ds_read_b128 v[84:87], v214 offset:1024\n" \
"ds_read_b128 v[88:91], v214 offset:2048\n" \
"ds_read_b128 v[92:95], v214 offset:3072\n" \
"ds_read_b128 v[96:99], v214 offset:4096\n" \
"ds_read_b128 v[100:103], v214 offset:5120\n" \
"ds_read_b128 v[104:107], v214 offset:6144\n" \
"ds_read_b128 v[108:111], v214 offset:7168\n" \
"ds_read_b128 v[112:115], v214 offset:8192\n" \
"ds_read_b128 v[116:119], v214 offset:9216\n" \
"ds_read_b128 v[120:123], v214 offset:10240\n" \
"ds_read_b128 v[124:127], v214 offset:11264\n" \
"ds_read_b128 v[128:131], v214 offset:12288\n" \
"ds_read_b128 v[132:135], v214 offset:13312\n" \
"ds_read_b128 v[136:139], v214 offset:14336\n" \
"s_waitcnt lgkmcnt(14)\n" \
"ds_read_b128 v[140:143], v214 offset:15360\n" \
"v_xor_b32 v211, 0x4000, v211\n" \
"v_xor_b32 v214, 0x4000, v214\n" \
"s_waitcnt vmcnt(0)\n" \
".Lp1_c%=:\n" \
"v_cmp_gt_u32 vcc, s60, v202\n" \
"s_cbranch_vccnz .Lp1_s%=\n" \
"s_waitcnt lgkmcnt(15)\n" \
"v_mfma_f32_16x16x32_bf16 v[160:163], a[160:163], v[80:83], 0\n" \
"s_waitcnt lgkmcnt(14)\n" \
"v_mfma_f32_16x16x32_bf16 v[164:167], a[160:163], v[84:87], 0\n" \
"s_waitcnt lgkmcnt(13)\n" \
"v_mfma_f32_16x16x32_bf16 v[160:163], a[164:167], v[88:91], v[160:163]\n" \
"s_waitcnt lgkmcnt(12)\n" \
"v_mfma_f32_16x16x32_bf16 v[164:167], a[164:167], v[92:95], v[164:167]\n" \
"s_waitcnt lgkmcnt(11)\n" \
"v_mfma_f32_16x16x32_bf16 v[160:163], a[168:171], v[96:99], v[160:163]\n" \
"s_waitcnt lgkmcnt(10)\n" \
"v_mfma_f32_16x16x32_bf16 v[164:167], a[168:171], v[100:103], v[164:167]\n" \
"s_waitcnt lgkmcnt(9)\n" \
"v_mfma_f32_16x16x32_bf16 v[160:163], a[172:175], v[104:107], v[160:163]\n" \
"s_waitcnt lgkmcnt(8)\n" \
"v_mfma_f32_16x16x32_bf16 v[164:167], a[172:175], v[108:111], v[164:167]\n" \
"s_waitcnt lgkmcnt(7)\n" \
"v_mfma_f32_16x16x32_bf16 v[160:163], a[176:179], v[112:115], v[160:163]\n" \
"s_waitcnt lgkmcnt(6)\n" \
"v_mfma_f32_16x16x32_bf16 v[164:167], a[176:179], v[116:119], v[164:167]\n" \
"s_waitcnt lgkmcnt(5)\n" \
"v_mfma_f32_16x16x32_bf16 v[160:163], a[180:183], v[120:123], v[160:163]\n" \
"s_waitcnt lgkmcnt(4)\n" \
"v_mfma_f32_16x16x32_bf16 v[164:167], a[180:183], v[124:127], v[164:167]\n" \
"s_waitcnt lgkmcnt(3)\n" \
"v_mfma_f32_16x16x32_bf16 v[160:163], a[184:187], v[128:131], v[160:163]\n" \
"s_waitcnt lgkmcnt(2)\n" \
"v_mfma_f32_16x16x32_bf16 v[164:167], a[184:187], v[132:135], v[164:167]\n" \
"s_waitcnt lgkmcnt(1)\n" \
"v_mfma_f32_16x16x32_bf16 v[160:163], a[188:191], v[136:139], v[160:163]\n" \
"s_waitcnt lgkmcnt(0)\n" \
"v_mfma_f32_16x16x32_bf16 v[164:167], a[188:191], v[140:143], v[164:167]\n" \
"v_mfma_f32_16x16x32_bf16 v[144:147], a[192:195], v[80:83], 0\n" \
"v_mfma_f32_16x16x32_bf16 v[148:151], a[192:195], v[84:87], 0\n" \
"v_mfma_f32_16x16x32_bf16 v[144:147], a[196:199], v[88:91], v[144:147]\n" \
"v_mfma_f32_16x16x32_bf16 v[148:151], a[196:199], v[92:95], v[148:151]\n" \
"s_nop 1\n" \
"v_cvt_pk_bf16_f32 v172, v160, v161\n" \
"v_mfma_f32_16x16x32_bf16 v[144:147], a[200:203], v[96:99], v[144:147]\n" \
"v_cvt_pk_bf16_f32 v173, v162, v163\n" \
"v_mfma_f32_16x16x32_bf16 v[148:151], a[200:203], v[100:103], v[148:151]\n" \
"v_cvt_pk_bf16_f32 v174, v164, v165\n" \
"v_mfma_f32_16x16x32_bf16 v[144:147], a[204:207], v[104:107], v[144:147]\n" \
"v_cvt_pk_bf16_f32 v175, v166, v167\n" \
"v_mfma_f32_16x16x32_bf16 v[148:151], a[204:207], v[108:111], v[148:151]\n" \
"global_store_dwordx4 v201, v[172:175], s[48:49] offset:1024 sc1\n" \
"v_mfma_f32_16x16x32_bf16 v[144:147], a[208:211], v[112:115], v[144:147]\n" \
"v_mfma_f32_16x16x32_bf16 v[148:151], a[208:211], v[116:119], v[148:151]\n" \
"v_mfma_f32_16x16x32_bf16 v[144:147], a[212:215], v[120:123], v[144:147]\n" \
"v_mfma_f32_16x16x32_bf16 v[148:151], a[212:215], v[124:127], v[148:151]\n" \
"v_mfma_f32_16x16x32_bf16 v[144:147], a[216:219], v[128:131], v[144:147]\n" \
"v_mfma_f32_16x16x32_bf16 v[148:151], a[216:219], v[132:135], v[148:151]\n" \
"v_mfma_f32_16x16x32_bf16 v[144:147], a[220:223], v[136:139], v[144:147]\n" \
"v_mfma_f32_16x16x32_bf16 v[148:151], a[220:223], v[140:143], v[148:151]\n" \
"v_mfma_f32_16x16x32_bf16 v[152:155], a[224:227], v[80:83], 0\n" \
"v_mfma_f32_16x16x32_bf16 v[156:159], a[224:227], v[84:87], 0\n" \
"v_mfma_f32_16x16x32_bf16 v[152:155], a[228:231], v[88:91], v[152:155]\n" \
"v_mfma_f32_16x16x32_bf16 v[156:159], a[228:231], v[92:95], v[156:159]\n" \
"s_nop 2\n" \
"v_cvt_pk_bf16_f32 v168, v144, v145\n" \
"v_mfma_f32_16x16x32_bf16 v[152:155], a[232:235], v[96:99], v[152:155]\n" \
"v_cvt_pk_bf16_f32 v169, v146, v147\n" \
"v_mfma_f32_16x16x32_bf16 v[156:159], a[232:235], v[100:103], v[156:159]\n" \
"v_cvt_pk_bf16_f32 v170, v148, v149\n" \
"v_mfma_f32_16x16x32_bf16 v[152:155], a[236:239], v[104:107], v[152:155]\n" \
"v_cvt_pk_bf16_f32 v171, v150, v151\n" \
"v_mfma_f32_16x16x32_bf16 v[156:159], a[236:239], v[108:111], v[156:159]\n" \
"global_store_dwordx4 v201, v[168:171], s[48:49] offset:2048 sc1\n" \
"v_mfma_f32_16x16x32_bf16 v[152:155], a[240:243], v[112:115], v[152:155]\n" \
"v_mfma_f32_16x16x32_bf16 v[156:159], a[240:243], v[116:119], v[156:159]\n" \
"v_mfma_f32_16x16x32_bf16 v[152:155], a[244:247], v[120:123], v[152:155]\n" \
"v_mfma_f32_16x16x32_bf16 v[156:159], a[244:247], v[124:127], v[156:159]\n" \
"v_mfma_f32_16x16x32_bf16 v[152:155], a[248:251], v[128:131], v[152:155]\n" \
"v_mfma_f32_16x16x32_bf16 v[156:159], a[248:251], v[132:135], v[156:159]\n" \
"v_mfma_f32_16x16x32_bf16 v[152:155], a[252:255], v[136:139], v[152:155]\n" \
"v_mfma_f32_16x16x32_bf16 v[156:159], a[252:255], v[140:143], v[156:159]\n" \
"v_mfma_f32_16x16x32_bf16 v[160:163], v[224:227], v[80:83], 0\n" \
"v_mfma_f32_16x16x32_bf16 v[164:167], v[224:227], v[84:87], 0\n" \
"v_mfma_f32_16x16x32_bf16 v[160:163], v[228:231], v[88:91], v[160:163]\n" \
"v_mfma_f32_16x16x32_bf16 v[164:167], v[228:231], v[92:95], v[164:167]\n" \
"s_nop 2\n" \
"v_cvt_pk_bf16_f32 v172, v152, v153\n" \
"v_mfma_f32_16x16x32_bf16 v[160:163], v[232:235], v[96:99], v[160:163]\n" \
"v_cvt_pk_bf16_f32 v173, v154, v155\n" \
"v_mfma_f32_16x16x32_bf16 v[164:167], v[232:235], v[100:103], v[164:167]\n" \
"v_cvt_pk_bf16_f32 v174, v156, v157\n" \
"v_mfma_f32_16x16x32_bf16 v[160:163], v[236:239], v[104:107], v[160:163]\n" \
"v_cvt_pk_bf16_f32 v175, v158, v159\n" \
"v_mfma_f32_16x16x32_bf16 v[164:167], v[236:239], v[108:111], v[164:167]\n" \
"global_store_dwordx4 v201, v[172:175], s[48:49] offset:3072 sc1\n" \
"v_mfma_f32_16x16x32_bf16 v[160:163], v[240:243], v[112:115], v[160:163]\n" \
"v_mfma_f32_16x16x32_bf16 v[164:167], v[240:243], v[116:119], v[164:167]\n" \
"v_mfma_f32_16x16x32_bf16 v[160:163], v[244:247], v[120:123], v[160:163]\n" \
"v_mfma_f32_16x16x32_bf16 v[164:167], v[244:247], v[124:127], v[164:167]\n" \
"v_mfma_f32_16x16x32_bf16 v[160:163], v[248:251], v[128:131], v[160:163]\n" \
"v_mfma_f32_16x16x32_bf16 v[164:167], v[248:251], v[132:135], v[164:167]\n" \
"v_mfma_f32_16x16x32_bf16 v[160:163], v[252:255], v[136:139], v[160:163]\n" \
"v_mfma_f32_16x16x32_bf16 v[164:167], v[252:255], v[140:143], v[164:167]\n" \
"s_nop 6\n" \
"v_cvt_pk_bf16_f32 v168, v160, v161\n" \
"v_cvt_pk_bf16_f32 v169, v162, v163\n" \
"v_cvt_pk_bf16_f32 v170, v164, v165\n" \
"v_cvt_pk_bf16_f32 v171, v166, v167\n" \
"global_store_dwordx4 v215, v[168:171], s[48:49] offset:0 sc1\n" \
"s_add_u32 s48, s48, 1179648\n" \
"s_addc_u32 s49, s49, 0\n" \
"s_cmp_eq_u32 s61, 0\n" \
"s_waitcnt vmcnt(6)\n" \
"s_cbranch_scc0 .Lnq%=\n" \
"s_waitcnt vmcnt(4)\n" \
".Lnq%=:\n" \
"s_mov_b32 s61, s58\n" \
"s_mov_b32 s58, s59\n" \
"s_cmp_le_u32 s59, 63\n" \
"s_cbranch_scc1 .Ltop%=\n" \
"s_waitcnt vmcnt(0)\n" \
".Lend%=:\n" \
"s_endpgm\n" \
".Larm_s%=:\n" \
"s_sub_u32 s73, s73, 1\n" \
"s_cbranch_scc1 .Lend%=\n" \
"s_sleep 4\n" \
"global_load_dword v168, v169, s[52:53] sc1\n" \
"s_waitcnt vmcnt(0)\n" \
"s_branch .Larm_c%=\n" \
".Lrdy_s%=:\n" \
"s_sub_u32 s73, s73, 1\n" \
"s_cbranch_scc1 .Lend%=\n" \
"s_sleep 4\n" \
"global_load_dword v168, v169, s[54:55] sc1\n" \
"s_waitcnt vmcnt(0)\n" \
"s_branch .Lrdy_c%=\n" \
".Lp0_s%=:\n" \
"s_sub_u32 s73, s73, 1\n" \
"s_cbranch_scc1 .Lend%=\n" \
"s_sleep 4\n" \
"global_load_dword v168, v206, s[54:55] sc1\n" \
"s_waitcnt vmcnt(0)\n" \
"s_branch .Lp0_c%=\n" \
".Lpg_s%=:\n" \
"s_sub_u32 s73, s73, 1\n" \
"s_cbranch_scc1 .Lend%=\n" \
"s_sleep 4\n" \
"global_load_dword v203, v205, s[54:55] sc1\n" \
"s_waitcnt vmcnt(0)\n" \
"s_branch .Lpg_c%=\n" \
".Lkm_s%=:\n" \
"s_sub_u32 s73, s73, 1\n" \
"s_cbranch_scc1 .Lend%=\n" \
"s_cmp_ge_u32 s58, 63\n" \
"s_cbranch_scc1 .Lkm_l%=\n" \
"global_load_dword v203, v205, s[54:55] sc1\n" \
"s_waitcnt vmcnt(0)\n" \
"v_readfirstlane_b32 s69, v203\n" \
"s_cmp_eq_u32 s58, 64\n" \
"s_cselect_b32 s74, s68, s67\n" \
"s_and_b32 s72, s69, 0xffff\n" \
"s_sub_i32 s72, s58, s72\n" \
"s_add_i32 s72, s72, 1\n" \
"s_cmp_lt_i32 s72, 0\n" \
"s_cselect_b32 s91, 0, 0x4000000\n" \
"s_mul_i32 s72, s72, s66\n" \
"s_add_u32 s69, s69, s74\n" \
"s_add_u32 s69, s69, s72\n" \
"s_branch .Lkm_w%=\n" \
".Lkm_u%=:\n" \
"s_waitcnt vmcnt(14)\n" \
"v_mov_b32 v169, s61\n" \
"s_mov_b64 exec, 1\n" \
"global_store_dword v207, v169, s[52:53] sc1\n" \
"global_store_dword v204, v203, s[52:53] sc1\n" \
"s_mov_b64 exec, -1\n" \
"s_mov_b32 s61, 0\n" \
"s_waitcnt vmcnt(4)\n" \
"s_branch .Lkm_c%=\n" \
".Lkm_p%=:\n" \
"global_load_dword v202, v206, s[54:55] sc1\n" \
"global_load_dword v203, v205, s[54:55] sc1\n" \
"s_waitcnt lgkmcnt(0)\n" \
"s_branch .Lkm_c%=\n" \
".Lp1_s%=:\n" \
"s_sub_u32 s73, s73, 1\n" \
"s_cbranch_scc1 .Lend%=\n" \
"s_sleep 4\n" \
"global_load_dword v202, v206, s[54:55] sc1\n" \
"s_waitcnt vmcnt(0)\n" \
"s_branch .Lp1_c%=\n"

#define KVF 0
#define KMF 0
typedef unsigned u32x16 __attribute__((ext_vector_type(16)));
typedef unsigned v4u __attribute__((ext_vector_type(4)));
struct Tm { unsigned K1, Kc, K2, X4, X3, XL, R, XM, DQ, DH, DF, DA, AP, AC, AL, FC; };
static constexpr unsigned MB = 1u << 20;
static constexpr unsigned O_FL = 4 * MB, O_XN = 5 * MB, O_Q = 8 * MB, O_KV = 160 * MB, O_KM = 24 * MB, O_H = 72 * MB, O_PA = 92 * MB, O_PF = 130 * MB, O_DBG = 146 * MB;
static constexpr unsigned F_LN = 0, F_KV = 4096, F_Q = 8192, F_RDY = 10240, F_H = 12288, F_FIN = 16384, F_F2 = 20480, F_ATT = 24576, F_PROG = 28672, F_HB = 32768;
static constexpr unsigned SPIN = WBUD;
__device__ __forceinline__ unsigned ld1(const char* p) { return __hip_atomic_load((const unsigned*)p, __ATOMIC_RELAXED, __HIP_MEMORY_SCOPE_AGENT); }
__device__ __forceinline__ void st1(char* p, unsigned v) { __hip_atomic_store((unsigned*)p, v, __ATOMIC_RELAXED, __HIP_MEMORY_SCOPE_AGENT); }
__device__ __forceinline__ unsigned hb_off() { return F_HB + (blockIdx.x & 15) * 128; }
__device__ __forceinline__ void hb_wait(const char* fl, unsigned h0) {
    for (unsigned b = SPIN; b && ld1(fl + hb_off()) == h0; b--) __builtin_amdgcn_s_sleep(1);
    asm volatile("" ::: "memory");
}
static constexpr unsigned LDS_REG0 = 7168, LDS_RSTR = 38912, LDS_TOTAL = 162816;
static constexpr unsigned W_F8 = 16 * 16384, W_P0 = 24 * 16384, W_PSTR = 43 * 16384, W_USED = 110 * 16384;
__device__ __forceinline__ unsigned rfl(unsigned x) { return __builtin_amdgcn_readfirstlane(x); }
__device__ __forceinline__ unsigned part_off(unsigned wb) { unsigned cq = wb / 3; return cq * 16384 + (cq % 5) * 256 + (wb % 3) * 2304; }
__device__ __forceinline__ char* al2m(char* p) { return (char*)(((size_t)p + (2 << 20) - 1) & ~(size_t)((2 << 20) - 1)); }
__device__ __forceinline__ unsigned kv_off(unsigned h, unsigned w) { return O_KV + h * 2 * MB + (w >> 1) * 16384 + ((h ^ KVF) & 1) * 8192 + (w & 1) * 4096; }
__device__ __forceinline__ unsigned km_off(unsigned h, unsigned w) { return O_KM + h * MB + ((h ^ KMF) & 2) * 4096 + w * 2048; }
#define PUT64(v, i, p) { unsigned long long q_ = (unsigned long long)(p); v[i] = rfl((unsigned)q_); v[i + 1] = rfl((unsigned)(q_ >> 32)); }

__device__ __forceinline__ void head_role(unsigned h, unsigned xc, const char* win, char* prog, const char* kvf, const char* Wq, const char* Wk,
                                          const char* Wv, const char* Wo, const Tm& k, char* dbg) {
    const unsigned l = threadIdx.x & 63, w = rfl(threadIdx.x >> 6);
    const unsigned g = l >> 4, n = l & 15, r = (l >> 2) & 3, c = l & 3;
    u32x16 pa, pb, sa, sb;
#pragma unroll
    for (unsigned i = 0; i < 4; i++) {
        unsigned dl = 16 * w + 4 * i + g, f = (dl & 1) | ((dl >> 1) & 6);
        pa[i] = dl * 8192 + (((n >> 1) ^ f) * 2 + (n & 1)) * 16 - i * 1024;
        unsigned sl = 8 * i + (l >> 3), el = (sl & ~3u) | ((sl & 1) << 1) | ((sl >> 1) & 1), f2 = 2 * ((sl >> 4) & 1) + ((sl >> 2) & 1), p = l & 7;
        pa[4 + i] = (32 * w + el) * 1152 + (((p >> 1) ^ f2) * 2 + (p & 1)) * 16 - i * 1024;
        unsigned t = i >> 1, qh = i & 1, row = (r >> 1) * 8 + 4 * (g & 1) + (r & 1), f3 = (row & 1) | ((row >> 1) & 6);
        pa[8 + i] = LDS_REG0 + (2 * qh + (g >> 1)) * LDS_RSTR + row * 256 + ((2 * w + t) ^ f3) * 32 + c * 8;
        unsigned s4 = 16 * (r & 1) + 8 * (g >> 1) + 4 * (r >> 1) + (g & 1), f4 = 2 * (r & 1) + (r >> 1);
        pa[12 + i] = LDS_REG0 + i * LDS_RSTR + s4 * 128 + (w ^ f4) * 32 + c * 8;
    }
    auto pos = [](unsigned k) { return 2 * ((k & 1) * 2 + (k >> 2)) + ((k >> 1) & 1); };
    pb[0] = k.XL; pb[1] = k.R; pb[2] = hb_off() - (F_KV + h * 128 + w * 4); pb[3] = l < 32 ? F_RDY + min(l, 17u) * 4 - (F_KV + h * 128 + w * 4) : 0;
    pb[4] = l * 16;
    pb[5] = l * 16 + LDS_REG0 + w * LDS_RSTR;
    pb[6] = (l & 2) ? 0x4E4EEEEEu : 0x44444444u;
    pb[7] = l * 4;
    pb[8] = (n == 4 * g ? 0x3F80u : 0) | (n == 4 * g + 2 ? 0x3F800000u : 0);
    pb[9] = (n == 2 * pos(2 * g) + 1 ? 0x3F80u : 0) | (n == 2 * pos(2 * g + 1) + 1 ? 0x3F800000u : 0);
    pb[10] = 5120 + (w >> 1) * 1024 + g * 256 + ((2 * (w & 1) + (n >> 3)) ^ ((0u - g) & 3)) * 32 + ((n >> 2) & 1) * 16 + (n & 3) * 4;
    const unsigned lp = (l & 60) | ((l & 1) << 1) | ((l >> 1) & 1);
    pb[11] = 5120 + ((lp & 15) >> 1) * 128 + (g ^ ((0u - (n >> 2)) & 3)) * 32 + (lp & 1) * 16;
    pb[12] = lp * 16 + w * 1024;
    pb[13] = 4096 + lp * 16 + w * 4;
    pb[14] = pb[15] = 0;
    const unsigned wb = h * 4 + w;
    PUT64(sa, 0, win + xc * 65536 + 4096);
    PUT64(sa, 2, win + W_F8 + xc * 16384);
    PUT64(sa, 4, win + kv_off(h, w));
    PUT64(sa, 6, win + W_P0 + W_PSTR + part_off(wb));
    sa[8] = sa[6] * 2 - W_PSTR; sa[9] = w;
    PUT64(sa, 10, win + km_off(h, w) - 16384);
    PUT64(sa, 12, win + W_F8 + 4096 + h * 128 + w * 4);
    sa[14] = rfl((unsigned)(prog - (win + W_F8 + 4096))); sa[15] = 0;
    PUT64(sb, 0, kvf + h * 128 + w * 4);
    PUT64(sb, 2, Wq + h * 256);
    PUT64(sb, 4, Wk + h * 256);
    PUT64(sb, 6, Wv + h * 256);
    PUT64(sb, 8, Wo + h * 147456);
    sb[10] = sb[11] = 0;
    sb[12] = rfl(LDS_REG0 + w * LDS_RSTR);
    sb[13] = rfl(k.K1 + k.K2);
    PUT64(sb, 14, dbg + wb * 4096);
    asm volatile(HEAD_ASM : : "{v[40:55]}"(pa), "{v[64:79]}"(pb), "{s[40:55]}"(sa), "{s[56:71]}"(sb) : "memory", "v255", "a255", "s101", "vcc");
}

__device__ __forceinline__ void red_role(unsigned cu, unsigned xc, char* win, char* fl, const char* mem0, const Tm& k, char* dbg, unsigned* lds) {
    const unsigned l = threadIdx.x & 63, wv = rfl(threadIdx.x >> 6);
    const unsigned u = cu + 18 * wv, T = u >> 1, rh = u & 1, ks = T >> 2, w = T & 3;
    u32x16 pa, sa;
#pragma unroll
    for (unsigned i = 0; i < 4; i++) pa[i] = part_off((8 * i + (l >> 3)) * 4 + w) + ks * 256 + rh * 128 + (l & 7) * 16;
    const unsigned c = l & 7, gD = (l >> 4) & 1, row = 4 * rh + 2 * (c >> 2) + gD;
    pa[4] = (4 * (row >> 1) + (c & 2) + (row & 1) + 16 * w) * 16 + 8 * (c & 1) + (((l >> 3) & 1) + 2 * (l >> 5)) * (ks < 8 ? 65536 : 16384);
    pa[5] = (l & 31) * 128;
    pa[6] = wv * 40960; pa[7] = pa[6] + 4096 + (l & 31) * 16;
    const unsigned m = l & 15;
#pragma unroll
    for (unsigned i = 0; i < 4; i++) pa[8 + i] = (m & 3) == i ? ((m & 4) ? 0x3F800000u : 0x3F80u) : 0;
    pa[12] = pa[13] = pa[14] = pa[15] = 0;
    const unsigned h0 = ld1(fl + hb_off());
    const unsigned long long m0 = *(const unsigned long long*)(mem0 + row * 1152 + ks * 128 + w * 32 + (c & 3) * 8);
    const v4u ones = {~0u, ~0u, ~0u, ~0u};
    unsigned best = ~0u;
    if (l < 8)
        for (int r = 0; r < 6; r++) asm volatile("global_store_dwordx4 %0, %1, %2 sc1" :: "v"(W_P0 + (wv & 1) * 8192 + (r >> 2) * W_PSTR + pa[r & 3]), "v"(ones), "s"(win) : "memory");
    for (int r = 0; r < 6; r++) {
        unsigned long long t0, t1; unsigned v_;
        asm volatile("s_waitcnt vmcnt(0)\n s_memtime %0\n global_load_dword %2, %3, %4 sc1\n s_waitcnt vmcnt(0)\n s_memtime %1\n s_waitcnt lgkmcnt(0)"
                     : "=&s"(t0), "=&s"(t1), "=&v"(v_) : "v"(W_P0 + (wv & 1) * 8192 + (r >> 2) * W_PSTR + rfl(pa[r & 3])), "s"(win) : "memory");
        best = min(best, (unsigned)(t1 - t0));
    }
    lds[wv] = (best | 1) * 2 + (wv & 1);
    hb_wait(fl, h0);
    __syncthreads();
    const unsigned vote = min(min(lds[0], lds[1]), min(lds[2], lds[3]));
    if (threadIdx.x == 0) st1(fl + F_RDY + 128 + cu * 4, vote);
#pragma unroll
    for (unsigned p = 0; p < 2; p++) {
#pragma unroll
        for (unsigned i = 0; i < 8; i++) asm volatile("global_store_dwordx4 %0, %1, %2 sc1" :: "v"(W_P0 + (i >> 2) * W_PSTR + p * 8192 + pa[i & 3]), "v"(ones), "s"(win) : "memory");
#pragma unroll
        for (unsigned i = 0; i < 4; i++)
            asm volatile("global_store_dwordx2 %0, %1, %2 sc1" :: "v"((ks < 8 ? ks * 1024 + i * 16384 : W_F8 + i * 1024) + p * 8192 + pa[4]), "v"(i ? ~0ull : m0), "s"(win) : "memory");
    }
    if (cu < 16) asm volatile("global_store_dwordx4 %0, %1, %2 sc1" :: "v"(W_F8 + 4096 + cu * 8192 + threadIdx.x * 16), "v"(ones), "s"(win) : "memory");
    asm volatile("s_waitcnt vmcnt(0)" ::: "memory");
    __syncthreads();
    if (threadIdx.x == 0) st1(fl + F_RDY + cu * 4, vote);
    unsigned vt = 0;
    for (unsigned b = SPIN; b && __builtin_amdgcn_ballot_w64(vt == 0); b--) vt = ld1(fl + F_RDY + min(l, 17u) * 4);
#pragma unroll
    for (int o = 1; o < 32; o *= 2) vt = min(vt, (unsigned)__shfl_xor((int)vt, o));
    win += (vt & 1) * 8192;
    PUT64(sa, 0, win + W_P0);
    sa[2] = sa[0] * 2 + W_PSTR; sa[3] = rfl(u == 71 ? 36 : 4);
    PUT64(sa, 4, ks < 8 ? win + ks * 1024 : win + W_F8);
    sa[6] = rfl(ks < 8 ? 14 : 10);
    sa[7] = rfl(k.K1 + ks * k.Kc);
    PUT64(sa, 8, win + W_F8 + 4096 + xc * 16384);
    PUT64(sa, 10, dbg + (128 + u) * 4096);
    sa[12] = rfl(k.K2 + k.X4);
    sa[13] = rfl(k.X3 - k.X4);
    sa[14] = rfl(k.XL - k.X3 - ks * k.Kc);
    sa[15] = rfl(k.R);
    asm volatile(RED_ASM : : "{v[32:47]}"(pa), "{v48}"(l * 16), "{v49}"(pa[6] + l * 16), "{s[40:55]}"(sa) : "memory", "v63", "s101", "vcc");
}
typedef unsigned v2u __attribute__((ext_vector_type(2)));
__device__ __forceinline__ unsigned tau(unsigned r) { return (r & 19) | ((r & 4) << 1) | ((r & 8) >> 1); }
__device__ __forceinline__ float blo(unsigned u) { return __uint_as_float(u << 16); }
__device__ __forceinline__ float bhi(unsigned u) { return __uint_as_float(u & 0xffff0000u); }
__device__ __forceinline__ unsigned pk2(float a, float b) { unsigned r; asm("v_cvt_pk_bf16_f32 %0, %1, %2" : "=v"(r) : "v"(a), "v"(b)); return r; }
__device__ __forceinline__ float rb(float a) { return blo(pk2(a, a)); }

template <int KIND>
__device__ __forceinline__ void a_role(unsigned c, char* ws, const char* W0, const char* W1, unsigned dly) {
    const unsigned l = threadIdx.x & 63, w = rfl(threadIdx.x >> 6);
    const unsigned g = l >> 4, n = l & 15, r = (l >> 2) & 3, cc = l & 3, hh = l >> 5;
    const unsigned RS = KIND == 2 ? 9216 : 8192;
    u32x16 pa, sa, sb;
#pragma unroll
    for (unsigned i = 0; i < 4; i++) pa[i] = (16 * w + 4 * i + g) * RS + (n ^ (g << 2)) * 16 - i * 1024;
    pa[4] = (8 * (g >> 1) + r) * 256 + ((4 * w + 2 * (g & 1) + (cc >> 1)) ^ (r << 2)) * 16 + 8 * (cc & 1);
    pa[5] = l * 16;
    pa[6] = l * 16 + w * 9216; pa[7] = pa[6] + 4096; pa[8] = pa[6] + 8192;
    const unsigned tok = tau(l & 31);
    if (KIND == 0) {
        const unsigned m = 4 * ((tok >> 4) | ((tok >> 1) & 2)) + ((tok >> 2) & 2) + (tok & 1);
        pa[9] = ((tok >> 1) & 1) * 1024 + (hh * 16 + m) * 16;
        pa[10] = 2048 + (g & 1) * 1024 + (hh * 16 + n) * 16;
    } else {
        pa[9] = (tok >> 4) * 1024 + (hh * 16 + (tok & 15)) * 16;
        pa[10] = pa[9] + (KIND == 1 ? MB : 8192);
    }
    pa[11] = l * 4;
    pa[12] = pa[13] = pa[14] = pa[15] = 0;
    PUT64(sa, 0, W0);
    PUT64(sa, 2, W1);
    sa[4] = 64 * RS;
    sa[5] = 36864;
    PUT64(sa, 6, ws + O_XN);
    PUT64(sa, 8, KIND == 0 ? al2m(ws) + kv_off(c, w) : KIND == 1 ? al2m(ws) + (km_off(2 * c, w) ^ 8192) : ws + O_H + c * 16384 + w * 2048);
    sa[10] = KIND == 0 ? 32768 : KIND == 1 ? 16384 : 294912;
    sa[11] = 32;
    PUT64(sa, 12, ws + O_FL + (KIND == 0 ? F_KV : KIND == 1 ? F_Q : F_H) + c * 128 + w * 4);
    PUT64(sa, 14, ws + O_FL + F_LN);
    sb = 0;
    sb[0] = rfl(w * 4096); sb[1] = rfl(w * 9216); sb[2] = 1; sb[3] = 1; sb[8] = dly; sb[9] = hb_off();
    PUT64(sb, 10, al2m(ws) + km_off(c, w));
    if (KIND == 0) asm volatile(KV_ASM : : "{v[96:111]}"(pa), "{s[40:55]}"(sa), "{s[56:71]}"(sb) : "memory", "v255", "a255", "s97", "vcc");
    if (KIND == 1) asm volatile(Q_ASM : "+{v[96:111]}"(pa), "+{s[40:55]}"(sa), "+{s[56:71]}"(sb) : : "memory", Q_CLOB, "vcc", "scc");
    if (KIND == 2) asm volatile(H_ASM : "+{v[96:111]}"(pa), "+{s[40:55]}"(sa), "+{s[56:71]}"(sb) : : "memory", H_CLOB, "vcc", "scc");
}

__device__ __forceinline__ void f2_role(unsigned c, char* ws, const char* W2, unsigned dly) {
    const unsigned l = threadIdx.x & 63, w = rfl(threadIdx.x >> 6);
    const unsigned g = l >> 4, r = (l >> 2) & 3, cc = l & 3, kc = c / 3, ng = c % 3;
    u32x16 pa, sa, sb;
#pragma unroll
    for (unsigned i = 0; i < 3; i++) { unsigned u = w * 192 + i * 64 + l; pa[i] = (u / 24) * 1152 + (u % 24) * 16 + 4096 - i * 1024; }
    pa[3] = 0;
    pa[4] = (4 * g + r) * 384 + 96 * w + 8 * cc;
    pa[5] = l * 16;
    pa[6] = l * 16 + w * 12288; pa[7] = pa[6] + 4096; pa[8] = pa[6] + 8192;
    pa[9] = (3 * kc + (r % 3)) * 128 + cc * 4;
    pa[10] = 0;
    pa[11] = l * 4;
    pa[12] = pa[13] = pa[14] = pa[15] = 0;
    sa = 0; sb = 0;
    PUT64(sa, 0, W2 + kc * 768 * 1152 + ng * 384 - 4096);
    PUT64(sa, 6, ws + O_H + kc * 49152);
    PUT64(sa, 8, ws + O_PF + kc * 36864 + (12 * ng + 3 * w) * 1024);
    PUT64(sa, 12, ws + O_FL + F_F2 + c * 128 + w * 4);
    PUT64(sa, 14, ws + O_FL + F_H);
    sb[0] = rfl(w * 3072); sb[1] = rfl(w * 12288); sb[8] = dly; sb[9] = hb_off() - F_H;
    asm volatile(F2_ASM : : "{v[96:111]}"(pa), "{s[40:55]}"(sa), "{s[56:71]}"(sb) : "memory", "v255", "a255", "s97", "vcc");
}

#define SPL 1
template <int LAST>
__device__ __forceinline__ void att_role(unsigned c, unsigned w, char* ws, const char* Wo, unsigned dly, unsigned per, unsigned lag, unsigned lagl) {
    unsigned l;
    asm volatile("v_mbcnt_lo_u32_b32 %0, -1, 0\n v_mbcnt_hi_u32_b32 %0, -1, %0" : "=v"(l));
    unsigned z = 0;
    if (LAST) asm volatile("" : "+s"(z), "+s"(w), "+s"(c), "+s"(ws));
    const unsigned g = l >> 4, n = l & 15, r = (l >> 2) & 3, cc = l & 3, par = LAST ? 63 : c >> 4, pr = c & 15, h = 2 * pr + (w >> 1), hf = w & 1;
    u32x16 pa, pb, sa, sb;
#pragma unroll
    for (unsigned i = 0; i < 8; i++) {
        const unsigned key = 16 * (g & 1) + 8 * ((i >> 1) & 1) + 4 * (g >> 1) + 2 * (i >> 2) + (i & 1);
        pa[i] = key <= 16 * hf + n ? 0u : 0xf0000000u;
    }
    pa[8] = (4 * g + r) * 1152 + 288 * w + 8 * cc;
    pa[9] = l * 16;
    pa[10] = l * 16 + w * 9216; pa[11] = pa[10] + 4096; pa[12] = pa[10] + 8192;
    pa[13] = F_PROG + h * 128 + 12;
    pa[14] = ((l & 4) ? F_KV + h * 128 : F_Q + pr * 128) + cc * 4;
    pa[15] = l * 4;
    pb = 0;
    pb[0] = 32768 + (w >> 1) * 16384 + l * 16;
    pb[1] = 32768 + (w >> 1) * 16384 + 2048 + (g & 1) * 512 + n * 16 + 8 * (g >> 1);
    const unsigned kap = 2 * r + (cc & 1), pos = 2 * ((kap & 1) * 2 + (kap >> 2)) + ((kap >> 1) & 1);
    pb[2] = (g * 16 + 2 * pos + 1) * 16;
    pb[3] = (w >> 1) * 8192 + hf * 1024 + l * 16;
    sa = z; sb = z;
    PUT64(sa, 0, Wo + pr * 294912);
    PUT64(sa, 2, al2m(ws) + (km_off(h, 2) ^ 8192) + par * 16384 + hf * 1024);
    PUT64(sa, 4, al2m(ws) + kv_off(h, 2 * hf) + par * 32768);
    PUT64(sa, 6, al2m(ws) + km_off(h, 2) + par * 16384);
    PUT64(sa, 8, ws + O_PA + (par * 16 + pr) * 36864 + 9 * w * 1024 + 4096);
    PUT64(sa, 12, ws + O_FL + F_ATT + (LAST ? 16 + pr : c) * 128 + w * 4);
    PUT64(sa, 14, ws + O_FL);
    sb[0] = rfl(32768 + w * 8192); sb[1] = rfl(w * 9216); sb[2] = rfl(par); sb[10] = per; sb[11] = lag; sb[12] = lagl; sb[8] = dly; sb[9] = hb_off();
    if (LAST == 2) asm volatile(ATB_ASM : : "{v[192:207]}"(pa), "{v[208:223]}"(pb), "{s[40:55]}"(sa), "{s[56:71]}"(sb) : "memory", "v255", "a255", "s97", "vcc");
    else if (LAST) asm volatile(ATA_ASM : : "{v[192:207]}"(pa), "{v[208:223]}"(pb), "{s[40:55]}"(sa), "{s[56:71]}"(sb) : "memory", "v255", "a255", "s97", "vcc");
    else asm volatile(ATT_ASM : : "{v[192:207]}"(pa), "{v[208:223]}"(pb), "{s[40:55]}"(sa), "{s[56:71]}"(sb) : "memory", "v255", "a255", "s97", "vcc");
}

__device__ __forceinline__ void ln_role(unsigned t, char* ws, const char* x) {
    const unsigned l = threadIdx.x & 63, w = threadIdx.x >> 6, rho = 8 * w + (l & 7), u = l >> 3;
    const __amdgpu_buffer_rsrc_t rs = __builtin_amdgcn_make_buffer_rsrc((void*)ws, 0, 160 * MB, 0x00020000);
    const v4u* src = (const v4u*)(x + (t * 32 + tau(rho)) * 1152) + u;
    const unsigned h0 = ld1(ws + O_FL + hb_off());
    v4u d[9];
#pragma unroll
    for (int j = 0; j < 9; j++) d[j] = src[8 * j];
    float s = 0;
#pragma unroll
    for (int j = 0; j < 9; j++)
#pragma unroll
        for (int k = 0; k < 4; k++) s += blo(d[j][k]) + bhi(d[j][k]);
    s += __shfl_xor(s, 8); s += __shfl_xor(s, 16); s += __shfl_xor(s, 32);
    const float m = s * (1.0f / 576);
    float q = 0;
#pragma unroll
    for (int j = 0; j < 9; j++)
#pragma unroll
        for (int k = 0; k < 4; k++) { float a = blo(d[j][k]) - m, b = bhi(d[j][k]) - m; q += a * a + b * b; }
    q += __shfl_xor(q, 8); q += __shfl_xor(q, 16); q += __shfl_xor(q, 32);
    const float mb = rb(m), inv = 1.0f / rb(sqrtf(rb(rb(q * (1.0f / 576)) + 1e-5f)));
#pragma unroll
    for (int j = 0; j < 9; j++) {
        v4u o;
#pragma unroll
        for (int k = 0; k < 4; k++) { const unsigned z = pk2(blo(d[j][k]) - mb, bhi(d[j][k]) - mb); o[k] = pk2(blo(z) * inv, bhi(z) * inv); }
        __builtin_amdgcn_raw_buffer_store_b128(o, rs, O_XN + t * 36864 + (u + 8 * j) * 512 + rho * 16, 0, 16);
    }
    if (w == 0) hb_wait(ws + O_FL, h0);
    asm volatile("s_waitcnt vmcnt(0)" ::: "memory");
    __syncthreads();
    if (threadIdx.x == 0) __builtin_amdgcn_raw_buffer_store_b32(1u, rs, O_FL + F_LN + t * 4, 0, 16);
}

#define FSL 32
#define TSL 20
#define FST 0
#define TW0 20
#define TFD 140
#define OST 16
#define PRW 1
#define DSL 2
#define OSC 0
#define PNT ""
template <int SL>
__device__ __forceinline__ void fin_wait(__amdgpu_buffer_rsrc_t rs, unsigned fo, unsigned& f, unsigned need, unsigned& bud) {
    for (bool again = false; bud && __builtin_amdgcn_ballot_w64(f < need) != 0; again = true) {
        bud--;
        if (again) __builtin_amdgcn_s_sleep(SL);
        asm volatile("" ::: "memory");
        f = __builtin_amdgcn_raw_buffer_load_b32(rs, fo, 0, 16);
    }
    __builtin_amdgcn_fence(__ATOMIC_ACQUIRE, "wavefront");
}
__device__ __forceinline__ float silu(float v) { return v * __builtin_amdgcn_rcpf(1.0f + __expf(-v)); }
struct Gate { v2u x0, x1; v4u e[6]; };
__device__ __forceinline__ void gate_load(Gate& g, __amdgpu_buffer_rsrc_t rs, const char* x, unsigned s, unsigned t, unsigned l) {
    const unsigned xo = (s * 32 + (l & 15)) * 1152 + t * 32 + (l >> 4) * 8;
    g.x0 = *(const v2u*)(x + xo); g.x1 = *(const v2u*)(x + xo + 16 * 1152);
#pragma unroll
    for (int p = 0; p < 6; p++) g.e[p] = __builtin_amdgcn_raw_buffer_load_b128(rs, l * 16, O_PF + (s * 6 + p) * 36864 + t * 1024, 16);
}
__device__ __forceinline__ void gate_eval(const Gate& g, float* gt) {
#pragma unroll
    for (int k = 0; k < 4; k++) {
        float lo = 0, hi = 0;
#pragma unroll
        for (int p = 0; p < 6; p++) { lo += blo(g.e[p][k]); hi += bhi(g.e[p][k]); }
        gt[2 * k] = silu(lo); gt[2 * k + 1] = silu(hi);
    }
}
__device__ __forceinline__ void fin_store(const Gate& g, const float* gt, const float* a, __amdgpu_buffer_rsrc_t ro, unsigned s, unsigned t, unsigned l) {
    const unsigned xo = (s * 32 + (l & 15)) * 1152 + t * 32 + (l >> 4) * 8;
    v4u o;
#pragma unroll
    for (int k = 0; k < 4; k++) { const unsigned xv = k < 2 ? g.x0[k] : g.x1[k - 2]; o[k] = pk2(blo(xv) + a[2 * k] * gt[2 * k], bhi(xv) + a[2 * k + 1] * gt[2 * k + 1]); }
    __builtin_amdgcn_raw_buffer_store_b64(v2u{o[0], o[1]}, ro, xo, 0, OSC);
    __builtin_amdgcn_raw_buffer_store_b64(v2u{o[2], o[3]}, ro, xo + 16 * 1152, 0, OSC);
}
__device__ __forceinline__ void sum4(const v4u* d, float* a) {
#pragma unroll
    for (int k = 0; k < 4; k++) {
        a[2 * k] = blo(d[0][k]) + blo(d[1][k]) + blo(d[2][k]) + blo(d[3][k]);
        a[2 * k + 1] = bhi(d[0][k]) + bhi(d[1][k]) + bhi(d[2][k]) + bhi(d[3][k]);
    }
}

__device__ __forceinline__ unsigned fin_flag(unsigned t, unsigned p, unsigned l) { return O_FL + (l < 32 ? F_F2 + ((l % 6) * 3 + t / 12) * 128 + ((t % 12) / 3) * 4 : F_ATT + (p * 16 + (l & 15)) * 128 + (t / 9) * 4); }
__device__ __forceinline__ void fin_role(unsigned c, char* ws, const char* x, char* out, unsigned per, unsigned lag) {
    const unsigned l = threadIdx.x & 63, w = rfl(threadIdx.x >> 6), i = (c % 9) * 4 + w;
    const __amdgpu_buffer_rsrc_t rs = __builtin_amdgcn_make_buffer_rsrc((void*)ws, 0, 160 * MB, 0x00020000);
    const __amdgpu_buffer_rsrc_t ro = __builtin_amdgcn_make_buffer_rsrc((void*)out, 0, 2048 * 1152, 0x00020000);
    unsigned bud = SPIN, f = 0;
    hb_wait(ws + O_FL, ld1(ws + O_FL + hb_off()));
    if (l == 0) st1(ws + O_FL + F_FIN + c * 64 + w * 4, 1);
    Gate g;
    float gt[8], a[8], b[8];
    const unsigned fo = fin_flag(i, (c / 9) & 1, l);
    for (unsigned s = c / 9; s < 62; s += 4) {
        fin_wait<FSL>(rs, fo, f, l < 32 ? s + 1 : 0, bud);
        gate_load(g, rs, x, s, i, l);
        const unsigned pg = rfl(__builtin_amdgcn_raw_buffer_load_b32(rs, O_FL + F_PROG + (c & 31) * 128 + 12, 0, 16)), due = pg + (s + 2 - (pg & 65535)) * per + lag;
        gate_eval(g, gt);
        while (due - ((unsigned)__builtin_amdgcn_s_memrealtime() << 16) < 0x4000000u) __builtin_amdgcn_s_sleep(2);
        if (s < 58) fin_wait<FSL>(rs, fo, f, s + 1, bud);
        else fin_wait<TSL / 2>(rs, fo, f, s + 1, bud);
        v4u d[16];
#pragma unroll
        for (int p = 0; p < 16; p++) d[p] = __builtin_amdgcn_raw_buffer_load_b128(rs, l * 16, O_PA + (s * 16 + p) * 36864 + i * 1024, 16);
        sum4(d, a);
#pragma unroll
        for (int p = 4; p < 16; p += 4) {
            sum4(d + p, b);
#pragma unroll
            for (int k = 0; k < 8; k++) a[k] += b[k];
        }
        fin_store(g, gt, a, ro, s, i, l);
    }
}
__device__ __forceinline__ void tail_role(unsigned c, char* ws, const char* x, char* out, float* lds, unsigned per, unsigned lag, unsigned lagl) {
    const unsigned l = threadIdx.x & 63, w = rfl(threadIdx.x >> 6);
    const __amdgpu_buffer_rsrc_t rs = __builtin_amdgcn_make_buffer_rsrc((void*)ws, 0, 160 * MB, 0x00020000);
    const __amdgpu_buffer_rsrc_t ro = __builtin_amdgcn_make_buffer_rsrc((void*)out, 0, 2048 * 1152, 0x00020000);
    const unsigned fo = fin_flag(c, (l >> 4) & 1, l), po = O_FL + F_PROG + (c & 31) * 128 + 12;
    const unsigned r = 8 * w + (l >> 3), e = ((r & 15) + 16 * ((l & 7) >> 1)) * 4 + 2 * (r >> 4) + (l & 1), xo = r * 1152 + c * 32 + (l & 7) * 4;
    const unsigned pa = O_FL + F_ATT + (c & 31) * 128 + 12288;
    unsigned bud = SPIN, f = 0, pg = 0, xv[2];
    float g0[2], g1[2];
    hb_wait(ws + O_FL, ld1(ws + O_FL + hb_off()));
    if (l == 0) st1(ws + O_FL + F_FIN + c * 64 + 16 + w * 4, 1);
#pragma unroll
    for (int k = 0; k < 2; k++) xv[k] = *(const unsigned*)(x + (62 + k) * 36864 + xo);
    if (PRW) {
        __builtin_amdgcn_raw_buffer_store_b32(xv[0], ro, xo, 62 * 36864, 16);
        __builtin_amdgcn_raw_buffer_store_b32(xv[1], ro, xo, 63 * 36864, 16);
        asm volatile("s_waitcnt vmcnt(0)" ::: "memory");
    }
    for (; bud && (pg & 65535) < 65; bud--) {
        const unsigned d = 65 - (pg & 65535), due = pg + d * per - (TW0 << 16);
        for (unsigned i = d * 3 / 8; d > 5 && i && (d > 20 || (int)(due - ((unsigned)__builtin_amdgcn_s_memrealtime() << 16)) > 0x2000000); i--) __builtin_amdgcn_s_sleep(127);
        do __builtin_amdgcn_s_sleep(4); while (due - ((unsigned)__builtin_amdgcn_s_memrealtime() << 16) < 0x4000000u);
        asm volatile("" ::: "memory");
        pg = rfl(__builtin_amdgcn_raw_buffer_load_b32(rs, d > 20 ? pa : po, 0, 16));
        f = __builtin_amdgcn_raw_buffer_load_b32(rs, d > 20 ? pa : d > 5 ? po : fo, 0, 16);
        if (d > 5) f = 0;
    }
    fin_wait<TSL>(rs, fo, f, l < 32 ? 64 : 1, bud);
#pragma unroll
    for (int k = 0; k < 2; k++) {
        float lo = 0, hi = 0;
#pragma unroll
        for (int p = 0; p < 6; p++) { const unsigned h = __builtin_amdgcn_raw_buffer_load_b32(rs, e * 4, O_PF + ((62 + k) * 6 + p) * 36864 + c * 1024, 16); lo += blo(h); hi += bhi(h); }
        g0[k] = silu(lo); g1[k] = silu(hi);
    }
    const unsigned d0 = lag - per + (((c % 9) * 12) << 16), d1 = lagl + (((SPL ? c % 9 % 5 : c % 9) * 12) << 16);
    const unsigned due = pg + (65 - (pg & 65535)) * per;
    v4u d[8];
#pragma unroll
    for (int k = 0; k < 2; k++) {
        while (due + (k ? d1 : d0) - ((unsigned)__builtin_amdgcn_s_memrealtime() << 16) < 0x4000000u) {}
        asm volatile("" ::: "memory");
#pragma unroll
        for (int p = 0; p < 4; p++) d[4 * k + p] = __builtin_amdgcn_raw_buffer_load_b128(rs, l * 16, O_PA + ((62 + k) * 16 + w * 4 + p) * 36864 + c * 1024, 16);
        asm volatile("" ::: "memory");
    }
#pragma unroll
    for (int k = 0; k < 2; k++) {
        for (;;) {
            unsigned m = 0;
#pragma unroll
            for (int p = 4 * k; p < 4 * k + 4; p++) m = max(max(m, max(d[p][0], d[p][1])), max(d[p][2], d[p][3]));
            if (__builtin_amdgcn_ballot_w64(m == ~0u) == 0 || bud == 0) break;
            bud--;
            __builtin_amdgcn_s_sleep(DSL);
            asm volatile("" ::: "memory");
#pragma unroll
            for (int p = 0; p < 4; p++) d[4 * k + p] = __builtin_amdgcn_raw_buffer_load_b128(rs, l * 16, O_PA + ((62 + k) * 16 + w * 4 + p) * 36864 + c * 1024, 16);
        }
        float a[8];
        sum4(d + 4 * k, a);
        float4* q = (float4*)lds + k * 512 + w * 128 + l * 2;
        q[0] = float4{a[0], a[1], a[2], a[3]}; q[1] = float4{a[4], a[5], a[6], a[7]};
    }
    asm volatile("s_waitcnt lgkmcnt(0)\n s_barrier" ::: "memory");
#pragma unroll
    for (int k = 0; k < 2; k++) {
        const float2* u = (const float2*)lds + k * 1024 + e;
        const float2 u0 = u[0], u1 = u[256], u2 = u[512], u3 = u[768];
        __builtin_amdgcn_raw_buffer_store_b32(pk2(blo(xv[k]) + (u0.x + u1.x + u2.x + u3.x) * g0[k], bhi(xv[k]) + (u0.y + u1.y + u2.y + u3.y) * g1[k]), ro, xo, (62 + k) * 36864, OST);
    }
}

__device__ __forceinline__ void pre_role(unsigned e, const char* Wq, const char* Wk, const char* Wv, const char* Wo) {
    const unsigned t0 = e & 31, ts = (e >> 5) & 7, nt = (e >> 8) & 15, k = (e >> 12) & 31, n = (e >> 17) & 31, c0 = (e >> 22) & 31, c1 = (e >> 27) * 9, t = threadIdx.x;
    for (unsigned j = k; j < (c1 - c0) * nt; j += n) {
        const unsigned c = c0 + j / nt, h = t0 + (j % nt) * ts, m = c / 9, ks = c % 9;
        const char* p = m < 3 ? (m == 0 ? Wq : m == 1 ? Wk : Wv) + h * 256 + ks * 524288 : Wo + h * 147456 + ks * 128;
        const unsigned o = m < 3 ? (t >> 4) * 8192 + (t & 15) * 16 : (t >> 3) * 1152 + (t & 7) * 16, st = m < 3 ? 16 * 8192 : 32 * 1152;
        v4u d;
        asm volatile("global_load_dwordx4 %0, %1, %5" PNT "\n global_load_dwordx4 %0, %2, %5" PNT "\n global_load_dwordx4 %0, %3, %5" PNT "\n global_load_dwordx4 %0, %4, %5" PNT "\n s_waitcnt vmcnt(40)"
                     : "=&{v[200:203]}"(d) : "v"(o), "v"(o + st), "v"(o + 2 * st), "v"(o + 3 * st), "s"(p) : "memory");
    }
    asm volatile("s_waitcnt vmcnt(0)" ::: "memory");
}

enum { R_NONE, R_HEAD, R_RED, R_KV, R_Q, R_H, R_F2, R_ATT, R_FIN, R_MON, R_LEAD, R_TAIL };
struct Map { unsigned e[256], p[256]; };

__device__ __forceinline__ void lead_role(char* fl) {
    const unsigned l = threadIdx.x;
    const v4u z = {0, 0, 0, 0};
    asm volatile("global_store_dwordx4 %0, %1, %2 sc1" :: "v"(l < 32 ? (l < 16 ? F_LN : F_RDY - 256) + l * 16 : l * 128), "v"(z), "s"(fl) : "memory");
    if (l < 18) asm volatile("global_store_dwordx4 %0, %1, %2 sc1" :: "v"(F_FIN + 64 + l * 128), "v"(z), "s"(fl) : "memory");
    if (l < 32) asm volatile("global_store_dwordx4 %0, %1, %2 sc1" :: "v"(F_ATT + 12288 + l * 128), "v"(z), "s"(fl) : "memory");
    if (l < 36) asm volatile("global_store_dwordx4 %0, %1, %2 sc1" :: "v"(F_FIN + 16 + l * 64), "v"(z), "s"(fl) : "memory");
    asm volatile("s_waitcnt vmcnt(0)" ::: "memory");
    __syncthreads();
    if (l >= 64) return;
    const unsigned o0 = l < 32 ? F_ATT + l * 128 : F_F2 + min(l - 32, 17u) * 128, o1 = F_FIN + min(l, 35u) * 64;
    for (unsigned n = 0; n < SPIN; n++) {
        if (l < 16) st1(fl + F_HB + l * 128, n);
        v4u a, b, d;
        asm volatile("global_load_dwordx4 %0, %3, %5 sc1\n global_load_dwordx4 %1, %4, %5 sc1\n global_load_dwordx4 %2, %4, %5 offset:16 sc1\n s_waitcnt vmcnt(0)" : "=&v"(a), "=&v"(b), "=&v"(d) : "v"(o0), "v"(o1), "s"(fl) : "memory");
        if (__builtin_amdgcn_ballot_w64(min(min(min(min(a[0], a[1]), min(a[2], a[3])), min(min(b[0], b[1]), min(b[2], b[3]))), min(min(d[0], d[1]), min(d[2], d[3]))) == 0) == 0) break;
    }
}

__global__ __launch_bounds__(256, 1) void k_main(char* ws, const char* x, const char* mem0, const char* Wq, const char* Wk, const char* Wv, const char* Wo,
                                                 const char* W1, const char* W2, char* out, Tm tm, Map map) {
    __shared__ __attribute__((aligned(16))) char lds[LDS_TOTAL];
    const unsigned e = map.e[blockIdx.x], role = e & 255, c = (e >> 8) & 255, ln = e >> 16, pe = map.p[blockIdx.x];
    asm volatile("" :: "v"(lds) : "memory");
    if (role == R_HEAD || role == R_RED) {
        char* win = al2m(ws);
        if (role == R_HEAD) head_role(c, blockIdx.x & tm.XM, win, ws + O_FL + F_PROG, ws + O_FL + F_KV, Wq, Wk, Wv, Wo, tm, ws + O_DBG);
        else red_role(c, blockIdx.x & tm.XM, win, ws + O_FL, mem0, tm, ws + O_DBG, (unsigned*)lds);
        return;
    }
    if (ln < 64) ln_role(ln, ws, x);
    if (pe) pre_role(pe, Wq, Wk, Wv, Wo);
    const unsigned wv = rfl(threadIdx.x >> 6);
    if (role == R_KV) a_role<0>(c, ws, Wk + c * 256, Wv + c * 256, 0);
    else if (role == R_Q) {
        a_role<1>(c, ws, Wq + c * 512, Wq + c * 512 + 256, tm.DQ);
        att_role<1>(c, wv, ws, Wo, 0, tm.AP + tm.K1 + tm.K2, tm.AC, tm.AL);
        __builtin_amdgcn_endpgm();
    } else if (role == R_H) {
        a_role<2>(c, ws, W1 + c * 512, W1 + c * 512 + 256, tm.DH);
        if (c < 16) att_role<2>(c, wv, ws, Wo, 0, tm.AP + tm.K1 + tm.K2, tm.AC, tm.AL);
        __builtin_amdgcn_endpgm();
    } else if (role == R_F2) f2_role(c, ws, W2, tm.DF);
    else if (role == R_ATT) att_role<0>(c, wv, ws, Wo, tm.DA, tm.AP + tm.K1 + tm.K2, tm.AC, tm.AL);
    else if (role == R_LEAD) lead_role(ws + O_FL);
    else if (role == R_FIN) fin_role(c, ws, x, out, tm.AP + tm.K1 + tm.K2, tm.AC + tm.FC + (TFD << 16));
    else if (role == R_TAIL) tail_role(c, ws, x, out, (float*)lds, tm.AP + tm.K1 + tm.K2, tm.AC + tm.FC, tm.AL + tm.FC);
}

#define K0 4
#define SKIP 256
static void make_map(Map& m, int variant, unsigned ch, unsigned ck) {
    int cnt[8] = {0, 0, 0, 0, 0, 0, 0, 0};
    for (int i = 0; i < 256; i++) m.e[i] = R_NONE | 255u << 16;
    auto put = [&](int xcd, unsigned role, unsigned c) { m.e[cnt[xcd]++ * 8 + xcd] = role | c << 8 | 255u << 16; };
    const int nr = variant == 4 ? 2 : variant == 5 ? 3 : 4, nw = 8 - nr;
    put(0, R_LEAD, 0);
    for (int h = 0; h < 32; h++) put(h * nr >> 5, R_HEAD, h);
    for (int c = 0; c < 18; c++) put(nr - 1 - c % nr, R_RED, c);
    for (int c = 0; c < 36; c++) put(c % nr, R_TAIL, c);
    int k = K0;
    auto least = [&]() { int b = nr; for (int x = nr; x < 8; x++) if (cnt[x] < cnt[b]) b = x; return cnt[b] < 32 ? b : nr - 1; };
    for (int c = 0; c < 32; c++) put(nr + k++ % nw, R_KV, c);
    for (int c = 0; c < 16; c++) put(nr + k++ % nw, R_Q, c);
    for (int c = 0; c < 18; c++) put(nr + k++ % nw, R_H, c);
    for (int c = 0; c < 18; c++) put(nr + k++ % nw, R_F2, c);
    for (int c = 0; c < 16; c++) { const int b = least(); put(b, R_ATT, c); put(b, R_ATT, c + 16); }
    for (int c = 0; c < 36; c++) put(least(), R_FIN, c);
    unsigned t = 0;
    const unsigned order[3] = {R_F2, R_ATT, R_Q};
    for (int pass = 0; pass < 3; pass++)
        for (int i = 0; i < 256 && t < 64; i++)
            if ((m.e[i] & 255) == order[pass]) m.e[i] = (m.e[i] & 0xffff) | t++ << 16;
    for (int x = 0; x < 8; x++) {
        unsigned t0 = 99, nt = 0, n = 0, j = 0;
        auto idle = [&](int i) { return ((m.e[i] & 255) == R_NONE && i != SKIP) || (m.e[i] & 255) == R_FIN || (m.e[i] & 255) == R_TAIL; };
        for (int i = x; i < 256; i += 8) {
            if ((m.e[i] & 255) == (x < nr ? R_HEAD : R_KV) && nt++ == 0) t0 = (m.e[i] >> 8) & 255;
            n += idle(i);
        }
        for (int i = x; i < 256; i += 8) m.p[i] = idle(i) ? t0 | (x < nr ? 1 : nw) << 5 | nt << 8 | j++ << 12 | n << 17 | (x < nr ? ch : ck) << 22 | (x < nr ? 4u : 3u) << 27 : 0;
    }
}

static void launch_all(char* ws, void* const* in, char* out, const Tm& tm, const Map& map, hipStream_t stream) {
    k_main<<<256, 256, 0, stream>>>(ws, (const char*)in[0], (const char*)in[1], (const char*)in[2], (const char*)in[3], (const char*)in[4], (const char*)in[5],
                                    (const char*)in[8], (const char*)in[9], out, tm, map);
}

extern "C" void kernel_launch(void* const* d_in, const int* in_sizes, int n_in, void* d_out, int out_size, void* d_ws,
                              size_t ws_size, hipStream_t stream) {
    Map map;
    make_map(map, 5, 3, 13);
    const Tm tm = {1048576u, 190054u, 2752512u, 1966080u, 4259840u, 7340032u, 3604480u, 3u, 0u, 0u, 0u, 0u, 5898240u, 3276800u, 4294311936u, 10158080u};
    launch_all((char*)d_ws, d_in, (char*)d_out, tm, map, stream);
}
```
